```python
import torch

def _softmax(x, axis=-1):
    return torch.softmax(x, dim=axis)
def _gelu(x):
    return torch.nn.functional.gelu(x, approximate="tanh")
def _silu(x):
    return torch.nn.functional.silu(x)
def _sigmoid(x):
    return torch.sigmoid(x)
def _layernorm(x, axis=-1, eps=1e-5):
    m = x.mean(dim=axis, keepdim=True)
    v = x.var(dim=axis, keepdim=True, unbiased=False)
    return (x - m) / torch.sqrt(v + eps)
def _scatter_add(tgt, idx, src, axis=0):
    return tgt.clone().index_add_(axis, idx, src)
def _segment_sum(src, seg, n):
    out = torch.zeros((n,) + src.shape[1:], dtype=src.dtype)
    return out.index_add_(0, seg, src)
def _pad(x, pw):
    flat = tuple(v for pair in reversed(pw) for v in pair)
    return torch.nn.functional.pad(x, flat)

kernel_name = 'gated_mix__sliding_local_dilated__compressed_kv_cla__prompt'

def setup_inputs():

    _w = (lambda a: (lambda t: t.to(torch.bfloat16) if t.is_floating_point() else t)(torch.as_tensor(a)))
    def _A_setup():
        B, S, D = 1, 2048, 512
        return {
            "x": _w((lambda *s: torch.randn(*s, dtype=torch.bfloat16) * 0.05)(B, S, D)),
            "wqkv": _w((lambda *s: torch.randn(*s, dtype=torch.bfloat16) * 1.6)(D, 3 * D)),
            "wo": _w((lambda *s: torch.randn(*s, dtype=torch.bfloat16) * 0.05)(D, D)),
        }
    _A_in = _A_setup()

    def _B_setup():
        B, H, Sq, Sk, D, M = 1, 32, 2048, 2048, 128, 512
        Dc = 4 * D
        return {
            "x": _w((lambda *s: torch.randn(*s, dtype=torch.bfloat16) * 0.05)(B, Sq, M)),
            "c_kv": _w((lambda *s: torch.randn(*s, dtype=torch.bfloat16) * 1.6)(B, Sk, Dc)),
            "w_q": _w((lambda *s: torch.randn(*s, dtype=torch.bfloat16) * 6.4)(M, H, D) * (M ** -0.5)),
            "w_uk": _w((lambda *s: torch.randn(*s, dtype=torch.bfloat16) * 6.4)(Dc, H, D) * (Dc ** -0.5)),
            "w_uv": _w((lambda *s: torch.randn(*s, dtype=torch.bfloat16) * 1.6)(Dc, H, D) * (Dc ** -0.5)),
            "w_o": _w((lambda *s: torch.randn(*s, dtype=torch.bfloat16) * 1.6)(H, D, M) * ((H * D) ** -0.5)),
        }
    _B_in = _B_setup()

    return {
        "A_x": _A_in["x"],
        "A_wqkv": _A_in["wqkv"],
        "A_wo": _A_in["wo"],
        "B_x": _B_in["x"],
        "B_c_kv": _B_in["c_kv"],
        "B_w_q": _B_in["w_q"],
        "B_w_uk": _B_in["w_uk"],
        "B_w_uv": _B_in["w_uv"],
        "B_w_o": _B_in["w_o"],
        "gate_w": _w((lambda *s: torch.randn(*s, dtype=torch.bfloat16) * 0.05)(512) * 16.0),
    }

def reference(A_x, A_wqkv, A_wo, B_x, B_c_kv, B_w_q, B_w_uk, B_w_uv, B_w_o, gate_w):
    def _A_ref(x, wqkv, wo):
        B, S, D = x.shape
        H = 32; Dh = D // H
        W, dil = 8, 2
        qkv = (lambda x, *d: x.permute(*d))((x @ wqkv).reshape(B, S, 3, H, Dh), 2, 0, 3, 1, 4)
        q, k, v = qkv[0], qkv[1], qkv[2]
        scale = Dh ** -0.5
        pos = torch.arange(S)
        off = torch.arange(W) * dil
        idx = pos[:, None] - off[None, :]
        mask = idx >= 0
        idx_c = torch.where(mask, idx, 0).reshape(-1)
        kg = k[:, :, idx_c, :].reshape(B, H, S, W, Dh)
        vg = v[:, :, idx_c, :].reshape(B, H, S, W, Dh)
        logits = torch.einsum("bhsd,bhswd->bhsw", q, kg) * scale
        logits = torch.where(mask[None, None, :, :], logits, -1e9)
        attn = _softmax(logits, axis=-1)
        o = torch.einsum("bhsw,bhswd->bhsd", attn, vg)
        return (lambda x, *d: x.permute(*d))(o, 0, 2, 1, 3).reshape(B, S, D) @ wo

    def _B_ref(x, c_kv, w_q, w_uk, w_uv, w_o):
        B, Sq, M = x.shape[0], x.shape[1], x.shape[2]
        Sk = c_kv.shape[1]
        H, D = w_q.shape[1], w_q.shape[2]
        q = torch.einsum("bqm,mhd->bhqd", x, w_q)
        k = torch.einsum("bkc,chd->bhkd", c_kv, w_uk)
        v = torch.einsum("bkc,chd->bhkd", c_kv, w_uv)
        scores = torch.einsum("bhqd,bhkd->bhqk", q, k) * (D ** -0.5)
        off = Sk - Sq
        qi = torch.arange(Sq)[:, None]
        kj = torch.arange(Sk)[None, :]
        cmask = kj <= (qi + off)
        scores = torch.where(cmask[None, None], scores, -1e9)
        p = _softmax(scores, axis=-1)
        ctx = torch.einsum("bhqk,bhkd->bhqd", p, v)
        return torch.einsum("bhqd,hdm->bqm", ctx, w_o)

    x = A_x
    g = _sigmoid((x * gate_w).sum(-1, keepdims=True))
    a = _A_ref(x, A_wqkv, A_wo)
    b = _B_ref(x, B_c_kv, B_w_q, B_w_uk, B_w_uv, B_w_o)
    return g * a + (1 - g) * b
```

```cpp
#include <hip/hip_runtime.h>
#include <hip/hip_bf16.h>
extern "C" __global__ __launch_bounds__(256, 1) void gated_mix__sliding_local_dilated__compressed_kv_cla__prompt_kernel(const void* x, const void* wqkv, const void* woa, const void* ckv, const void* wq, const void* wuk, const void* wuv, const void* wob, const void* gw, void* out, void* ws) {
  __shared__ __attribute__((aligned(16))) char lds[163840];
  unsigned wid = __builtin_amdgcn_readfirstlane(threadIdx.x >> 6);
  unsigned bid = blockIdx.x;
  unsigned ldsb = (unsigned)(size_t)lds;
  auto ka = __builtin_amdgcn_kernarg_segment_ptr();
  asm volatile(
R"~(.macro M a:vararg
v_mfma_f32_16x16x32_bf16 \a
.endm
.macro BL a:vararg
buffer_load_dwordx4 \a
.endm
.macro BS a:vararg
buffer_store_dwordx4 \a
.endm
.macro TR a:vararg
ds_read_b64_tr_b16 \a
.endm
.macro RD a:vararg
ds_read_b128 \a
.endm
.macro WR a:vararg
ds_write_b128 \a
.endm
.macro CV a:vararg
v_cvt_pk_bf16_f32 \a
.endm
.macro EX a:vararg
v_exp_f32 \a
.endm
.macro AR a:vararg
v_accvgpr_read_b32 \a
.endm
.macro AW a:vararg
v_accvgpr_write_b32 \a
.endm
.macro SA a:vararg
s_add_u32 \a
.endm
.macro W a:vararg
s_waitcnt \a
.endm
s_mov_b32 s36,%[wid]
s_mov_b32 s37,%[bid]
s_load_dwordx16 s[8:23],%[ka],0
s_load_dwordx4 s[24:27],%[ka],64
s_load_dwordx2 s[28:29],%[ka],0x50
v_mbcnt_lo_u32_b32 v255,-1,0
v_mbcnt_hi_u32_b32 v255,-1,v255
s_lshr_b32 s88,s37,3
s_getpc_b64 s[94:95]
fk_pc0:
s_lshl_b32 s93,s88,13
s_cmp_lt_u32 s93,(fk_exit-fk_pc0)
s_cselect_b32 s92,0x80,0
s_cmp_eq_u32 s36,0
s_cselect_b32 s92,s92,0
s_min_u32 s93,s93,(fk_exit-fk_pc0-8192)
v_mul_u32_u24 v242,s92,v255
v_add_u32 v242,s93,v242
global_load_dword v248,v242,s[94:95]
W lgkmcnt(0)
s_and_b32 s85,s37,7
s_lshl_b32 s85,s85,2
s_lshr_b32 s86,s37,3
s_and_b32 s86,s86,3
SA s38,s85,s86
s_lshr_b32 s39,s37,5
s_lshr_b32 s40,s36,1
s_and_b32 s41,s36,1
s_mov_b64 s[48:49],s[14:15]
s_and_b32 s49,s49,0xffff
s_mov_b32 s50,0x200000
s_mov_b32 s51,0x20000
s_mov_b64 s[84:85],s[18:19]
s_mov_b64 s[86:87],s[20:21]
s_cmp_eq_u32 s40,0
s_cselect_b32 s84,s84,s86
s_cselect_b32 s85,s85,s87
s_mov_b64 s[52:53],s[84:85]
s_and_b32 s53,s53,0xffff
s_mov_b32 s54,0x400000
s_mov_b32 s55,0x20000
v_mbcnt_lo_u32_b32 v255,-1,0
v_mbcnt_hi_u32_b32 v255,-1,v255
v_lshlrev_b32 v254,4,v255
s_lshl_b32 s60,s39,18
s_lshl_b32 s84,s36,16
SA s60,s60,s84
s_lshl_b32 s62,s36,13
s_lshl_b32 s61,s41,18
s_lshl_b32 s84,s38,8
SA s61,s61,s84
s_lshl_b32 s63,s40,14
s_lshl_b32 s84,s41,13
SA s63,s63,s84
SA s63,s63,0x8000
v_lshrrev_b32 v238,3,v255
v_and_b32 v239,7,v255
v_lshrrev_b32 v240,1,v238
v_xor_b32 v239,v239,v240
v_mul_u32_u24 v238,0x400,v238
v_bfe_u32 v242,v255,2,3
v_mul_u32_u24 v242,0x2000,v242
v_lshrrev_b32 v243,5,v255
v_lshl_add_u32 v242,v243,6,v242
v_and_b32 v243,3,v255
v_xor_b32 v241,0,v239
v_lshl_add_u32 v241,v241,4,v238
v_add_u32 v192,0,v241
SA m0,s62,0
v_xor_b32 v244,0,v243
v_lshl_add_u32 v244,v244,4,v242
v_add_u32 v200,0,v244
BL v192,s[48:51],s60 offen lds
SA m0,s63,0
s_nop 0
BL v200,s[52:55],s61 offen lds
v_xor_b32 v241,4,v239
v_lshl_add_u32 v241,v241,4,v238
v_add_u32 v193,0x2000,v241
SA m0,s62,0x400
v_xor_b32 v244,0,v243
v_lshl_add_u32 v244,v244,4,v242
v_add_u32 v201,0x80,v244
BL v193,s[48:51],s60 offen lds
SA m0,s63,0x400
s_nop 0
BL v201,s[52:55],s61 offen lds
v_xor_b32 v241,0,v239
v_lshl_add_u32 v241,v241,4,v238
v_add_u32 v194,0x4000,v241
SA m0,s62,0x800
v_xor_b32 v244,2,v243
v_lshl_add_u32 v244,v244,4,v242
v_add_u32 v202,0x10000,v244
BL v194,s[48:51],s60 offen lds
SA m0,s63,0x800
s_nop 0
BL v202,s[52:55],s61 offen lds
v_xor_b32 v241,4,v239
v_lshl_add_u32 v241,v241,4,v238
v_add_u32 v195,0x6000,v241
SA m0,s62,0xc00
v_xor_b32 v244,2,v243
v_lshl_add_u32 v244,v244,4,v242
v_add_u32 v203,0x10080,v244
BL v195,s[48:51],s60 offen lds
SA m0,s63,0xc00
s_nop 0
BL v203,s[52:55],s61 offen lds
v_xor_b32 v241,0,v239
v_lshl_add_u32 v241,v241,4,v238
v_add_u32 v196,0x8000,v241
SA m0,s62,0x1000
v_xor_b32 v244,0,v243
v_lshl_add_u32 v244,v244,4,v242
v_add_u32 v204,0x20000,v244
BL v196,s[48:51],s60 offen lds
SA m0,s63,0x1000
s_nop 0
BL v204,s[52:55],s61 offen lds
v_xor_b32 v241,4,v239
v_lshl_add_u32 v241,v241,4,v238
v_add_u32 v197,0xa000,v241
SA m0,s62,0x1400
v_xor_b32 v244,0,v243
v_lshl_add_u32 v244,v244,4,v242
v_add_u32 v205,0x20080,v244
BL v197,s[48:51],s60 offen lds
SA m0,s63,0x1400
s_nop 0
BL v205,s[52:55],s61 offen lds
v_xor_b32 v241,0,v239
v_lshl_add_u32 v241,v241,4,v238
v_add_u32 v198,0xc000,v241
SA m0,s62,0x1800
v_xor_b32 v244,2,v243
v_lshl_add_u32 v244,v244,4,v242
v_add_u32 v206,0x30000,v244
BL v198,s[48:51],s60 offen lds
SA m0,s63,0x1800
s_nop 0
BL v206,s[52:55],s61 offen lds
v_xor_b32 v241,4,v239
v_lshl_add_u32 v241,v241,4,v238
v_add_u32 v199,0xe000,v241
SA m0,s62,0x1c00
v_xor_b32 v244,2,v243
v_lshl_add_u32 v244,v244,4,v242
v_add_u32 v207,0x30080,v244
BL v199,s[48:51],s60 offen lds
SA m0,s63,0x1c00
s_nop 0
BL v207,s[52:55],s61 offen lds
SA s60,s60,0x80
SA s61,s61,0x80000
SA m0,s62,0x10000
s_nop 0
BL v192,s[48:51],s60 offen lds
SA m0,s63,0x10000
s_nop 0
BL v200,s[52:55],s61 offen lds
SA m0,s62,0x10400
s_nop 0
BL v193,s[48:51],s60 offen lds
SA m0,s63,0x10400
s_nop 0
BL v201,s[52:55],s61 offen lds
SA m0,s62,0x10800
s_nop 0
BL v194,s[48:51],s60 offen lds
SA m0,s63,0x10800
s_nop 0
BL v202,s[52:55],s61 offen lds
SA m0,s62,0x10c00
s_nop 0
BL v195,s[48:51],s60 offen lds
SA m0,s63,0x10c00
s_nop 0
BL v203,s[52:55],s61 offen lds
SA m0,s62,0x11000
s_nop 0
BL v196,s[48:51],s60 offen lds
SA m0,s63,0x11000
s_nop 0
BL v204,s[52:55],s61 offen lds
SA m0,s62,0x11400
s_nop 0
BL v197,s[48:51],s60 offen lds
SA m0,s63,0x11400
s_nop 0
BL v205,s[52:55],s61 offen lds
SA m0,s62,0x11800
s_nop 0
BL v198,s[48:51],s60 offen lds
SA m0,s63,0x11800
s_nop 0
BL v206,s[52:55],s61 offen lds
SA m0,s62,0x11c00
s_nop 0
BL v199,s[48:51],s60 offen lds
SA m0,s63,0x11c00
s_nop 0
BL v207,s[52:55],s61 offen lds
SA s60,s60,0x80
SA s61,s61,0x80000
s_mov_b64 s[44:45],s[28:29]
s_and_b32 s45,s45,0xffff
s_mov_b32 s46,0x5000000
s_mov_b32 s47,0x20000
v_mov_b32 v238,0
s_mov_b32 s84,0x3500000
buffer_load_dword v251,v238,s[44:47],s84 offen sc1
s_lshl_b32 s88,s37,2
SA s88,s88,s36
s_mov_b64 s[90:91],s[18:19]
s_mov_b64 s[92:93],s[20:21]
s_mov_b64 s[94:95],s[14:15]
s_cmp_lt_u32 s88,0x180
s_cselect_b32 s90,s90,s92
s_cselect_b32 s91,s91,s93
s_cselect_b32 s89,0,0x180
s_sub_u32 s89,s88,s89
SA s89,s89,0x80
s_lshl_b32 s89,s89,13
v_lshlrev_b32 v243,7,v255
s_sub_u32 s92,s88,0x300
s_min_u32 s92,s92,0xbf
s_lshl_b32 s92,s92,6
v_add_u32 v244,s92,v255
v_mul_u32_u24 v245,0xaaab,v244
v_lshrrev_b32 v245,18,v245
v_mul_u32_u24 v246,6,v245
v_sub_u32 v246,v244,v246
v_add_u32 v246,2,v246
v_lshlrev_b32 v245,10,v245
v_lshl_add_u32 v245,v246,7,v245
s_cmp_lt_u32 s88,0x300
s_cselect_b32 s90,s90,s94
s_cselect_b32 s91,s91,s95
s_cselect_b32 s89,s89,0
s_cselect_b32 s92,1,0
v_cmp_eq_u32 vcc,s92,1
SA s90,s90,s89
s_addc_u32 s91,s91,0
v_cndmask_b32 v243,v245,v243,vcc
global_load_dword v249,v243,s[90:91]
v_cmp_eq_u32 vcc,0,v255
v_mov_b32 v250,0x40000000
s_nop 0
v_cndmask_b32 v250,v250,0,vcc
s_lshl_b32 s64,s40,24
s_lshl_b32 s84,s38,19
SA s64,s64,s84
s_lshl_b32 s84,s39,16
SA s64,s64,s84
s_lshl_b32 s84,s41,15
SA s64,s64,s84
v_and_b32 v238,15,v255
v_lshrrev_b32 v239,1,v238
v_lshrrev_b32 v240,4,v255
v_xor_b32 v239,v239,v240
v_lshlrev_b32 v238,7,v238
v_xor_b32 v240,0,v239
v_lshl_add_u32 v208,v240,4,v238
v_xor_b32 v240,4,v239
v_lshl_add_u32 v209,v240,4,v238
s_lshl_b32 s84,s41,14
v_add_u32 v208,s84,v208
v_add_u32 v210,0x10000,v208
v_add_u32 v209,s84,v209
v_add_u32 v211,0x10000,v209
v_lshrrev_b32 v238,4,v255
v_lshlrev_b32 v238,11,v238
v_bfe_u32 v239,v255,2,2
v_lshl_add_u32 v238,v239,6,v238
v_and_b32 v239,3,v255
v_lshl_add_u32 v238,v239,3,v238
v_bfe_u32 v239,v255,4,1
v_lshl_add_u32 v212,v239,5,v238
v_xor_b32 v239,1,v239
v_lshl_add_u32 v213,v239,5,v238
s_lshl_b32 s84,s40,14
SA s84,s84,0x8000
v_add_u32 v212,s84,v212
v_add_u32 v214,0x10000,v212
v_add_u32 v213,s84,v213
v_add_u32 v215,0x10000,v213
s_and_b32 s67,s37,7
s_lshr_b32 s66,s37,3
v_lshrrev_b32 v238,3,v255
v_and_b32 v239,7,v255
v_lshrrev_b32 v240,1,v238
v_xor_b32 v239,v239,v240
v_lshlrev_b32 v238,10,v238
s_mul_i32 s84,s36,3
SA s84,s84,0
s_and_b32 s85,s84,1
s_lshl_b32 s85,s85,2
v_xor_b32 v241,s85,v239
v_lshl_add_u32 v216,v241,4,v238
s_lshl_b32 s86,s66,6
s_lshl_b32 s87,s84,3
SA s86,s86,s87
s_sub_u32 s88,s86,16
s_cmp_lt_u32 s86,16
s_cselect_b32 s88,s86,s88
s_lshl_b32 s68,s88,10
s_lshl_b32 s71,s84,10
s_mul_i32 s84,s36,3
SA s84,s84,1
s_and_b32 s85,s84,1
s_lshl_b32 s85,s85,2
v_xor_b32 v241,s85,v239
v_lshl_add_u32 v217,v241,4,v238
s_lshl_b32 s86,s66,6
s_lshl_b32 s87,s84,3
SA s86,s86,s87
s_sub_u32 s88,s86,16
s_cmp_lt_u32 s86,16
s_cselect_b32 s88,s86,s88
s_lshl_b32 s69,s88,10
s_lshl_b32 s72,s84,10
s_mul_i32 s84,s36,3
SA s84,s84,2
s_and_b32 s85,s84,1
s_lshl_b32 s85,s85,2
v_xor_b32 v241,s85,v239
v_lshl_add_u32 v218,v241,4,v238
s_lshl_b32 s86,s66,6
s_lshl_b32 s87,s84,3
SA s86,s86,s87
s_sub_u32 s88,s86,16
s_cmp_lt_u32 s86,16
s_cselect_b32 s88,s86,s88
s_lshl_b32 s70,s88,10
s_lshl_b32 s73,s84,10
v_bfe_u32 v238,v255,2,3
v_mul_u32_u24 v238,0xc00,v238
v_lshrrev_b32 v239,5,v255
v_lshl_add_u32 v238,v239,6,v238
v_and_b32 v239,3,v255
s_mul_i32 s84,s36,6
SA s84,s84,0
s_and_b32 s85,s84,1
s_lshl_b32 s85,s85,1
v_xor_b32 v240,s85,v239
v_lshl_add_u32 v240,v240,4,v238
s_and_b32 s85,s84,7
s_mul_i32 s85,s85,0x6000
s_lshr_b32 s86,s84,3
s_lshl_b32 s86,s86,10
SA s85,s85,s86
v_add_u32 v219,s85,v240
s_mul_i32 s84,s36,6
SA s84,s84,1
s_and_b32 s85,s84,1
s_lshl_b32 s85,s85,1
v_xor_b32 v240,s85,v239
v_lshl_add_u32 v240,v240,4,v238
s_and_b32 s85,s84,7
s_mul_i32 s85,s85,0x6000
s_lshr_b32 s86,s84,3
s_lshl_b32 s86,s86,10
SA s85,s85,s86
v_add_u32 v220,s85,v240
s_mul_i32 s84,s36,6
SA s84,s84,2
s_and_b32 s85,s84,1
s_lshl_b32 s85,s85,1
v_xor_b32 v240,s85,v239
v_lshl_add_u32 v240,v240,4,v238
s_and_b32 s85,s84,7
s_mul_i32 s85,s85,0x6000
s_lshr_b32 s86,s84,3
s_lshl_b32 s86,s86,10
SA s85,s85,s86
v_add_u32 v221,s85,v240
s_mul_i32 s84,s36,6
SA s84,s84,3
s_and_b32 s85,s84,1
s_lshl_b32 s85,s85,1
v_xor_b32 v240,s85,v239
v_lshl_add_u32 v240,v240,4,v238
s_and_b32 s85,s84,7
s_mul_i32 s85,s85,0x6000
s_lshr_b32 s86,s84,3
s_lshl_b32 s86,s86,10
SA s85,s85,s86
v_add_u32 v222,s85,v240
s_mul_i32 s84,s36,6
SA s84,s84,4
s_and_b32 s85,s84,1
s_lshl_b32 s85,s85,1
v_xor_b32 v240,s85,v239
v_lshl_add_u32 v240,v240,4,v238
s_and_b32 s85,s84,7
s_mul_i32 s85,s85,0x6000
s_lshr_b32 s86,s84,3
s_lshl_b32 s86,s86,10
SA s85,s85,s86
v_add_u32 v223,s85,v240
s_mul_i32 s84,s36,6
SA s84,s84,5
s_and_b32 s85,s84,1
s_lshl_b32 s85,s85,1
v_xor_b32 v240,s85,v239
v_lshl_add_u32 v240,v240,4,v238
s_and_b32 s85,s84,7
s_mul_i32 s85,s85,0x6000
s_lshr_b32 s86,s84,3
s_lshl_b32 s86,s86,10
SA s85,s85,s86
v_add_u32 v224,s85,v240
s_lshl_b32 s74,s67,7
s_mul_i32 s75,s36,0x1800
SA s75,s75,0x3000
v_and_b32 v238,15,v255
v_lshrrev_b32 v239,1,v238
v_lshrrev_b32 v240,4,v255
v_xor_b32 v239,v239,v240
v_lshlrev_b32 v238,7,v238
v_xor_b32 v240,0,v239
v_lshl_add_u32 v225,v240,4,v238
v_xor_b32 v240,4,v239
v_lshl_add_u32 v226,v240,4,v238
v_add_u32 v231,0x1b000,v225
v_add_u32 v232,0x1b000,v226
v_add_u32 v229,0x12000,v225
v_add_u32 v230,0x12000,v226
v_add_u32 v227,0x9000,v225
v_add_u32 v228,0x9000,v226
v_lshrrev_b32 v238,4,v255
v_lshlrev_b32 v238,10,v238
v_bfe_u32 v239,v255,2,2
v_lshl_add_u32 v238,v239,6,v238
v_and_b32 v239,3,v255
v_lshl_add_u32 v238,v239,3,v238
v_bfe_u32 v239,v255,4,1
v_xor_b32 v239,s41,v239
v_lshl_add_u32 v238,v239,5,v238
s_lshl_b32 s84,s40,9
SA s84,s84,0x3000
SA s85,s84,0
v_add_u32 v233,s85,v238
SA s85,s84,0x9000
v_add_u32 v234,s85,v238
SA s85,s84,0x12000
v_add_u32 v235,s85,v238
SA s85,s84,0x1b000
v_add_u32 v236,s85,v238
v_mov_b32 v0,0
v_mov_b32 v1,0
v_mov_b32 v2,0
v_mov_b32 v3,0
v_mov_b32 v4,0
v_mov_b32 v5,0
v_mov_b32 v6,0
v_mov_b32 v7,0
v_mov_b32 v8,0
v_mov_b32 v9,0
v_mov_b32 v10,0
v_mov_b32 v11,0
v_mov_b32 v12,0
v_mov_b32 v13,0
v_mov_b32 v14,0
v_mov_b32 v15,0
v_mov_b32 v16,0
v_mov_b32 v17,0
v_mov_b32 v18,0
v_mov_b32 v19,0
v_mov_b32 v20,0
v_mov_b32 v21,0
v_mov_b32 v22,0
v_mov_b32 v23,0
v_mov_b32 v24,0
v_mov_b32 v25,0
v_mov_b32 v26,0
v_mov_b32 v27,0
v_mov_b32 v28,0
v_mov_b32 v29,0
v_mov_b32 v30,0
v_mov_b32 v31,0
v_mov_b32 v32,0
v_mov_b32 v33,0
v_mov_b32 v34,0
v_mov_b32 v35,0
v_mov_b32 v36,0
v_mov_b32 v37,0
v_mov_b32 v38,0
v_mov_b32 v39,0
v_mov_b32 v40,0
v_mov_b32 v41,0
v_mov_b32 v42,0
v_mov_b32 v43,0
v_mov_b32 v44,0
v_mov_b32 v45,0
v_mov_b32 v46,0
v_mov_b32 v47,0
v_mov_b32 v48,0
v_mov_b32 v49,0
v_mov_b32 v50,0
v_mov_b32 v51,0
v_mov_b32 v52,0
v_mov_b32 v53,0
v_mov_b32 v54,0
v_mov_b32 v55,0
v_mov_b32 v56,0
v_mov_b32 v57,0
v_mov_b32 v58,0
v_mov_b32 v59,0
v_mov_b32 v60,0
v_mov_b32 v61,0
v_mov_b32 v62,0
v_mov_b32 v63,0
v_mov_b32 v64,0
v_mov_b32 v65,0
v_mov_b32 v66,0
v_mov_b32 v67,0
v_mov_b32 v68,0
v_mov_b32 v69,0
v_mov_b32 v70,0
v_mov_b32 v71,0
v_mov_b32 v72,0
v_mov_b32 v73,0
v_mov_b32 v74,0
v_mov_b32 v75,0
v_mov_b32 v76,0
v_mov_b32 v77,0
v_mov_b32 v78,0
v_mov_b32 v79,0
v_mov_b32 v80,0
v_mov_b32 v81,0
v_mov_b32 v82,0
v_mov_b32 v83,0
v_mov_b32 v84,0
v_mov_b32 v85,0
v_mov_b32 v86,0
v_mov_b32 v87,0
v_mov_b32 v88,0
v_mov_b32 v89,0
v_mov_b32 v90,0
v_mov_b32 v91,0
v_mov_b32 v92,0
v_mov_b32 v93,0
v_mov_b32 v94,0
v_mov_b32 v95,0
v_mov_b32 v96,0
v_mov_b32 v97,0
v_mov_b32 v98,0
v_mov_b32 v99,0
v_mov_b32 v100,0
v_mov_b32 v101,0
v_mov_b32 v102,0
v_mov_b32 v103,0
v_mov_b32 v104,0
v_mov_b32 v105,0
v_mov_b32 v106,0
v_mov_b32 v107,0
v_mov_b32 v108,0
v_mov_b32 v109,0
v_mov_b32 v110,0
v_mov_b32 v111,0
v_mov_b32 v112,0
v_mov_b32 v113,0
v_mov_b32 v114,0
v_mov_b32 v115,0
v_mov_b32 v116,0
v_mov_b32 v117,0
v_mov_b32 v118,0
v_mov_b32 v119,0
v_mov_b32 v120,0
v_mov_b32 v121,0
v_mov_b32 v122,0
v_mov_b32 v123,0
v_mov_b32 v124,0
v_mov_b32 v125,0
v_mov_b32 v126,0
v_mov_b32 v127,0
v_mov_b32 v128,0
v_mov_b32 v129,0
v_mov_b32 v130,0
v_mov_b32 v131,0
v_mov_b32 v132,0
v_mov_b32 v133,0
v_mov_b32 v134,0
v_mov_b32 v135,0
v_mov_b32 v136,0
v_mov_b32 v137,0
v_mov_b32 v138,0
v_mov_b32 v139,0
v_mov_b32 v140,0
v_mov_b32 v141,0
v_mov_b32 v142,0
v_mov_b32 v143,0
v_mov_b32 v144,0
v_mov_b32 v145,0
v_mov_b32 v146,0
v_mov_b32 v147,0
v_mov_b32 v148,0
v_mov_b32 v149,0
v_mov_b32 v150,0
v_mov_b32 v151,0
v_mov_b32 v152,0
v_mov_b32 v153,0
v_mov_b32 v154,0
v_mov_b32 v155,0
v_mov_b32 v156,0
v_mov_b32 v157,0
v_mov_b32 v158,0
v_mov_b32 v159,0
v_mov_b32 v160,0
v_mov_b32 v161,0
v_mov_b32 v162,0
v_mov_b32 v163,0
v_mov_b32 v164,0
v_mov_b32 v165,0
v_mov_b32 v166,0
v_mov_b32 v167,0
v_mov_b32 v168,0
v_mov_b32 v169,0
v_mov_b32 v170,0
v_mov_b32 v171,0
v_mov_b32 v172,0
v_mov_b32 v173,0
v_mov_b32 v174,0
v_mov_b32 v175,0
v_mov_b32 v176,0
v_mov_b32 v177,0
v_mov_b32 v178,0
v_mov_b32 v179,0
v_mov_b32 v180,0
v_mov_b32 v181,0
v_mov_b32 v182,0
v_mov_b32 v183,0
v_mov_b32 v184,0
v_mov_b32 v185,0
v_mov_b32 v186,0
v_mov_b32 v187,0
v_mov_b32 v188,0
v_mov_b32 v189,0
v_mov_b32 v190,0
)~"
R"~(v_mov_b32 v191,0
AW a0,0
AW a1,0
AW a2,0
AW a3,0
AW a4,0
AW a5,0
AW a6,0
AW a7,0
AW a8,0
AW a9,0
AW a10,0
AW a11,0
AW a12,0
AW a13,0
AW a14,0
AW a15,0
AW a16,0
AW a17,0
AW a18,0
AW a19,0
AW a20,0
AW a21,0
AW a22,0
AW a23,0
AW a24,0
AW a25,0
AW a26,0
AW a27,0
AW a28,0
AW a29,0
AW a30,0
AW a31,0
AW a32,0
AW a33,0
AW a34,0
AW a35,0
AW a36,0
AW a37,0
AW a38,0
AW a39,0
AW a40,0
AW a41,0
AW a42,0
AW a43,0
AW a44,0
AW a45,0
AW a46,0
AW a47,0
AW a48,0
AW a49,0
AW a50,0
AW a51,0
AW a52,0
AW a53,0
AW a54,0
AW a55,0
AW a56,0
AW a57,0
AW a58,0
AW a59,0
AW a60,0
AW a61,0
AW a62,0
AW a63,0
W vmcnt(18)
s_barrier
s_cmp_eq_u32 s40,0
s_cbranch_scc0 fk_kv_v
RD a[64:67],v208
RD a[68:71],v209
RD a[72:75],v208 offset:2048
RD a[76:79],v209 offset:2048
RD a[80:83],v208 offset:4096
RD a[84:87],v209 offset:4096
RD a[88:91],v208 offset:6144
RD a[92:95],v209 offset:6144
RD a[96:99],v208 offset:8192
RD a[100:103],v209 offset:8192
RD a[104:107],v208 offset:10240
RD a[108:111],v209 offset:10240
RD a[112:115],v208 offset:12288
RD a[116:119],v209 offset:12288
RD a[120:123],v208 offset:14336
RD a[124:127],v209 offset:14336
TR a[128:129],v212
TR a[130:131],v212 offset:256
TR a[132:133],v212 offset:8192
TR a[134:135],v212 offset:8448
TR a[136:137],v213
TR a[138:139],v213 offset:256
TR a[140:141],v213 offset:8192
TR a[142:143],v213 offset:8448
W lgkmcnt(6)
M v[0:3],a[128:131],a[64:67],v[0:3]
W lgkmcnt(4)
M v[0:3],a[132:135],a[68:71],v[0:3]
TR a[144:145],v212 offset:512
M v[8:11],a[128:131],a[72:75],v[8:11]
M v[8:11],a[132:135],a[76:79],v[8:11]
M v[32:35],a[128:131],a[80:83],v[32:35]
M v[32:35],a[132:135],a[84:87],v[32:35]
TR a[146:147],v212 offset:768
M v[40:43],a[128:131],a[88:91],v[40:43]
M v[40:43],a[132:135],a[92:95],v[40:43]
M v[16:19],a[128:131],a[96:99],v[16:19]
M v[16:19],a[132:135],a[100:103],v[16:19]
TR a[148:149],v212 offset:8704
M v[24:27],a[128:131],a[104:107],v[24:27]
M v[24:27],a[132:135],a[108:111],v[24:27]
M v[48:51],a[128:131],a[112:115],v[48:51]
M v[48:51],a[132:135],a[116:119],v[48:51]
TR a[150:151],v212 offset:8960
M v[56:59],a[128:131],a[120:123],v[56:59]
M v[56:59],a[132:135],a[124:127],v[56:59]
W lgkmcnt(6)
M v[4:7],a[136:139],a[64:67],v[4:7]
W lgkmcnt(4)
M v[4:7],a[140:143],a[68:71],v[4:7]
TR a[152:153],v213 offset:512
M v[12:15],a[136:139],a[72:75],v[12:15]
M v[12:15],a[140:143],a[76:79],v[12:15]
M v[36:39],a[136:139],a[80:83],v[36:39]
M v[36:39],a[140:143],a[84:87],v[36:39]
TR a[154:155],v213 offset:768
M v[44:47],a[136:139],a[88:91],v[44:47]
M v[44:47],a[140:143],a[92:95],v[44:47]
M v[20:23],a[136:139],a[96:99],v[20:23]
M v[20:23],a[140:143],a[100:103],v[20:23]
TR a[156:157],v213 offset:8704
M v[28:31],a[136:139],a[104:107],v[28:31]
M v[28:31],a[140:143],a[108:111],v[28:31]
M v[52:55],a[136:139],a[112:115],v[52:55]
M v[52:55],a[140:143],a[116:119],v[52:55]
TR a[158:159],v213 offset:8960
M v[60:63],a[136:139],a[120:123],v[60:63]
M v[60:63],a[140:143],a[124:127],v[60:63]
W lgkmcnt(6)
M v[64:67],a[144:147],a[64:67],v[64:67]
W lgkmcnt(4)
M v[64:67],a[148:151],a[68:71],v[64:67]
TR a[128:129],v212 offset:1024
M v[72:75],a[144:147],a[72:75],v[72:75]
M v[72:75],a[148:151],a[76:79],v[72:75]
M v[96:99],a[144:147],a[80:83],v[96:99]
M v[96:99],a[148:151],a[84:87],v[96:99]
TR a[130:131],v212 offset:1280
M v[104:107],a[144:147],a[88:91],v[104:107]
M v[104:107],a[148:151],a[92:95],v[104:107]
M v[80:83],a[144:147],a[96:99],v[80:83]
M v[80:83],a[148:151],a[100:103],v[80:83]
TR a[132:133],v212 offset:9216
M v[88:91],a[144:147],a[104:107],v[88:91]
M v[88:91],a[148:151],a[108:111],v[88:91]
M v[112:115],a[144:147],a[112:115],v[112:115]
M v[112:115],a[148:151],a[116:119],v[112:115]
TR a[134:135],v212 offset:9472
M v[120:123],a[144:147],a[120:123],v[120:123]
M v[120:123],a[148:151],a[124:127],v[120:123]
W lgkmcnt(6)
M v[68:71],a[152:155],a[64:67],v[68:71]
W lgkmcnt(4)
M v[68:71],a[156:159],a[68:71],v[68:71]
TR a[136:137],v213 offset:1024
M v[76:79],a[152:155],a[72:75],v[76:79]
M v[76:79],a[156:159],a[76:79],v[76:79]
M v[100:103],a[152:155],a[80:83],v[100:103]
M v[100:103],a[156:159],a[84:87],v[100:103]
TR a[138:139],v213 offset:1280
M v[108:111],a[152:155],a[88:91],v[108:111]
M v[108:111],a[156:159],a[92:95],v[108:111]
M v[84:87],a[152:155],a[96:99],v[84:87]
M v[84:87],a[156:159],a[100:103],v[84:87]
TR a[140:141],v213 offset:9216
M v[92:95],a[152:155],a[104:107],v[92:95]
M v[92:95],a[156:159],a[108:111],v[92:95]
M v[116:119],a[152:155],a[112:115],v[116:119]
M v[116:119],a[156:159],a[116:119],v[116:119]
TR a[142:143],v213 offset:9472
M v[124:127],a[152:155],a[120:123],v[124:127]
M v[124:127],a[156:159],a[124:127],v[124:127]
W lgkmcnt(6)
M v[128:131],a[128:131],a[64:67],v[128:131]
W lgkmcnt(4)
M v[128:131],a[132:135],a[68:71],v[128:131]
TR a[144:145],v212 offset:1536
M v[136:139],a[128:131],a[72:75],v[136:139]
M v[136:139],a[132:135],a[76:79],v[136:139]
M v[160:163],a[128:131],a[80:83],v[160:163]
M v[160:163],a[132:135],a[84:87],v[160:163]
TR a[146:147],v212 offset:1792
M v[168:171],a[128:131],a[88:91],v[168:171]
M v[168:171],a[132:135],a[92:95],v[168:171]
M v[144:147],a[128:131],a[96:99],v[144:147]
M v[144:147],a[132:135],a[100:103],v[144:147]
TR a[148:149],v212 offset:9728
M v[152:155],a[128:131],a[104:107],v[152:155]
M v[152:155],a[132:135],a[108:111],v[152:155]
M v[176:179],a[128:131],a[112:115],v[176:179]
M v[176:179],a[132:135],a[116:119],v[176:179]
TR a[150:151],v212 offset:9984
M v[184:187],a[128:131],a[120:123],v[184:187]
M v[184:187],a[132:135],a[124:127],v[184:187]
W lgkmcnt(6)
M v[132:135],a[136:139],a[64:67],v[132:135]
W lgkmcnt(4)
M v[132:135],a[140:143],a[68:71],v[132:135]
TR a[152:153],v213 offset:1536
M v[140:143],a[136:139],a[72:75],v[140:143]
M v[140:143],a[140:143],a[76:79],v[140:143]
M v[164:167],a[136:139],a[80:83],v[164:167]
M v[164:167],a[140:143],a[84:87],v[164:167]
TR a[154:155],v213 offset:1792
M v[172:175],a[136:139],a[88:91],v[172:175]
M v[172:175],a[140:143],a[92:95],v[172:175]
M v[148:151],a[136:139],a[96:99],v[148:151]
M v[148:151],a[140:143],a[100:103],v[148:151]
TR a[156:157],v213 offset:9728
M v[156:159],a[136:139],a[104:107],v[156:159]
M v[156:159],a[140:143],a[108:111],v[156:159]
M v[180:183],a[136:139],a[112:115],v[180:183]
M v[180:183],a[140:143],a[116:119],v[180:183]
TR a[158:159],v213 offset:9984
M v[188:191],a[136:139],a[120:123],v[188:191]
M v[188:191],a[140:143],a[124:127],v[188:191]
W lgkmcnt(6)
M a[0:3],a[144:147],a[64:67],a[0:3]
W lgkmcnt(4)
M a[0:3],a[148:151],a[68:71],a[0:3]
M a[8:11],a[144:147],a[72:75],a[8:11]
M a[8:11],a[148:151],a[76:79],a[8:11]
M a[32:35],a[144:147],a[80:83],a[32:35]
M a[32:35],a[148:151],a[84:87],a[32:35]
M a[40:43],a[144:147],a[88:91],a[40:43]
M a[40:43],a[148:151],a[92:95],a[40:43]
M a[16:19],a[144:147],a[96:99],a[16:19]
M a[16:19],a[148:151],a[100:103],a[16:19]
M a[24:27],a[144:147],a[104:107],a[24:27]
M a[24:27],a[148:151],a[108:111],a[24:27]
M a[48:51],a[144:147],a[112:115],a[48:51]
M a[48:51],a[148:151],a[116:119],a[48:51]
M a[56:59],a[144:147],a[120:123],a[56:59]
M a[56:59],a[148:151],a[124:127],a[56:59]
W vmcnt(2) lgkmcnt(0)
s_barrier
M a[4:7],a[152:155],a[64:67],a[4:7]
M a[4:7],a[156:159],a[68:71],a[4:7]
TR a[128:129],v214
M a[12:15],a[152:155],a[72:75],a[12:15]
RD a[64:67],v210
RD a[68:71],v211
M a[12:15],a[156:159],a[76:79],a[12:15]
TR a[136:137],v215
M a[36:39],a[152:155],a[80:83],a[36:39]
RD a[72:75],v210 offset:2048
RD a[76:79],v211 offset:2048
M a[36:39],a[156:159],a[84:87],a[36:39]
TR a[130:131],v214 offset:256
M a[44:47],a[152:155],a[88:91],a[44:47]
RD a[80:83],v210 offset:4096
RD a[84:87],v211 offset:4096
M a[44:47],a[156:159],a[92:95],a[44:47]
TR a[138:139],v215 offset:256
M a[20:23],a[152:155],a[96:99],a[20:23]
RD a[88:91],v210 offset:6144
RD a[92:95],v211 offset:6144
M a[20:23],a[156:159],a[100:103],a[20:23]
TR a[132:133],v214 offset:8192
M a[28:31],a[152:155],a[104:107],a[28:31]
RD a[96:99],v210 offset:8192
RD a[100:103],v211 offset:8192
M a[28:31],a[156:159],a[108:111],a[28:31]
TR a[140:141],v215 offset:8192
M a[52:55],a[152:155],a[112:115],a[52:55]
RD a[104:107],v210 offset:10240
RD a[108:111],v211 offset:10240
M a[52:55],a[156:159],a[116:119],a[52:55]
TR a[134:135],v214 offset:8448
M a[60:63],a[152:155],a[120:123],a[60:63]
RD a[112:115],v210 offset:12288
RD a[116:119],v211 offset:12288
M a[60:63],a[156:159],a[124:127],a[60:63]
TR a[142:143],v215 offset:8448
RD a[120:123],v210 offset:14336
RD a[124:127],v211 offset:14336
W lgkmcnt(15)
M v[0:3],a[128:131],a[64:67],v[0:3]
W lgkmcnt(5)
M v[0:3],a[132:135],a[68:71],v[0:3]
TR a[144:145],v214 offset:512
M v[8:11],a[128:131],a[72:75],v[8:11]
SA m0,s62,0
M v[8:11],a[132:135],a[76:79],v[8:11]
BL v192,s[48:51],s60 offen lds
M v[32:35],a[128:131],a[80:83],v[32:35]
M v[32:35],a[132:135],a[84:87],v[32:35]
TR a[146:147],v214 offset:768
M v[40:43],a[128:131],a[88:91],v[40:43]
M v[40:43],a[132:135],a[92:95],v[40:43]
SA m0,s63,0
M v[16:19],a[128:131],a[96:99],v[16:19]
BL v200,s[52:55],s61 offen lds
M v[16:19],a[132:135],a[100:103],v[16:19]
TR a[148:149],v214 offset:8704
M v[24:27],a[128:131],a[104:107],v[24:27]
M v[24:27],a[132:135],a[108:111],v[24:27]
W lgkmcnt(7)
M v[48:51],a[128:131],a[112:115],v[48:51]
W lgkmcnt(6)
M v[48:51],a[132:135],a[116:119],v[48:51]
SA m0,s62,0x400
TR a[150:151],v214 offset:8960
W lgkmcnt(5)
M v[56:59],a[128:131],a[120:123],v[56:59]
BL v193,s[48:51],s60 offen lds
W lgkmcnt(4)
M v[56:59],a[132:135],a[124:127],v[56:59]
M v[4:7],a[136:139],a[64:67],v[4:7]
M v[4:7],a[140:143],a[68:71],v[4:7]
TR a[152:153],v215 offset:512
M v[12:15],a[136:139],a[72:75],v[12:15]
SA m0,s63,0x400
M v[12:15],a[140:143],a[76:79],v[12:15]
BL v201,s[52:55],s61 offen lds
M v[36:39],a[136:139],a[80:83],v[36:39]
M v[36:39],a[140:143],a[84:87],v[36:39]
TR a[154:155],v215 offset:768
M v[44:47],a[136:139],a[88:91],v[44:47]
M v[44:47],a[140:143],a[92:95],v[44:47]
SA m0,s62,0x800
M v[20:23],a[136:139],a[96:99],v[20:23]
BL v194,s[48:51],s60 offen lds
M v[20:23],a[140:143],a[100:103],v[20:23]
TR a[156:157],v215 offset:8704
M v[28:31],a[136:139],a[104:107],v[28:31]
M v[28:31],a[140:143],a[108:111],v[28:31]
M v[52:55],a[136:139],a[112:115],v[52:55]
M v[52:55],a[140:143],a[116:119],v[52:55]
SA m0,s63,0x800
TR a[158:159],v215 offset:8960
M v[60:63],a[136:139],a[120:123],v[60:63]
BL v202,s[52:55],s61 offen lds
M v[60:63],a[140:143],a[124:127],v[60:63]
W lgkmcnt(6)
M v[64:67],a[144:147],a[64:67],v[64:67]
W lgkmcnt(4)
M v[64:67],a[148:151],a[68:71],v[64:67]
TR a[128:129],v214 offset:1024
M v[72:75],a[144:147],a[72:75],v[72:75]
SA m0,s62,0xc00
M v[72:75],a[148:151],a[76:79],v[72:75]
BL v195,s[48:51],s60 offen lds
M v[96:99],a[144:147],a[80:83],v[96:99]
M v[96:99],a[148:151],a[84:87],v[96:99]
TR a[130:131],v214 offset:1280
M v[104:107],a[144:147],a[88:91],v[104:107]
M v[104:107],a[148:151],a[92:95],v[104:107]
SA m0,s63,0xc00
M v[80:83],a[144:147],a[96:99],v[80:83]
BL v203,s[52:55],s61 offen lds
M v[80:83],a[148:151],a[100:103],v[80:83]
TR a[132:133],v214 offset:9216
M v[88:91],a[144:147],a[104:107],v[88:91]
M v[88:91],a[148:151],a[108:111],v[88:91]
M v[112:115],a[144:147],a[112:115],v[112:115]
M v[112:115],a[148:151],a[116:119],v[112:115]
SA m0,s62,0x1000
TR a[134:135],v214 offset:9472
M v[120:123],a[144:147],a[120:123],v[120:123]
BL v196,s[48:51],s60 offen lds
M v[120:123],a[148:151],a[124:127],v[120:123]
W lgkmcnt(6)
M v[68:71],a[152:155],a[64:67],v[68:71]
W lgkmcnt(4)
M v[68:71],a[156:159],a[68:71],v[68:71]
TR a[136:137],v215 offset:1024
M v[76:79],a[152:155],a[72:75],v[76:79]
SA m0,s63,0x1000
M v[76:79],a[156:159],a[76:79],v[76:79]
BL v204,s[52:55],s61 offen lds
M v[100:103],a[152:155],a[80:83],v[100:103]
M v[100:103],a[156:159],a[84:87],v[100:103]
TR a[138:139],v215 offset:1280
M v[108:111],a[152:155],a[88:91],v[108:111]
M v[108:111],a[156:159],a[92:95],v[108:111]
SA m0,s62,0x1400
M v[84:87],a[152:155],a[96:99],v[84:87]
BL v197,s[48:51],s60 offen lds
M v[84:87],a[156:159],a[100:103],v[84:87]
TR a[140:141],v215 offset:9216
M v[92:95],a[152:155],a[104:107],v[92:95]
M v[92:95],a[156:159],a[108:111],v[92:95]
M v[116:119],a[152:155],a[112:115],v[116:119]
M v[116:119],a[156:159],a[116:119],v[116:119]
SA m0,s63,0x1400
TR a[142:143],v215 offset:9472
M v[124:127],a[152:155],a[120:123],v[124:127]
BL v205,s[52:55],s61 offen lds
M v[124:127],a[156:159],a[124:127],v[124:127]
W lgkmcnt(6)
M v[128:131],a[128:131],a[64:67],v[128:131]
W lgkmcnt(4)
M v[128:131],a[132:135],a[68:71],v[128:131]
TR a[144:145],v214 offset:1536
M v[136:139],a[128:131],a[72:75],v[136:139]
SA m0,s62,0x1800
M v[136:139],a[132:135],a[76:79],v[136:139]
BL v198,s[48:51],s60 offen lds
M v[160:163],a[128:131],a[80:83],v[160:163]
M v[160:163],a[132:135],a[84:87],v[160:163]
TR a[146:147],v214 offset:1792
M v[168:171],a[128:131],a[88:91],v[168:171]
M v[168:171],a[132:135],a[92:95],v[168:171]
SA m0,s63,0x1800
M v[144:147],a[128:131],a[96:99],v[144:147]
BL v206,s[52:55],s61 offen lds
M v[144:147],a[132:135],a[100:103],v[144:147]
TR a[148:149],v214 offset:9728
M v[152:155],a[128:131],a[104:107],v[152:155]
M v[152:155],a[132:135],a[108:111],v[152:155]
M v[176:179],a[128:131],a[112:115],v[176:179]
M v[176:179],a[132:135],a[116:119],v[176:179]
SA m0,s62,0x1c00
TR a[150:151],v214 offset:9984
M v[184:187],a[128:131],a[120:123],v[184:187]
BL v199,s[48:51],s60 offen lds
M v[184:187],a[132:135],a[124:127],v[184:187]
W lgkmcnt(6)
M v[132:135],a[136:139],a[64:67],v[132:135]
W lgkmcnt(4)
M v[132:135],a[140:143],a[68:71],v[132:135]
TR a[152:153],v215 offset:1536
M v[140:143],a[136:139],a[72:75],v[140:143]
SA m0,s63,0x1c00
M v[140:143],a[140:143],a[76:79],v[140:143]
BL v207,s[52:55],s61 offen lds
M v[164:167],a[136:139],a[80:83],v[164:167]
SA s60,s60,0x80
M v[164:167],a[140:143],a[84:87],v[164:167]
SA s61,s61,0x80000
TR a[154:155],v215 offset:1792
M v[172:175],a[136:139],a[88:91],v[172:175]
M v[172:175],a[140:143],a[92:95],v[172:175]
M v[148:151],a[136:139],a[96:99],v[148:151]
M v[148:151],a[140:143],a[100:103],v[148:151]
TR a[156:157],v215 offset:9728
M v[156:159],a[136:139],a[104:107],v[156:159]
M v[156:159],a[140:143],a[108:111],v[156:159]
M v[180:183],a[136:139],a[112:115],v[180:183]
M v[180:183],a[140:143],a[116:119],v[180:183]
)~"
R"~(TR a[158:159],v215 offset:9984
M v[188:191],a[136:139],a[120:123],v[188:191]
M v[188:191],a[140:143],a[124:127],v[188:191]
W lgkmcnt(6)
M a[0:3],a[144:147],a[64:67],a[0:3]
W lgkmcnt(4)
M a[0:3],a[148:151],a[68:71],a[0:3]
M a[8:11],a[144:147],a[72:75],a[8:11]
M a[8:11],a[148:151],a[76:79],a[8:11]
M a[32:35],a[144:147],a[80:83],a[32:35]
M a[32:35],a[148:151],a[84:87],a[32:35]
M a[40:43],a[144:147],a[88:91],a[40:43]
M a[40:43],a[148:151],a[92:95],a[40:43]
M a[16:19],a[144:147],a[96:99],a[16:19]
M a[16:19],a[148:151],a[100:103],a[16:19]
M a[24:27],a[144:147],a[104:107],a[24:27]
M a[24:27],a[148:151],a[108:111],a[24:27]
M a[48:51],a[144:147],a[112:115],a[48:51]
M a[48:51],a[148:151],a[116:119],a[48:51]
M a[56:59],a[144:147],a[120:123],a[56:59]
M a[56:59],a[148:151],a[124:127],a[56:59]
W vmcnt(0) lgkmcnt(0)
s_barrier
M a[4:7],a[152:155],a[64:67],a[4:7]
M a[4:7],a[156:159],a[68:71],a[4:7]
TR a[128:129],v212
M a[12:15],a[152:155],a[72:75],a[12:15]
RD a[64:67],v208
RD a[68:71],v209
M a[12:15],a[156:159],a[76:79],a[12:15]
TR a[136:137],v213
M a[36:39],a[152:155],a[80:83],a[36:39]
RD a[72:75],v208 offset:2048
RD a[76:79],v209 offset:2048
M a[36:39],a[156:159],a[84:87],a[36:39]
TR a[130:131],v212 offset:256
M a[44:47],a[152:155],a[88:91],a[44:47]
RD a[80:83],v208 offset:4096
RD a[84:87],v209 offset:4096
M a[44:47],a[156:159],a[92:95],a[44:47]
TR a[138:139],v213 offset:256
M a[20:23],a[152:155],a[96:99],a[20:23]
RD a[88:91],v208 offset:6144
RD a[92:95],v209 offset:6144
M a[20:23],a[156:159],a[100:103],a[20:23]
TR a[132:133],v212 offset:8192
M a[28:31],a[152:155],a[104:107],a[28:31]
RD a[96:99],v208 offset:8192
RD a[100:103],v209 offset:8192
M a[28:31],a[156:159],a[108:111],a[28:31]
TR a[140:141],v213 offset:8192
M a[52:55],a[152:155],a[112:115],a[52:55]
RD a[104:107],v208 offset:10240
RD a[108:111],v209 offset:10240
M a[52:55],a[156:159],a[116:119],a[52:55]
TR a[134:135],v212 offset:8448
M a[60:63],a[152:155],a[120:123],a[60:63]
RD a[112:115],v208 offset:12288
RD a[116:119],v209 offset:12288
M a[60:63],a[156:159],a[124:127],a[60:63]
TR a[142:143],v213 offset:8448
RD a[120:123],v208 offset:14336
RD a[124:127],v209 offset:14336
W lgkmcnt(15)
M v[0:3],a[128:131],a[64:67],v[0:3]
W lgkmcnt(5)
M v[0:3],a[132:135],a[68:71],v[0:3]
TR a[144:145],v212 offset:512
M v[8:11],a[128:131],a[72:75],v[8:11]
SA m0,s62,0x10000
M v[8:11],a[132:135],a[76:79],v[8:11]
BL v192,s[48:51],s60 offen lds
M v[32:35],a[128:131],a[80:83],v[32:35]
M v[32:35],a[132:135],a[84:87],v[32:35]
TR a[146:147],v212 offset:768
M v[40:43],a[128:131],a[88:91],v[40:43]
M v[40:43],a[132:135],a[92:95],v[40:43]
SA m0,s63,0x10000
M v[16:19],a[128:131],a[96:99],v[16:19]
BL v200,s[52:55],s61 offen lds
M v[16:19],a[132:135],a[100:103],v[16:19]
TR a[148:149],v212 offset:8704
M v[24:27],a[128:131],a[104:107],v[24:27]
M v[24:27],a[132:135],a[108:111],v[24:27]
W lgkmcnt(7)
M v[48:51],a[128:131],a[112:115],v[48:51]
W lgkmcnt(6)
M v[48:51],a[132:135],a[116:119],v[48:51]
SA m0,s62,0x10400
TR a[150:151],v212 offset:8960
W lgkmcnt(5)
M v[56:59],a[128:131],a[120:123],v[56:59]
BL v193,s[48:51],s60 offen lds
W lgkmcnt(4)
M v[56:59],a[132:135],a[124:127],v[56:59]
M v[4:7],a[136:139],a[64:67],v[4:7]
M v[4:7],a[140:143],a[68:71],v[4:7]
TR a[152:153],v213 offset:512
M v[12:15],a[136:139],a[72:75],v[12:15]
SA m0,s63,0x10400
M v[12:15],a[140:143],a[76:79],v[12:15]
BL v201,s[52:55],s61 offen lds
M v[36:39],a[136:139],a[80:83],v[36:39]
M v[36:39],a[140:143],a[84:87],v[36:39]
TR a[154:155],v213 offset:768
M v[44:47],a[136:139],a[88:91],v[44:47]
M v[44:47],a[140:143],a[92:95],v[44:47]
SA m0,s62,0x10800
M v[20:23],a[136:139],a[96:99],v[20:23]
BL v194,s[48:51],s60 offen lds
M v[20:23],a[140:143],a[100:103],v[20:23]
TR a[156:157],v213 offset:8704
M v[28:31],a[136:139],a[104:107],v[28:31]
M v[28:31],a[140:143],a[108:111],v[28:31]
M v[52:55],a[136:139],a[112:115],v[52:55]
M v[52:55],a[140:143],a[116:119],v[52:55]
SA m0,s63,0x10800
TR a[158:159],v213 offset:8960
M v[60:63],a[136:139],a[120:123],v[60:63]
BL v202,s[52:55],s61 offen lds
M v[60:63],a[140:143],a[124:127],v[60:63]
W lgkmcnt(6)
M v[64:67],a[144:147],a[64:67],v[64:67]
W lgkmcnt(4)
M v[64:67],a[148:151],a[68:71],v[64:67]
TR a[128:129],v212 offset:1024
M v[72:75],a[144:147],a[72:75],v[72:75]
SA m0,s62,0x10c00
M v[72:75],a[148:151],a[76:79],v[72:75]
BL v195,s[48:51],s60 offen lds
M v[96:99],a[144:147],a[80:83],v[96:99]
M v[96:99],a[148:151],a[84:87],v[96:99]
TR a[130:131],v212 offset:1280
M v[104:107],a[144:147],a[88:91],v[104:107]
M v[104:107],a[148:151],a[92:95],v[104:107]
SA m0,s63,0x10c00
M v[80:83],a[144:147],a[96:99],v[80:83]
BL v203,s[52:55],s61 offen lds
M v[80:83],a[148:151],a[100:103],v[80:83]
TR a[132:133],v212 offset:9216
M v[88:91],a[144:147],a[104:107],v[88:91]
M v[88:91],a[148:151],a[108:111],v[88:91]
M v[112:115],a[144:147],a[112:115],v[112:115]
M v[112:115],a[148:151],a[116:119],v[112:115]
SA m0,s62,0x11000
TR a[134:135],v212 offset:9472
M v[120:123],a[144:147],a[120:123],v[120:123]
BL v196,s[48:51],s60 offen lds
M v[120:123],a[148:151],a[124:127],v[120:123]
W lgkmcnt(6)
M v[68:71],a[152:155],a[64:67],v[68:71]
W lgkmcnt(4)
M v[68:71],a[156:159],a[68:71],v[68:71]
TR a[136:137],v213 offset:1024
M v[76:79],a[152:155],a[72:75],v[76:79]
SA m0,s63,0x11000
M v[76:79],a[156:159],a[76:79],v[76:79]
BL v204,s[52:55],s61 offen lds
M v[100:103],a[152:155],a[80:83],v[100:103]
M v[100:103],a[156:159],a[84:87],v[100:103]
TR a[138:139],v213 offset:1280
M v[108:111],a[152:155],a[88:91],v[108:111]
M v[108:111],a[156:159],a[92:95],v[108:111]
SA m0,s62,0x11400
M v[84:87],a[152:155],a[96:99],v[84:87]
BL v197,s[48:51],s60 offen lds
M v[84:87],a[156:159],a[100:103],v[84:87]
TR a[140:141],v213 offset:9216
M v[92:95],a[152:155],a[104:107],v[92:95]
M v[92:95],a[156:159],a[108:111],v[92:95]
M v[116:119],a[152:155],a[112:115],v[116:119]
M v[116:119],a[156:159],a[116:119],v[116:119]
SA m0,s63,0x11400
TR a[142:143],v213 offset:9472
M v[124:127],a[152:155],a[120:123],v[124:127]
BL v205,s[52:55],s61 offen lds
M v[124:127],a[156:159],a[124:127],v[124:127]
W lgkmcnt(6)
M v[128:131],a[128:131],a[64:67],v[128:131]
W lgkmcnt(4)
M v[128:131],a[132:135],a[68:71],v[128:131]
TR a[144:145],v212 offset:1536
M v[136:139],a[128:131],a[72:75],v[136:139]
SA m0,s62,0x11800
M v[136:139],a[132:135],a[76:79],v[136:139]
BL v198,s[48:51],s60 offen lds
M v[160:163],a[128:131],a[80:83],v[160:163]
M v[160:163],a[132:135],a[84:87],v[160:163]
TR a[146:147],v212 offset:1792
M v[168:171],a[128:131],a[88:91],v[168:171]
M v[168:171],a[132:135],a[92:95],v[168:171]
SA m0,s63,0x11800
M v[144:147],a[128:131],a[96:99],v[144:147]
BL v206,s[52:55],s61 offen lds
M v[144:147],a[132:135],a[100:103],v[144:147]
TR a[148:149],v212 offset:9728
M v[152:155],a[128:131],a[104:107],v[152:155]
M v[152:155],a[132:135],a[108:111],v[152:155]
M v[176:179],a[128:131],a[112:115],v[176:179]
M v[176:179],a[132:135],a[116:119],v[176:179]
SA m0,s62,0x11c00
TR a[150:151],v212 offset:9984
M v[184:187],a[128:131],a[120:123],v[184:187]
BL v199,s[48:51],s60 offen lds
M v[184:187],a[132:135],a[124:127],v[184:187]
W lgkmcnt(6)
M v[132:135],a[136:139],a[64:67],v[132:135]
W lgkmcnt(4)
M v[132:135],a[140:143],a[68:71],v[132:135]
TR a[152:153],v213 offset:1536
M v[140:143],a[136:139],a[72:75],v[140:143]
SA m0,s63,0x11c00
M v[140:143],a[140:143],a[76:79],v[140:143]
BL v207,s[52:55],s61 offen lds
M v[164:167],a[136:139],a[80:83],v[164:167]
SA s60,s60,0x80
M v[164:167],a[140:143],a[84:87],v[164:167]
SA s61,s61,0x80000
TR a[154:155],v213 offset:1792
M v[172:175],a[136:139],a[88:91],v[172:175]
M v[172:175],a[140:143],a[92:95],v[172:175]
M v[148:151],a[136:139],a[96:99],v[148:151]
M v[148:151],a[140:143],a[100:103],v[148:151]
TR a[156:157],v213 offset:9728
M v[156:159],a[136:139],a[104:107],v[156:159]
M v[156:159],a[140:143],a[108:111],v[156:159]
M v[180:183],a[136:139],a[112:115],v[180:183]
M v[180:183],a[140:143],a[116:119],v[180:183]
TR a[158:159],v213 offset:9984
M v[188:191],a[136:139],a[120:123],v[188:191]
M v[188:191],a[140:143],a[124:127],v[188:191]
W lgkmcnt(6)
M a[0:3],a[144:147],a[64:67],a[0:3]
W lgkmcnt(4)
M a[0:3],a[148:151],a[68:71],a[0:3]
M a[8:11],a[144:147],a[72:75],a[8:11]
M a[8:11],a[148:151],a[76:79],a[8:11]
M a[32:35],a[144:147],a[80:83],a[32:35]
M a[32:35],a[148:151],a[84:87],a[32:35]
M a[40:43],a[144:147],a[88:91],a[40:43]
M a[40:43],a[148:151],a[92:95],a[40:43]
M a[16:19],a[144:147],a[96:99],a[16:19]
M a[16:19],a[148:151],a[100:103],a[16:19]
M a[24:27],a[144:147],a[104:107],a[24:27]
M a[24:27],a[148:151],a[108:111],a[24:27]
M a[48:51],a[144:147],a[112:115],a[48:51]
M a[48:51],a[148:151],a[116:119],a[48:51]
M a[56:59],a[144:147],a[120:123],a[56:59]
M a[56:59],a[148:151],a[124:127],a[56:59]
W vmcnt(0) lgkmcnt(0)
s_barrier
M a[4:7],a[152:155],a[64:67],a[4:7]
M a[4:7],a[156:159],a[68:71],a[4:7]
TR a[128:129],v214
M a[12:15],a[152:155],a[72:75],a[12:15]
RD a[64:67],v210
RD a[68:71],v211
M a[12:15],a[156:159],a[76:79],a[12:15]
TR a[136:137],v215
M a[36:39],a[152:155],a[80:83],a[36:39]
RD a[72:75],v210 offset:2048
RD a[76:79],v211 offset:2048
M a[36:39],a[156:159],a[84:87],a[36:39]
TR a[130:131],v214 offset:256
M a[44:47],a[152:155],a[88:91],a[44:47]
RD a[80:83],v210 offset:4096
RD a[84:87],v211 offset:4096
M a[44:47],a[156:159],a[92:95],a[44:47]
TR a[138:139],v215 offset:256
M a[20:23],a[152:155],a[96:99],a[20:23]
RD a[88:91],v210 offset:6144
RD a[92:95],v211 offset:6144
M a[20:23],a[156:159],a[100:103],a[20:23]
TR a[132:133],v214 offset:8192
M a[28:31],a[152:155],a[104:107],a[28:31]
RD a[96:99],v210 offset:8192
RD a[100:103],v211 offset:8192
M a[28:31],a[156:159],a[108:111],a[28:31]
TR a[140:141],v215 offset:8192
M a[52:55],a[152:155],a[112:115],a[52:55]
RD a[104:107],v210 offset:10240
RD a[108:111],v211 offset:10240
M a[52:55],a[156:159],a[116:119],a[52:55]
TR a[134:135],v214 offset:8448
M a[60:63],a[152:155],a[120:123],a[60:63]
RD a[112:115],v210 offset:12288
RD a[116:119],v211 offset:12288
M a[60:63],a[156:159],a[124:127],a[60:63]
TR a[142:143],v215 offset:8448
RD a[120:123],v210 offset:14336
RD a[124:127],v211 offset:14336
W lgkmcnt(15)
M v[0:3],a[128:131],a[64:67],v[0:3]
W lgkmcnt(5)
M v[0:3],a[132:135],a[68:71],v[0:3]
TR a[144:145],v214 offset:512
M v[8:11],a[128:131],a[72:75],v[8:11]
SA m0,s62,0
M v[8:11],a[132:135],a[76:79],v[8:11]
BL v192,s[48:51],s60 offen lds
M v[32:35],a[128:131],a[80:83],v[32:35]
M v[32:35],a[132:135],a[84:87],v[32:35]
TR a[146:147],v214 offset:768
M v[40:43],a[128:131],a[88:91],v[40:43]
M v[40:43],a[132:135],a[92:95],v[40:43]
SA m0,s63,0
M v[16:19],a[128:131],a[96:99],v[16:19]
BL v200,s[52:55],s61 offen lds
M v[16:19],a[132:135],a[100:103],v[16:19]
TR a[148:149],v214 offset:8704
M v[24:27],a[128:131],a[104:107],v[24:27]
M v[24:27],a[132:135],a[108:111],v[24:27]
W lgkmcnt(7)
M v[48:51],a[128:131],a[112:115],v[48:51]
W lgkmcnt(6)
M v[48:51],a[132:135],a[116:119],v[48:51]
SA m0,s62,0x400
TR a[150:151],v214 offset:8960
W lgkmcnt(5)
M v[56:59],a[128:131],a[120:123],v[56:59]
BL v193,s[48:51],s60 offen lds
W lgkmcnt(4)
M v[56:59],a[132:135],a[124:127],v[56:59]
M v[4:7],a[136:139],a[64:67],v[4:7]
M v[4:7],a[140:143],a[68:71],v[4:7]
TR a[152:153],v215 offset:512
M v[12:15],a[136:139],a[72:75],v[12:15]
SA m0,s63,0x400
M v[12:15],a[140:143],a[76:79],v[12:15]
BL v201,s[52:55],s61 offen lds
M v[36:39],a[136:139],a[80:83],v[36:39]
M v[36:39],a[140:143],a[84:87],v[36:39]
TR a[154:155],v215 offset:768
M v[44:47],a[136:139],a[88:91],v[44:47]
M v[44:47],a[140:143],a[92:95],v[44:47]
SA m0,s62,0x800
M v[20:23],a[136:139],a[96:99],v[20:23]
BL v194,s[48:51],s60 offen lds
M v[20:23],a[140:143],a[100:103],v[20:23]
TR a[156:157],v215 offset:8704
M v[28:31],a[136:139],a[104:107],v[28:31]
M v[28:31],a[140:143],a[108:111],v[28:31]
M v[52:55],a[136:139],a[112:115],v[52:55]
M v[52:55],a[140:143],a[116:119],v[52:55]
SA m0,s63,0x800
TR a[158:159],v215 offset:8960
M v[60:63],a[136:139],a[120:123],v[60:63]
BL v202,s[52:55],s61 offen lds
M v[60:63],a[140:143],a[124:127],v[60:63]
W lgkmcnt(6)
M v[64:67],a[144:147],a[64:67],v[64:67]
W lgkmcnt(4)
M v[64:67],a[148:151],a[68:71],v[64:67]
TR a[128:129],v214 offset:1024
M v[72:75],a[144:147],a[72:75],v[72:75]
SA m0,s62,0xc00
M v[72:75],a[148:151],a[76:79],v[72:75]
BL v195,s[48:51],s60 offen lds
M v[96:99],a[144:147],a[80:83],v[96:99]
M v[96:99],a[148:151],a[84:87],v[96:99]
TR a[130:131],v214 offset:1280
M v[104:107],a[144:147],a[88:91],v[104:107]
M v[104:107],a[148:151],a[92:95],v[104:107]
SA m0,s63,0xc00
M v[80:83],a[144:147],a[96:99],v[80:83]
BL v203,s[52:55],s61 offen lds
M v[80:83],a[148:151],a[100:103],v[80:83]
TR a[132:133],v214 offset:9216
M v[88:91],a[144:147],a[104:107],v[88:91]
M v[88:91],a[148:151],a[108:111],v[88:91]
M v[112:115],a[144:147],a[112:115],v[112:115]
M v[112:115],a[148:151],a[116:119],v[112:115]
SA m0,s62,0x1000
TR a[134:135],v214 offset:9472
M v[120:123],a[144:147],a[120:123],v[120:123]
BL v196,s[48:51],s60 offen lds
M v[120:123],a[148:151],a[124:127],v[120:123]
W lgkmcnt(6)
M v[68:71],a[152:155],a[64:67],v[68:71]
W lgkmcnt(4)
M v[68:71],a[156:159],a[68:71],v[68:71]
TR a[136:137],v215 offset:1024
M v[76:79],a[152:155],a[72:75],v[76:79]
SA m0,s63,0x1000
M v[76:79],a[156:159],a[76:79],v[76:79]
BL v204,s[52:55],s61 offen lds
M v[100:103],a[152:155],a[80:83],v[100:103]
M v[100:103],a[156:159],a[84:87],v[100:103]
TR a[138:139],v215 offset:1280
M v[108:111],a[152:155],a[88:91],v[108:111]
M v[108:111],a[156:159],a[92:95],v[108:111]
SA m0,s62,0x1400
M v[84:87],a[152:155],a[96:99],v[84:87]
BL v197,s[48:51],s60 offen lds
M v[84:87],a[156:159],a[100:103],v[84:87]
TR a[140:141],v215 offset:9216
M v[92:95],a[152:155],a[104:107],v[92:95]
M v[92:95],a[156:159],a[108:111],v[92:95]
M v[116:119],a[152:155],a[112:115],v[116:119]
M v[116:119],a[156:159],a[116:119],v[116:119]
SA m0,s63,0x1400
TR a[142:143],v215 offset:9472
M v[124:127],a[152:155],a[120:123],v[124:127]
BL v205,s[52:55],s61 offen lds
M v[124:127],a[156:159],a[124:127],v[124:127]
W lgkmcnt(6)
M v[128:131],a[128:131],a[64:67],v[128:131]
W lgkmcnt(4)
M v[128:131],a[132:135],a[68:71],v[128:131]
TR a[144:145],v214 offset:1536
M v[136:139],a[128:131],a[72:75],v[136:139]
)~"
R"~(SA m0,s62,0x1800
M v[136:139],a[132:135],a[76:79],v[136:139]
BL v198,s[48:51],s60 offen lds
M v[160:163],a[128:131],a[80:83],v[160:163]
M v[160:163],a[132:135],a[84:87],v[160:163]
TR a[146:147],v214 offset:1792
M v[168:171],a[128:131],a[88:91],v[168:171]
M v[168:171],a[132:135],a[92:95],v[168:171]
SA m0,s63,0x1800
M v[144:147],a[128:131],a[96:99],v[144:147]
BL v206,s[52:55],s61 offen lds
M v[144:147],a[132:135],a[100:103],v[144:147]
TR a[148:149],v214 offset:9728
M v[152:155],a[128:131],a[104:107],v[152:155]
M v[152:155],a[132:135],a[108:111],v[152:155]
M v[176:179],a[128:131],a[112:115],v[176:179]
M v[176:179],a[132:135],a[116:119],v[176:179]
SA m0,s62,0x1c00
TR a[150:151],v214 offset:9984
M v[184:187],a[128:131],a[120:123],v[184:187]
BL v199,s[48:51],s60 offen lds
M v[184:187],a[132:135],a[124:127],v[184:187]
W lgkmcnt(6)
M v[132:135],a[136:139],a[64:67],v[132:135]
W lgkmcnt(4)
M v[132:135],a[140:143],a[68:71],v[132:135]
TR a[152:153],v215 offset:1536
M v[140:143],a[136:139],a[72:75],v[140:143]
SA m0,s63,0x1c00
M v[140:143],a[140:143],a[76:79],v[140:143]
BL v207,s[52:55],s61 offen lds
M v[164:167],a[136:139],a[80:83],v[164:167]
SA s60,s60,0x80
M v[164:167],a[140:143],a[84:87],v[164:167]
SA s61,s61,0x80000
TR a[154:155],v215 offset:1792
M v[172:175],a[136:139],a[88:91],v[172:175]
M v[172:175],a[140:143],a[92:95],v[172:175]
M v[148:151],a[136:139],a[96:99],v[148:151]
M v[148:151],a[140:143],a[100:103],v[148:151]
TR a[156:157],v215 offset:9728
M v[156:159],a[136:139],a[104:107],v[156:159]
M v[156:159],a[140:143],a[108:111],v[156:159]
M v[180:183],a[136:139],a[112:115],v[180:183]
M v[180:183],a[140:143],a[116:119],v[180:183]
TR a[158:159],v215 offset:9984
M v[188:191],a[136:139],a[120:123],v[188:191]
M v[188:191],a[140:143],a[124:127],v[188:191]
W lgkmcnt(6)
M a[0:3],a[144:147],a[64:67],a[0:3]
W lgkmcnt(4)
M a[0:3],a[148:151],a[68:71],a[0:3]
M a[8:11],a[144:147],a[72:75],a[8:11]
M a[8:11],a[148:151],a[76:79],a[8:11]
M a[32:35],a[144:147],a[80:83],a[32:35]
M a[32:35],a[148:151],a[84:87],a[32:35]
M a[40:43],a[144:147],a[88:91],a[40:43]
M a[40:43],a[148:151],a[92:95],a[40:43]
M a[16:19],a[144:147],a[96:99],a[16:19]
M a[16:19],a[148:151],a[100:103],a[16:19]
M a[24:27],a[144:147],a[104:107],a[24:27]
M a[24:27],a[148:151],a[108:111],a[24:27]
M a[48:51],a[144:147],a[112:115],a[48:51]
M a[48:51],a[148:151],a[116:119],a[48:51]
M a[56:59],a[144:147],a[120:123],a[56:59]
M a[56:59],a[148:151],a[124:127],a[56:59]
W vmcnt(0) lgkmcnt(0)
s_barrier
M a[4:7],a[152:155],a[64:67],a[4:7]
M a[4:7],a[156:159],a[68:71],a[4:7]
TR a[128:129],v212
M a[12:15],a[152:155],a[72:75],a[12:15]
RD a[64:67],v208
RD a[68:71],v209
M a[12:15],a[156:159],a[76:79],a[12:15]
TR a[136:137],v213
M a[36:39],a[152:155],a[80:83],a[36:39]
RD a[72:75],v208 offset:2048
RD a[76:79],v209 offset:2048
M a[36:39],a[156:159],a[84:87],a[36:39]
TR a[130:131],v212 offset:256
M a[44:47],a[152:155],a[88:91],a[44:47]
RD a[80:83],v208 offset:4096
RD a[84:87],v209 offset:4096
M a[44:47],a[156:159],a[92:95],a[44:47]
TR a[138:139],v213 offset:256
M a[20:23],a[152:155],a[96:99],a[20:23]
RD a[88:91],v208 offset:6144
RD a[92:95],v209 offset:6144
M a[20:23],a[156:159],a[100:103],a[20:23]
TR a[132:133],v212 offset:8192
M a[28:31],a[152:155],a[104:107],a[28:31]
RD a[96:99],v208 offset:8192
RD a[100:103],v209 offset:8192
M a[28:31],a[156:159],a[108:111],a[28:31]
TR a[140:141],v213 offset:8192
M a[52:55],a[152:155],a[112:115],a[52:55]
RD a[104:107],v208 offset:10240
RD a[108:111],v209 offset:10240
M a[52:55],a[156:159],a[116:119],a[52:55]
TR a[134:135],v212 offset:8448
M a[60:63],a[152:155],a[120:123],a[60:63]
RD a[112:115],v208 offset:12288
RD a[116:119],v209 offset:12288
M a[60:63],a[156:159],a[124:127],a[60:63]
TR a[142:143],v213 offset:8448
RD a[120:123],v208 offset:14336
RD a[124:127],v209 offset:14336
W lgkmcnt(15)
M v[0:3],a[128:131],a[64:67],v[0:3]
W lgkmcnt(5)
M v[0:3],a[132:135],a[68:71],v[0:3]
TR a[144:145],v212 offset:512
M v[8:11],a[128:131],a[72:75],v[8:11]
SA m0,s62,0x10000
M v[8:11],a[132:135],a[76:79],v[8:11]
BL v192,s[48:51],s60 offen lds
M v[32:35],a[128:131],a[80:83],v[32:35]
M v[32:35],a[132:135],a[84:87],v[32:35]
TR a[146:147],v212 offset:768
M v[40:43],a[128:131],a[88:91],v[40:43]
M v[40:43],a[132:135],a[92:95],v[40:43]
SA m0,s63,0x10000
M v[16:19],a[128:131],a[96:99],v[16:19]
BL v200,s[52:55],s61 offen lds
M v[16:19],a[132:135],a[100:103],v[16:19]
TR a[148:149],v212 offset:8704
M v[24:27],a[128:131],a[104:107],v[24:27]
M v[24:27],a[132:135],a[108:111],v[24:27]
W lgkmcnt(7)
M v[48:51],a[128:131],a[112:115],v[48:51]
W lgkmcnt(6)
M v[48:51],a[132:135],a[116:119],v[48:51]
SA m0,s62,0x10400
TR a[150:151],v212 offset:8960
W lgkmcnt(5)
M v[56:59],a[128:131],a[120:123],v[56:59]
BL v193,s[48:51],s60 offen lds
W lgkmcnt(4)
M v[56:59],a[132:135],a[124:127],v[56:59]
M v[4:7],a[136:139],a[64:67],v[4:7]
M v[4:7],a[140:143],a[68:71],v[4:7]
TR a[152:153],v213 offset:512
M v[12:15],a[136:139],a[72:75],v[12:15]
SA m0,s63,0x10400
M v[12:15],a[140:143],a[76:79],v[12:15]
BL v201,s[52:55],s61 offen lds
M v[36:39],a[136:139],a[80:83],v[36:39]
M v[36:39],a[140:143],a[84:87],v[36:39]
TR a[154:155],v213 offset:768
M v[44:47],a[136:139],a[88:91],v[44:47]
M v[44:47],a[140:143],a[92:95],v[44:47]
SA m0,s62,0x10800
M v[20:23],a[136:139],a[96:99],v[20:23]
BL v194,s[48:51],s60 offen lds
M v[20:23],a[140:143],a[100:103],v[20:23]
TR a[156:157],v213 offset:8704
M v[28:31],a[136:139],a[104:107],v[28:31]
M v[28:31],a[140:143],a[108:111],v[28:31]
M v[52:55],a[136:139],a[112:115],v[52:55]
M v[52:55],a[140:143],a[116:119],v[52:55]
SA m0,s63,0x10800
TR a[158:159],v213 offset:8960
M v[60:63],a[136:139],a[120:123],v[60:63]
BL v202,s[52:55],s61 offen lds
M v[60:63],a[140:143],a[124:127],v[60:63]
W lgkmcnt(6)
M v[64:67],a[144:147],a[64:67],v[64:67]
W lgkmcnt(4)
M v[64:67],a[148:151],a[68:71],v[64:67]
TR a[128:129],v212 offset:1024
M v[72:75],a[144:147],a[72:75],v[72:75]
SA m0,s62,0x10c00
M v[72:75],a[148:151],a[76:79],v[72:75]
BL v195,s[48:51],s60 offen lds
M v[96:99],a[144:147],a[80:83],v[96:99]
M v[96:99],a[148:151],a[84:87],v[96:99]
TR a[130:131],v212 offset:1280
M v[104:107],a[144:147],a[88:91],v[104:107]
M v[104:107],a[148:151],a[92:95],v[104:107]
SA m0,s63,0x10c00
M v[80:83],a[144:147],a[96:99],v[80:83]
BL v203,s[52:55],s61 offen lds
M v[80:83],a[148:151],a[100:103],v[80:83]
TR a[132:133],v212 offset:9216
M v[88:91],a[144:147],a[104:107],v[88:91]
M v[88:91],a[148:151],a[108:111],v[88:91]
M v[112:115],a[144:147],a[112:115],v[112:115]
M v[112:115],a[148:151],a[116:119],v[112:115]
SA m0,s62,0x11000
TR a[134:135],v212 offset:9472
M v[120:123],a[144:147],a[120:123],v[120:123]
BL v196,s[48:51],s60 offen lds
M v[120:123],a[148:151],a[124:127],v[120:123]
W lgkmcnt(6)
M v[68:71],a[152:155],a[64:67],v[68:71]
W lgkmcnt(4)
M v[68:71],a[156:159],a[68:71],v[68:71]
TR a[136:137],v213 offset:1024
M v[76:79],a[152:155],a[72:75],v[76:79]
SA m0,s63,0x11000
M v[76:79],a[156:159],a[76:79],v[76:79]
BL v204,s[52:55],s61 offen lds
M v[100:103],a[152:155],a[80:83],v[100:103]
M v[100:103],a[156:159],a[84:87],v[100:103]
TR a[138:139],v213 offset:1280
M v[108:111],a[152:155],a[88:91],v[108:111]
M v[108:111],a[156:159],a[92:95],v[108:111]
SA m0,s62,0x11400
M v[84:87],a[152:155],a[96:99],v[84:87]
BL v197,s[48:51],s60 offen lds
M v[84:87],a[156:159],a[100:103],v[84:87]
TR a[140:141],v213 offset:9216
M v[92:95],a[152:155],a[104:107],v[92:95]
M v[92:95],a[156:159],a[108:111],v[92:95]
M v[116:119],a[152:155],a[112:115],v[116:119]
M v[116:119],a[156:159],a[116:119],v[116:119]
SA m0,s63,0x11400
TR a[142:143],v213 offset:9472
M v[124:127],a[152:155],a[120:123],v[124:127]
BL v205,s[52:55],s61 offen lds
M v[124:127],a[156:159],a[124:127],v[124:127]
W lgkmcnt(6)
M v[128:131],a[128:131],a[64:67],v[128:131]
W lgkmcnt(4)
M v[128:131],a[132:135],a[68:71],v[128:131]
TR a[144:145],v212 offset:1536
M v[136:139],a[128:131],a[72:75],v[136:139]
SA m0,s62,0x11800
M v[136:139],a[132:135],a[76:79],v[136:139]
BL v198,s[48:51],s60 offen lds
M v[160:163],a[128:131],a[80:83],v[160:163]
M v[160:163],a[132:135],a[84:87],v[160:163]
TR a[146:147],v212 offset:1792
M v[168:171],a[128:131],a[88:91],v[168:171]
M v[168:171],a[132:135],a[92:95],v[168:171]
SA m0,s63,0x11800
M v[144:147],a[128:131],a[96:99],v[144:147]
BL v206,s[52:55],s61 offen lds
M v[144:147],a[132:135],a[100:103],v[144:147]
TR a[148:149],v212 offset:9728
M v[152:155],a[128:131],a[104:107],v[152:155]
M v[152:155],a[132:135],a[108:111],v[152:155]
M v[176:179],a[128:131],a[112:115],v[176:179]
M v[176:179],a[132:135],a[116:119],v[176:179]
SA m0,s62,0x11c00
TR a[150:151],v212 offset:9984
M v[184:187],a[128:131],a[120:123],v[184:187]
BL v199,s[48:51],s60 offen lds
M v[184:187],a[132:135],a[124:127],v[184:187]
W lgkmcnt(6)
M v[132:135],a[136:139],a[64:67],v[132:135]
W lgkmcnt(4)
M v[132:135],a[140:143],a[68:71],v[132:135]
TR a[152:153],v213 offset:1536
M v[140:143],a[136:139],a[72:75],v[140:143]
SA m0,s63,0x11c00
M v[140:143],a[140:143],a[76:79],v[140:143]
BL v207,s[52:55],s61 offen lds
M v[164:167],a[136:139],a[80:83],v[164:167]
SA s60,s60,0x80
M v[164:167],a[140:143],a[84:87],v[164:167]
SA s61,s61,0x80000
TR a[154:155],v213 offset:1792
M v[172:175],a[136:139],a[88:91],v[172:175]
M v[172:175],a[140:143],a[92:95],v[172:175]
M v[148:151],a[136:139],a[96:99],v[148:151]
M v[148:151],a[140:143],a[100:103],v[148:151]
TR a[156:157],v213 offset:9728
M v[156:159],a[136:139],a[104:107],v[156:159]
M v[156:159],a[140:143],a[108:111],v[156:159]
M v[180:183],a[136:139],a[112:115],v[180:183]
M v[180:183],a[140:143],a[116:119],v[180:183]
TR a[158:159],v213 offset:9984
M v[188:191],a[136:139],a[120:123],v[188:191]
M v[188:191],a[140:143],a[124:127],v[188:191]
W lgkmcnt(6)
M a[0:3],a[144:147],a[64:67],a[0:3]
W lgkmcnt(4)
M a[0:3],a[148:151],a[68:71],a[0:3]
M a[8:11],a[144:147],a[72:75],a[8:11]
M a[8:11],a[148:151],a[76:79],a[8:11]
M a[32:35],a[144:147],a[80:83],a[32:35]
M a[32:35],a[148:151],a[84:87],a[32:35]
M a[40:43],a[144:147],a[88:91],a[40:43]
M a[40:43],a[148:151],a[92:95],a[40:43]
M a[16:19],a[144:147],a[96:99],a[16:19]
M a[16:19],a[148:151],a[100:103],a[16:19]
M a[24:27],a[144:147],a[104:107],a[24:27]
M a[24:27],a[148:151],a[108:111],a[24:27]
M a[48:51],a[144:147],a[112:115],a[48:51]
M a[48:51],a[148:151],a[116:119],a[48:51]
M a[56:59],a[144:147],a[120:123],a[56:59]
M a[56:59],a[148:151],a[124:127],a[56:59]
W vmcnt(0) lgkmcnt(0)
s_barrier
M a[4:7],a[152:155],a[64:67],a[4:7]
M a[4:7],a[156:159],a[68:71],a[4:7]
TR a[128:129],v214
M a[12:15],a[152:155],a[72:75],a[12:15]
RD a[64:67],v210
RD a[68:71],v211
M a[12:15],a[156:159],a[76:79],a[12:15]
TR a[136:137],v215
M a[36:39],a[152:155],a[80:83],a[36:39]
RD a[72:75],v210 offset:2048
RD a[76:79],v211 offset:2048
M a[36:39],a[156:159],a[84:87],a[36:39]
TR a[130:131],v214 offset:256
M a[44:47],a[152:155],a[88:91],a[44:47]
RD a[80:83],v210 offset:4096
RD a[84:87],v211 offset:4096
M a[44:47],a[156:159],a[92:95],a[44:47]
TR a[138:139],v215 offset:256
M a[20:23],a[152:155],a[96:99],a[20:23]
RD a[88:91],v210 offset:6144
RD a[92:95],v211 offset:6144
M a[20:23],a[156:159],a[100:103],a[20:23]
TR a[132:133],v214 offset:8192
M a[28:31],a[152:155],a[104:107],a[28:31]
RD a[96:99],v210 offset:8192
RD a[100:103],v211 offset:8192
M a[28:31],a[156:159],a[108:111],a[28:31]
TR a[140:141],v215 offset:8192
M a[52:55],a[152:155],a[112:115],a[52:55]
RD a[104:107],v210 offset:10240
RD a[108:111],v211 offset:10240
M a[52:55],a[156:159],a[116:119],a[52:55]
TR a[134:135],v214 offset:8448
M a[60:63],a[152:155],a[120:123],a[60:63]
RD a[112:115],v210 offset:12288
RD a[116:119],v211 offset:12288
M a[60:63],a[156:159],a[124:127],a[60:63]
TR a[142:143],v215 offset:8448
RD a[120:123],v210 offset:14336
RD a[124:127],v211 offset:14336
W lgkmcnt(15)
M v[0:3],a[128:131],a[64:67],v[0:3]
W lgkmcnt(5)
M v[0:3],a[132:135],a[68:71],v[0:3]
TR a[144:145],v214 offset:512
M v[8:11],a[128:131],a[72:75],v[8:11]
SA m0,s62,0
M v[8:11],a[132:135],a[76:79],v[8:11]
BL v192,s[48:51],s60 offen lds
M v[32:35],a[128:131],a[80:83],v[32:35]
M v[32:35],a[132:135],a[84:87],v[32:35]
TR a[146:147],v214 offset:768
M v[40:43],a[128:131],a[88:91],v[40:43]
M v[40:43],a[132:135],a[92:95],v[40:43]
SA m0,s63,0
M v[16:19],a[128:131],a[96:99],v[16:19]
BL v200,s[52:55],s61 offen lds
M v[16:19],a[132:135],a[100:103],v[16:19]
TR a[148:149],v214 offset:8704
M v[24:27],a[128:131],a[104:107],v[24:27]
M v[24:27],a[132:135],a[108:111],v[24:27]
W lgkmcnt(7)
M v[48:51],a[128:131],a[112:115],v[48:51]
W lgkmcnt(6)
M v[48:51],a[132:135],a[116:119],v[48:51]
SA m0,s62,0x400
TR a[150:151],v214 offset:8960
W lgkmcnt(5)
M v[56:59],a[128:131],a[120:123],v[56:59]
BL v193,s[48:51],s60 offen lds
W lgkmcnt(4)
M v[56:59],a[132:135],a[124:127],v[56:59]
M v[4:7],a[136:139],a[64:67],v[4:7]
M v[4:7],a[140:143],a[68:71],v[4:7]
TR a[152:153],v215 offset:512
M v[12:15],a[136:139],a[72:75],v[12:15]
SA m0,s63,0x400
M v[12:15],a[140:143],a[76:79],v[12:15]
BL v201,s[52:55],s61 offen lds
M v[36:39],a[136:139],a[80:83],v[36:39]
M v[36:39],a[140:143],a[84:87],v[36:39]
TR a[154:155],v215 offset:768
M v[44:47],a[136:139],a[88:91],v[44:47]
M v[44:47],a[140:143],a[92:95],v[44:47]
SA m0,s62,0x800
M v[20:23],a[136:139],a[96:99],v[20:23]
BL v194,s[48:51],s60 offen lds
M v[20:23],a[140:143],a[100:103],v[20:23]
TR a[156:157],v215 offset:8704
M v[28:31],a[136:139],a[104:107],v[28:31]
M v[28:31],a[140:143],a[108:111],v[28:31]
M v[52:55],a[136:139],a[112:115],v[52:55]
M v[52:55],a[140:143],a[116:119],v[52:55]
SA m0,s63,0x800
TR a[158:159],v215 offset:8960
M v[60:63],a[136:139],a[120:123],v[60:63]
BL v202,s[52:55],s61 offen lds
M v[60:63],a[140:143],a[124:127],v[60:63]
W lgkmcnt(6)
M v[64:67],a[144:147],a[64:67],v[64:67]
W lgkmcnt(4)
M v[64:67],a[148:151],a[68:71],v[64:67]
TR a[128:129],v214 offset:1024
M v[72:75],a[144:147],a[72:75],v[72:75]
SA m0,s62,0xc00
M v[72:75],a[148:151],a[76:79],v[72:75]
BL v195,s[48:51],s60 offen lds
M v[96:99],a[144:147],a[80:83],v[96:99]
M v[96:99],a[148:151],a[84:87],v[96:99]
TR a[130:131],v214 offset:1280
M v[104:107],a[144:147],a[88:91],v[104:107]
M v[104:107],a[148:151],a[92:95],v[104:107]
)~"
R"~(SA m0,s63,0xc00
M v[80:83],a[144:147],a[96:99],v[80:83]
BL v203,s[52:55],s61 offen lds
M v[80:83],a[148:151],a[100:103],v[80:83]
TR a[132:133],v214 offset:9216
M v[88:91],a[144:147],a[104:107],v[88:91]
M v[88:91],a[148:151],a[108:111],v[88:91]
M v[112:115],a[144:147],a[112:115],v[112:115]
M v[112:115],a[148:151],a[116:119],v[112:115]
SA m0,s62,0x1000
TR a[134:135],v214 offset:9472
M v[120:123],a[144:147],a[120:123],v[120:123]
BL v196,s[48:51],s60 offen lds
M v[120:123],a[148:151],a[124:127],v[120:123]
W lgkmcnt(6)
M v[68:71],a[152:155],a[64:67],v[68:71]
W lgkmcnt(4)
M v[68:71],a[156:159],a[68:71],v[68:71]
TR a[136:137],v215 offset:1024
M v[76:79],a[152:155],a[72:75],v[76:79]
SA m0,s63,0x1000
M v[76:79],a[156:159],a[76:79],v[76:79]
BL v204,s[52:55],s61 offen lds
M v[100:103],a[152:155],a[80:83],v[100:103]
M v[100:103],a[156:159],a[84:87],v[100:103]
TR a[138:139],v215 offset:1280
M v[108:111],a[152:155],a[88:91],v[108:111]
M v[108:111],a[156:159],a[92:95],v[108:111]
SA m0,s62,0x1400
M v[84:87],a[152:155],a[96:99],v[84:87]
BL v197,s[48:51],s60 offen lds
M v[84:87],a[156:159],a[100:103],v[84:87]
TR a[140:141],v215 offset:9216
M v[92:95],a[152:155],a[104:107],v[92:95]
M v[92:95],a[156:159],a[108:111],v[92:95]
M v[116:119],a[152:155],a[112:115],v[116:119]
M v[116:119],a[156:159],a[116:119],v[116:119]
SA m0,s63,0x1400
TR a[142:143],v215 offset:9472
M v[124:127],a[152:155],a[120:123],v[124:127]
BL v205,s[52:55],s61 offen lds
M v[124:127],a[156:159],a[124:127],v[124:127]
W lgkmcnt(6)
M v[128:131],a[128:131],a[64:67],v[128:131]
W lgkmcnt(4)
M v[128:131],a[132:135],a[68:71],v[128:131]
TR a[144:145],v214 offset:1536
M v[136:139],a[128:131],a[72:75],v[136:139]
SA m0,s62,0x1800
M v[136:139],a[132:135],a[76:79],v[136:139]
BL v198,s[48:51],s60 offen lds
M v[160:163],a[128:131],a[80:83],v[160:163]
M v[160:163],a[132:135],a[84:87],v[160:163]
TR a[146:147],v214 offset:1792
M v[168:171],a[128:131],a[88:91],v[168:171]
M v[168:171],a[132:135],a[92:95],v[168:171]
SA m0,s63,0x1800
M v[144:147],a[128:131],a[96:99],v[144:147]
BL v206,s[52:55],s61 offen lds
M v[144:147],a[132:135],a[100:103],v[144:147]
TR a[148:149],v214 offset:9728
M v[152:155],a[128:131],a[104:107],v[152:155]
M v[152:155],a[132:135],a[108:111],v[152:155]
M v[176:179],a[128:131],a[112:115],v[176:179]
M v[176:179],a[132:135],a[116:119],v[176:179]
SA m0,s62,0x1c00
TR a[150:151],v214 offset:9984
M v[184:187],a[128:131],a[120:123],v[184:187]
BL v199,s[48:51],s60 offen lds
M v[184:187],a[132:135],a[124:127],v[184:187]
W lgkmcnt(6)
M v[132:135],a[136:139],a[64:67],v[132:135]
W lgkmcnt(4)
M v[132:135],a[140:143],a[68:71],v[132:135]
TR a[152:153],v215 offset:1536
M v[140:143],a[136:139],a[72:75],v[140:143]
SA m0,s63,0x1c00
M v[140:143],a[140:143],a[76:79],v[140:143]
BL v207,s[52:55],s61 offen lds
M v[164:167],a[136:139],a[80:83],v[164:167]
SA s60,s60,0x80
M v[164:167],a[140:143],a[84:87],v[164:167]
SA s61,s61,0x80000
TR a[154:155],v215 offset:1792
M v[172:175],a[136:139],a[88:91],v[172:175]
M v[172:175],a[140:143],a[92:95],v[172:175]
M v[148:151],a[136:139],a[96:99],v[148:151]
M v[148:151],a[140:143],a[100:103],v[148:151]
TR a[156:157],v215 offset:9728
M v[156:159],a[136:139],a[104:107],v[156:159]
M v[156:159],a[140:143],a[108:111],v[156:159]
M v[180:183],a[136:139],a[112:115],v[180:183]
M v[180:183],a[140:143],a[116:119],v[180:183]
TR a[158:159],v215 offset:9984
M v[188:191],a[136:139],a[120:123],v[188:191]
M v[188:191],a[140:143],a[124:127],v[188:191]
W lgkmcnt(6)
M a[0:3],a[144:147],a[64:67],a[0:3]
W lgkmcnt(4)
M a[0:3],a[148:151],a[68:71],a[0:3]
M a[8:11],a[144:147],a[72:75],a[8:11]
M a[8:11],a[148:151],a[76:79],a[8:11]
M a[32:35],a[144:147],a[80:83],a[32:35]
M a[32:35],a[148:151],a[84:87],a[32:35]
M a[40:43],a[144:147],a[88:91],a[40:43]
M a[40:43],a[148:151],a[92:95],a[40:43]
M a[16:19],a[144:147],a[96:99],a[16:19]
M a[16:19],a[148:151],a[100:103],a[16:19]
M a[24:27],a[144:147],a[104:107],a[24:27]
M a[24:27],a[148:151],a[108:111],a[24:27]
M a[48:51],a[144:147],a[112:115],a[48:51]
M a[48:51],a[148:151],a[116:119],a[48:51]
M a[56:59],a[144:147],a[120:123],a[56:59]
M a[56:59],a[148:151],a[124:127],a[56:59]
W vmcnt(0) lgkmcnt(0)
s_barrier
M a[4:7],a[152:155],a[64:67],a[4:7]
M a[4:7],a[156:159],a[68:71],a[4:7]
TR a[128:129],v212
M a[12:15],a[152:155],a[72:75],a[12:15]
RD a[64:67],v208
RD a[68:71],v209
M a[12:15],a[156:159],a[76:79],a[12:15]
TR a[136:137],v213
M a[36:39],a[152:155],a[80:83],a[36:39]
RD a[72:75],v208 offset:2048
RD a[76:79],v209 offset:2048
M a[36:39],a[156:159],a[84:87],a[36:39]
TR a[130:131],v212 offset:256
M a[44:47],a[152:155],a[88:91],a[44:47]
RD a[80:83],v208 offset:4096
RD a[84:87],v209 offset:4096
M a[44:47],a[156:159],a[92:95],a[44:47]
TR a[138:139],v213 offset:256
M a[20:23],a[152:155],a[96:99],a[20:23]
RD a[88:91],v208 offset:6144
RD a[92:95],v209 offset:6144
M a[20:23],a[156:159],a[100:103],a[20:23]
TR a[132:133],v212 offset:8192
M a[28:31],a[152:155],a[104:107],a[28:31]
RD a[96:99],v208 offset:8192
RD a[100:103],v209 offset:8192
M a[28:31],a[156:159],a[108:111],a[28:31]
TR a[140:141],v213 offset:8192
M a[52:55],a[152:155],a[112:115],a[52:55]
RD a[104:107],v208 offset:10240
RD a[108:111],v209 offset:10240
M a[52:55],a[156:159],a[116:119],a[52:55]
TR a[134:135],v212 offset:8448
M a[60:63],a[152:155],a[120:123],a[60:63]
RD a[112:115],v208 offset:12288
RD a[116:119],v209 offset:12288
M a[60:63],a[156:159],a[124:127],a[60:63]
TR a[142:143],v213 offset:8448
RD a[120:123],v208 offset:14336
RD a[124:127],v209 offset:14336
W lgkmcnt(15)
M v[0:3],a[128:131],a[64:67],v[0:3]
W lgkmcnt(5)
M v[0:3],a[132:135],a[68:71],v[0:3]
TR a[144:145],v212 offset:512
M v[8:11],a[128:131],a[72:75],v[8:11]
SA m0,s62,0x10000
M v[8:11],a[132:135],a[76:79],v[8:11]
BL v192,s[48:51],s60 offen lds
M v[32:35],a[128:131],a[80:83],v[32:35]
M v[32:35],a[132:135],a[84:87],v[32:35]
TR a[146:147],v212 offset:768
M v[40:43],a[128:131],a[88:91],v[40:43]
M v[40:43],a[132:135],a[92:95],v[40:43]
SA m0,s63,0x10000
M v[16:19],a[128:131],a[96:99],v[16:19]
BL v200,s[52:55],s61 offen lds
M v[16:19],a[132:135],a[100:103],v[16:19]
TR a[148:149],v212 offset:8704
M v[24:27],a[128:131],a[104:107],v[24:27]
M v[24:27],a[132:135],a[108:111],v[24:27]
W lgkmcnt(7)
M v[48:51],a[128:131],a[112:115],v[48:51]
W lgkmcnt(6)
M v[48:51],a[132:135],a[116:119],v[48:51]
SA m0,s62,0x10400
TR a[150:151],v212 offset:8960
W lgkmcnt(5)
M v[56:59],a[128:131],a[120:123],v[56:59]
BL v193,s[48:51],s60 offen lds
W lgkmcnt(4)
M v[56:59],a[132:135],a[124:127],v[56:59]
M v[4:7],a[136:139],a[64:67],v[4:7]
M v[4:7],a[140:143],a[68:71],v[4:7]
TR a[152:153],v213 offset:512
M v[12:15],a[136:139],a[72:75],v[12:15]
SA m0,s63,0x10400
M v[12:15],a[140:143],a[76:79],v[12:15]
BL v201,s[52:55],s61 offen lds
M v[36:39],a[136:139],a[80:83],v[36:39]
M v[36:39],a[140:143],a[84:87],v[36:39]
TR a[154:155],v213 offset:768
M v[44:47],a[136:139],a[88:91],v[44:47]
M v[44:47],a[140:143],a[92:95],v[44:47]
SA m0,s62,0x10800
M v[20:23],a[136:139],a[96:99],v[20:23]
BL v194,s[48:51],s60 offen lds
M v[20:23],a[140:143],a[100:103],v[20:23]
TR a[156:157],v213 offset:8704
M v[28:31],a[136:139],a[104:107],v[28:31]
M v[28:31],a[140:143],a[108:111],v[28:31]
M v[52:55],a[136:139],a[112:115],v[52:55]
M v[52:55],a[140:143],a[116:119],v[52:55]
SA m0,s63,0x10800
TR a[158:159],v213 offset:8960
M v[60:63],a[136:139],a[120:123],v[60:63]
BL v202,s[52:55],s61 offen lds
M v[60:63],a[140:143],a[124:127],v[60:63]
W lgkmcnt(6)
M v[64:67],a[144:147],a[64:67],v[64:67]
W lgkmcnt(4)
M v[64:67],a[148:151],a[68:71],v[64:67]
TR a[128:129],v212 offset:1024
M v[72:75],a[144:147],a[72:75],v[72:75]
SA m0,s62,0x10c00
M v[72:75],a[148:151],a[76:79],v[72:75]
BL v195,s[48:51],s60 offen lds
M v[96:99],a[144:147],a[80:83],v[96:99]
M v[96:99],a[148:151],a[84:87],v[96:99]
TR a[130:131],v212 offset:1280
M v[104:107],a[144:147],a[88:91],v[104:107]
M v[104:107],a[148:151],a[92:95],v[104:107]
SA m0,s63,0x10c00
M v[80:83],a[144:147],a[96:99],v[80:83]
BL v203,s[52:55],s61 offen lds
M v[80:83],a[148:151],a[100:103],v[80:83]
TR a[132:133],v212 offset:9216
M v[88:91],a[144:147],a[104:107],v[88:91]
M v[88:91],a[148:151],a[108:111],v[88:91]
M v[112:115],a[144:147],a[112:115],v[112:115]
M v[112:115],a[148:151],a[116:119],v[112:115]
SA m0,s62,0x11000
TR a[134:135],v212 offset:9472
M v[120:123],a[144:147],a[120:123],v[120:123]
BL v196,s[48:51],s60 offen lds
M v[120:123],a[148:151],a[124:127],v[120:123]
W lgkmcnt(6)
M v[68:71],a[152:155],a[64:67],v[68:71]
W lgkmcnt(4)
M v[68:71],a[156:159],a[68:71],v[68:71]
TR a[136:137],v213 offset:1024
M v[76:79],a[152:155],a[72:75],v[76:79]
SA m0,s63,0x11000
M v[76:79],a[156:159],a[76:79],v[76:79]
BL v204,s[52:55],s61 offen lds
M v[100:103],a[152:155],a[80:83],v[100:103]
M v[100:103],a[156:159],a[84:87],v[100:103]
TR a[138:139],v213 offset:1280
M v[108:111],a[152:155],a[88:91],v[108:111]
M v[108:111],a[156:159],a[92:95],v[108:111]
SA m0,s62,0x11400
M v[84:87],a[152:155],a[96:99],v[84:87]
BL v197,s[48:51],s60 offen lds
M v[84:87],a[156:159],a[100:103],v[84:87]
TR a[140:141],v213 offset:9216
M v[92:95],a[152:155],a[104:107],v[92:95]
M v[92:95],a[156:159],a[108:111],v[92:95]
M v[116:119],a[152:155],a[112:115],v[116:119]
M v[116:119],a[156:159],a[116:119],v[116:119]
SA m0,s63,0x11400
TR a[142:143],v213 offset:9472
M v[124:127],a[152:155],a[120:123],v[124:127]
BL v205,s[52:55],s61 offen lds
M v[124:127],a[156:159],a[124:127],v[124:127]
W lgkmcnt(6)
M v[128:131],a[128:131],a[64:67],v[128:131]
W lgkmcnt(4)
M v[128:131],a[132:135],a[68:71],v[128:131]
TR a[144:145],v212 offset:1536
M v[136:139],a[128:131],a[72:75],v[136:139]
SA m0,s62,0x11800
M v[136:139],a[132:135],a[76:79],v[136:139]
BL v198,s[48:51],s60 offen lds
M v[160:163],a[128:131],a[80:83],v[160:163]
M v[160:163],a[132:135],a[84:87],v[160:163]
TR a[146:147],v212 offset:1792
M v[168:171],a[128:131],a[88:91],v[168:171]
M v[168:171],a[132:135],a[92:95],v[168:171]
SA m0,s63,0x11800
M v[144:147],a[128:131],a[96:99],v[144:147]
BL v206,s[52:55],s61 offen lds
M v[144:147],a[132:135],a[100:103],v[144:147]
TR a[148:149],v212 offset:9728
M v[152:155],a[128:131],a[104:107],v[152:155]
M v[152:155],a[132:135],a[108:111],v[152:155]
M v[176:179],a[128:131],a[112:115],v[176:179]
M v[176:179],a[132:135],a[116:119],v[176:179]
SA m0,s62,0x11c00
TR a[150:151],v212 offset:9984
M v[184:187],a[128:131],a[120:123],v[184:187]
BL v199,s[48:51],s60 offen lds
M v[184:187],a[132:135],a[124:127],v[184:187]
W lgkmcnt(6)
M v[132:135],a[136:139],a[64:67],v[132:135]
W lgkmcnt(4)
M v[132:135],a[140:143],a[68:71],v[132:135]
TR a[152:153],v213 offset:1536
M v[140:143],a[136:139],a[72:75],v[140:143]
SA m0,s63,0x11c00
M v[140:143],a[140:143],a[76:79],v[140:143]
BL v207,s[52:55],s61 offen lds
M v[164:167],a[136:139],a[80:83],v[164:167]
SA s60,s60,0x80
M v[164:167],a[140:143],a[84:87],v[164:167]
s_lshl_b32 s90,s37,2
SA s90,s90,s36
s_mov_b64 s[56:57],s[8:9]
s_and_b32 s57,s57,0xffff
s_mov_b32 s58,0x200000
s_mov_b32 s59,0x20000
s_mul_i32 s93,s90,0x800
v_min_u32 v241,15,v255
v_lshlrev_b32 v241,7,v241
SA s61,s61,0x80000
TR a[154:155],v213 offset:1792
M v[172:175],a[136:139],a[88:91],v[172:175]
buffer_load_dword v242,v241,s[56:59],s93 offen
M v[172:175],a[140:143],a[92:95],v[172:175]
M v[148:151],a[136:139],a[96:99],v[148:151]
M v[148:151],a[140:143],a[100:103],v[148:151]
s_mov_b64 s[56:57],s[10:11]
s_and_b32 s57,s57,0xffff
s_mov_b32 s58,0x180000
s_mov_b32 s59,0x20000
s_mul_i32 s93,s90,0x600
v_min_u32 v241,11,v255
v_lshlrev_b32 v241,7,v241
TR a[156:157],v213 offset:9728
M v[156:159],a[136:139],a[104:107],v[156:159]
buffer_load_dword v243,v241,s[56:59],s93 offen
M v[156:159],a[140:143],a[108:111],v[156:159]
M v[180:183],a[136:139],a[112:115],v[180:183]
M v[180:183],a[140:143],a[116:119],v[180:183]
s_mov_b64 s[56:57],s[16:17]
s_and_b32 s57,s57,0xffff
s_mov_b32 s58,0x400000
s_mov_b32 s59,0x20000
s_mul_i32 s93,s90,0x1000
v_min_u32 v241,31,v255
v_lshlrev_b32 v241,7,v241
TR a[158:159],v213 offset:9984
M v[188:191],a[136:139],a[120:123],v[188:191]
buffer_load_dword v237,v241,s[56:59],s93 offen
M v[188:191],a[140:143],a[124:127],v[188:191]
W lgkmcnt(6)
M a[0:3],a[144:147],a[64:67],a[0:3]
W lgkmcnt(4)
M a[0:3],a[148:151],a[68:71],a[0:3]
s_mov_b64 s[56:57],s[22:23]
s_and_b32 s57,s57,0xffff
s_mov_b32 s58,0x400000
s_mov_b32 s59,0x20000
s_mul_i32 s93,s90,0x1000
v_min_u32 v241,31,v255
v_lshlrev_b32 v241,7,v241
M a[8:11],a[144:147],a[72:75],a[8:11]
buffer_load_dword v248,v241,s[56:59],s93 offen
M a[8:11],a[148:151],a[76:79],a[8:11]
M a[32:35],a[144:147],a[80:83],a[32:35]
M a[32:35],a[148:151],a[84:87],a[32:35]
s_mov_b64 s[56:57],s[12:13]
s_and_b32 s57,s57,0xffff
s_mov_b32 s58,0x80000
s_mov_b32 s59,0x20000
s_mul_i32 s93,s90,0x200
v_min_u32 v241,3,v255
v_lshlrev_b32 v241,7,v241
M a[40:43],a[144:147],a[88:91],a[40:43]
buffer_load_dword v249,v241,s[56:59],s93 offen
M a[40:43],a[148:151],a[92:95],a[40:43]
M a[16:19],a[144:147],a[96:99],a[16:19]
M a[16:19],a[148:151],a[100:103],a[16:19]
M a[24:27],a[144:147],a[104:107],a[24:27]
M a[24:27],a[148:151],a[108:111],a[24:27]
M a[48:51],a[144:147],a[112:115],a[48:51]
M a[48:51],a[148:151],a[116:119],a[48:51]
M a[56:59],a[144:147],a[120:123],a[56:59]
M a[56:59],a[148:151],a[124:127],a[56:59]
W vmcnt(5) lgkmcnt(0)
s_barrier
M a[4:7],a[152:155],a[64:67],a[4:7]
M a[4:7],a[156:159],a[68:71],a[4:7]
TR a[128:129],v214
M a[12:15],a[152:155],a[72:75],a[12:15]
RD a[64:67],v210
RD a[68:71],v211
M a[12:15],a[156:159],a[76:79],a[12:15]
TR a[136:137],v215
M a[36:39],a[152:155],a[80:83],a[36:39]
RD a[72:75],v210 offset:2048
RD a[76:79],v211 offset:2048
M a[36:39],a[156:159],a[84:87],a[36:39]
TR a[130:131],v214 offset:256
M a[44:47],a[152:155],a[88:91],a[44:47]
RD a[80:83],v210 offset:4096
RD a[84:87],v211 offset:4096
M a[44:47],a[156:159],a[92:95],a[44:47]
TR a[138:139],v215 offset:256
M a[20:23],a[152:155],a[96:99],a[20:23]
RD a[88:91],v210 offset:6144
RD a[92:95],v211 offset:6144
M a[20:23],a[156:159],a[100:103],a[20:23]
TR a[132:133],v214 offset:8192
M a[28:31],a[152:155],a[104:107],a[28:31]
RD a[96:99],v210 offset:8192
RD a[100:103],v211 offset:8192
M a[28:31],a[156:159],a[108:111],a[28:31]
)~"
R"~(TR a[140:141],v215 offset:8192
M a[52:55],a[152:155],a[112:115],a[52:55]
RD a[104:107],v210 offset:10240
RD a[108:111],v211 offset:10240
M a[52:55],a[156:159],a[116:119],a[52:55]
TR a[134:135],v214 offset:8448
M a[60:63],a[152:155],a[120:123],a[60:63]
RD a[112:115],v210 offset:12288
RD a[116:119],v211 offset:12288
M a[60:63],a[156:159],a[124:127],a[60:63]
TR a[142:143],v215 offset:8448
RD a[120:123],v210 offset:14336
RD a[124:127],v211 offset:14336
W lgkmcnt(15)
M v[0:3],a[128:131],a[64:67],v[0:3]
W lgkmcnt(5)
M v[0:3],a[132:135],a[68:71],v[0:3]
TR a[144:145],v214 offset:512
M v[8:11],a[128:131],a[72:75],v[8:11]
M v[8:11],a[132:135],a[76:79],v[8:11]
M v[32:35],a[128:131],a[80:83],v[32:35]
M v[32:35],a[132:135],a[84:87],v[32:35]
TR a[146:147],v214 offset:768
M v[40:43],a[128:131],a[88:91],v[40:43]
M v[40:43],a[132:135],a[92:95],v[40:43]
M v[16:19],a[128:131],a[96:99],v[16:19]
M v[16:19],a[132:135],a[100:103],v[16:19]
TR a[148:149],v214 offset:8704
M v[24:27],a[128:131],a[104:107],v[24:27]
M v[24:27],a[132:135],a[108:111],v[24:27]
W lgkmcnt(7)
M v[48:51],a[128:131],a[112:115],v[48:51]
W lgkmcnt(6)
M v[48:51],a[132:135],a[116:119],v[48:51]
TR a[150:151],v214 offset:8960
W lgkmcnt(5)
M v[56:59],a[128:131],a[120:123],v[56:59]
W lgkmcnt(4)
M v[56:59],a[132:135],a[124:127],v[56:59]
M v[4:7],a[136:139],a[64:67],v[4:7]
M v[4:7],a[140:143],a[68:71],v[4:7]
TR a[152:153],v215 offset:512
M v[12:15],a[136:139],a[72:75],v[12:15]
M v[12:15],a[140:143],a[76:79],v[12:15]
M v[36:39],a[136:139],a[80:83],v[36:39]
M v[36:39],a[140:143],a[84:87],v[36:39]
TR a[154:155],v215 offset:768
M v[44:47],a[136:139],a[88:91],v[44:47]
M v[44:47],a[140:143],a[92:95],v[44:47]
M v[20:23],a[136:139],a[96:99],v[20:23]
M v[20:23],a[140:143],a[100:103],v[20:23]
TR a[156:157],v215 offset:8704
M v[28:31],a[136:139],a[104:107],v[28:31]
M v[28:31],a[140:143],a[108:111],v[28:31]
M v[52:55],a[136:139],a[112:115],v[52:55]
M v[52:55],a[140:143],a[116:119],v[52:55]
TR a[158:159],v215 offset:8960
M v[60:63],a[136:139],a[120:123],v[60:63]
M v[60:63],a[140:143],a[124:127],v[60:63]
W lgkmcnt(6)
M v[64:67],a[144:147],a[64:67],v[64:67]
W lgkmcnt(4)
M v[64:67],a[148:151],a[68:71],v[64:67]
TR a[128:129],v214 offset:1024
M v[72:75],a[144:147],a[72:75],v[72:75]
M v[72:75],a[148:151],a[76:79],v[72:75]
M v[96:99],a[144:147],a[80:83],v[96:99]
M v[96:99],a[148:151],a[84:87],v[96:99]
TR a[130:131],v214 offset:1280
M v[104:107],a[144:147],a[88:91],v[104:107]
M v[104:107],a[148:151],a[92:95],v[104:107]
M v[80:83],a[144:147],a[96:99],v[80:83]
M v[80:83],a[148:151],a[100:103],v[80:83]
TR a[132:133],v214 offset:9216
M v[88:91],a[144:147],a[104:107],v[88:91]
M v[88:91],a[148:151],a[108:111],v[88:91]
M v[112:115],a[144:147],a[112:115],v[112:115]
M v[112:115],a[148:151],a[116:119],v[112:115]
TR a[134:135],v214 offset:9472
M v[120:123],a[144:147],a[120:123],v[120:123]
M v[120:123],a[148:151],a[124:127],v[120:123]
W lgkmcnt(6)
M v[68:71],a[152:155],a[64:67],v[68:71]
W lgkmcnt(4)
M v[68:71],a[156:159],a[68:71],v[68:71]
TR a[136:137],v215 offset:1024
M v[76:79],a[152:155],a[72:75],v[76:79]
M v[76:79],a[156:159],a[76:79],v[76:79]
M v[100:103],a[152:155],a[80:83],v[100:103]
M v[100:103],a[156:159],a[84:87],v[100:103]
TR a[138:139],v215 offset:1280
M v[108:111],a[152:155],a[88:91],v[108:111]
M v[108:111],a[156:159],a[92:95],v[108:111]
M v[84:87],a[152:155],a[96:99],v[84:87]
M v[84:87],a[156:159],a[100:103],v[84:87]
TR a[140:141],v215 offset:9216
M v[92:95],a[152:155],a[104:107],v[92:95]
M v[92:95],a[156:159],a[108:111],v[92:95]
M v[116:119],a[152:155],a[112:115],v[116:119]
M v[116:119],a[156:159],a[116:119],v[116:119]
TR a[142:143],v215 offset:9472
M v[124:127],a[152:155],a[120:123],v[124:127]
M v[124:127],a[156:159],a[124:127],v[124:127]
W lgkmcnt(6)
M v[128:131],a[128:131],a[64:67],v[128:131]
W lgkmcnt(4)
M v[128:131],a[132:135],a[68:71],v[128:131]
TR a[144:145],v214 offset:1536
M v[136:139],a[128:131],a[72:75],v[136:139]
M v[136:139],a[132:135],a[76:79],v[136:139]
M v[160:163],a[128:131],a[80:83],v[160:163]
M v[160:163],a[132:135],a[84:87],v[160:163]
TR a[146:147],v214 offset:1792
M v[168:171],a[128:131],a[88:91],v[168:171]
M v[168:171],a[132:135],a[92:95],v[168:171]
M v[144:147],a[128:131],a[96:99],v[144:147]
M v[144:147],a[132:135],a[100:103],v[144:147]
TR a[148:149],v214 offset:9728
M v[152:155],a[128:131],a[104:107],v[152:155]
M v[152:155],a[132:135],a[108:111],v[152:155]
M v[176:179],a[128:131],a[112:115],v[176:179]
M v[176:179],a[132:135],a[116:119],v[176:179]
TR a[150:151],v214 offset:9984
M v[184:187],a[128:131],a[120:123],v[184:187]
M v[184:187],a[132:135],a[124:127],v[184:187]
W lgkmcnt(6)
M v[132:135],a[136:139],a[64:67],v[132:135]
W lgkmcnt(4)
M v[132:135],a[140:143],a[68:71],v[132:135]
TR a[152:153],v215 offset:1536
M v[140:143],a[136:139],a[72:75],v[140:143]
M v[140:143],a[140:143],a[76:79],v[140:143]
M v[164:167],a[136:139],a[80:83],v[164:167]
M v[164:167],a[140:143],a[84:87],v[164:167]
TR a[154:155],v215 offset:1792
M v[172:175],a[136:139],a[88:91],v[172:175]
M v[172:175],a[140:143],a[92:95],v[172:175]
M v[148:151],a[136:139],a[96:99],v[148:151]
M v[148:151],a[140:143],a[100:103],v[148:151]
TR a[156:157],v215 offset:9728
M v[156:159],a[136:139],a[104:107],v[156:159]
M v[156:159],a[140:143],a[108:111],v[156:159]
M v[180:183],a[136:139],a[112:115],v[180:183]
M v[180:183],a[140:143],a[116:119],v[180:183]
TR a[158:159],v215 offset:9984
M v[188:191],a[136:139],a[120:123],v[188:191]
M v[188:191],a[140:143],a[124:127],v[188:191]
W lgkmcnt(6)
M a[0:3],a[144:147],a[64:67],a[0:3]
W lgkmcnt(4)
M a[0:3],a[148:151],a[68:71],a[0:3]
M a[8:11],a[144:147],a[72:75],a[8:11]
M a[8:11],a[148:151],a[76:79],a[8:11]
M a[32:35],a[144:147],a[80:83],a[32:35]
M a[32:35],a[148:151],a[84:87],a[32:35]
M a[40:43],a[144:147],a[88:91],a[40:43]
M a[40:43],a[148:151],a[92:95],a[40:43]
M a[16:19],a[144:147],a[96:99],a[16:19]
M a[16:19],a[148:151],a[100:103],a[16:19]
M a[24:27],a[144:147],a[104:107],a[24:27]
M a[24:27],a[148:151],a[108:111],a[24:27]
M a[48:51],a[144:147],a[112:115],a[48:51]
M a[48:51],a[148:151],a[116:119],a[48:51]
M a[56:59],a[144:147],a[120:123],a[56:59]
M a[56:59],a[148:151],a[124:127],a[56:59]
W lgkmcnt(2)
M a[4:7],a[152:155],a[64:67],a[4:7]
W lgkmcnt(0)
M a[4:7],a[156:159],a[68:71],a[4:7]
M a[12:15],a[152:155],a[72:75],a[12:15]
M a[12:15],a[156:159],a[76:79],a[12:15]
M a[36:39],a[152:155],a[80:83],a[36:39]
M a[36:39],a[156:159],a[84:87],a[36:39]
M a[44:47],a[152:155],a[88:91],a[44:47]
M a[44:47],a[156:159],a[92:95],a[44:47]
M a[20:23],a[152:155],a[96:99],a[20:23]
M a[20:23],a[156:159],a[100:103],a[20:23]
M a[28:31],a[152:155],a[104:107],a[28:31]
M a[28:31],a[156:159],a[108:111],a[28:31]
M a[52:55],a[152:155],a[112:115],a[52:55]
M a[52:55],a[156:159],a[116:119],a[52:55]
M a[60:63],a[152:155],a[120:123],a[60:63]
M a[60:63],a[156:159],a[124:127],a[60:63]
s_branch fk_kv_done
fk_kv_v:
RD a[64:67],v208
RD a[68:71],v209
RD a[72:75],v208 offset:2048
RD a[76:79],v209 offset:2048
RD a[80:83],v208 offset:4096
RD a[84:87],v209 offset:4096
RD a[88:91],v208 offset:6144
RD a[92:95],v209 offset:6144
RD a[96:99],v208 offset:8192
RD a[100:103],v209 offset:8192
RD a[104:107],v208 offset:10240
RD a[108:111],v209 offset:10240
RD a[112:115],v208 offset:12288
RD a[116:119],v209 offset:12288
RD a[120:123],v208 offset:14336
RD a[124:127],v209 offset:14336
TR a[128:129],v212
TR a[130:131],v212 offset:256
TR a[132:133],v212 offset:8192
TR a[134:135],v212 offset:8448
TR a[136:137],v213
TR a[138:139],v213 offset:256
TR a[140:141],v213 offset:8192
TR a[142:143],v213 offset:8448
W lgkmcnt(6)
M v[0:3],a[64:67],a[128:131],v[0:3]
W lgkmcnt(4)
M v[0:3],a[68:71],a[132:135],v[0:3]
TR a[144:145],v212 offset:512
M v[4:7],a[80:83],a[128:131],v[4:7]
M v[4:7],a[84:87],a[132:135],v[4:7]
M v[8:11],a[72:75],a[128:131],v[8:11]
M v[8:11],a[76:79],a[132:135],v[8:11]
TR a[146:147],v212 offset:768
M v[12:15],a[88:91],a[128:131],v[12:15]
M v[12:15],a[92:95],a[132:135],v[12:15]
M v[16:19],a[96:99],a[128:131],v[16:19]
M v[16:19],a[100:103],a[132:135],v[16:19]
TR a[148:149],v212 offset:8704
M v[20:23],a[112:115],a[128:131],v[20:23]
M v[20:23],a[116:119],a[132:135],v[20:23]
M v[24:27],a[104:107],a[128:131],v[24:27]
M v[24:27],a[108:111],a[132:135],v[24:27]
TR a[150:151],v212 offset:8960
M v[28:31],a[120:123],a[128:131],v[28:31]
M v[28:31],a[124:127],a[132:135],v[28:31]
W lgkmcnt(6)
M v[32:35],a[64:67],a[136:139],v[32:35]
W lgkmcnt(4)
M v[32:35],a[68:71],a[140:143],v[32:35]
TR a[152:153],v213 offset:512
M v[36:39],a[80:83],a[136:139],v[36:39]
M v[36:39],a[84:87],a[140:143],v[36:39]
M v[40:43],a[72:75],a[136:139],v[40:43]
M v[40:43],a[76:79],a[140:143],v[40:43]
TR a[154:155],v213 offset:768
M v[44:47],a[88:91],a[136:139],v[44:47]
M v[44:47],a[92:95],a[140:143],v[44:47]
M v[48:51],a[96:99],a[136:139],v[48:51]
M v[48:51],a[100:103],a[140:143],v[48:51]
TR a[156:157],v213 offset:8704
M v[52:55],a[112:115],a[136:139],v[52:55]
M v[52:55],a[116:119],a[140:143],v[52:55]
M v[56:59],a[104:107],a[136:139],v[56:59]
M v[56:59],a[108:111],a[140:143],v[56:59]
TR a[158:159],v213 offset:8960
M v[60:63],a[120:123],a[136:139],v[60:63]
M v[60:63],a[124:127],a[140:143],v[60:63]
W lgkmcnt(6)
M v[64:67],a[64:67],a[144:147],v[64:67]
W lgkmcnt(4)
M v[64:67],a[68:71],a[148:151],v[64:67]
TR a[128:129],v212 offset:1024
M v[68:71],a[80:83],a[144:147],v[68:71]
M v[68:71],a[84:87],a[148:151],v[68:71]
M v[72:75],a[72:75],a[144:147],v[72:75]
M v[72:75],a[76:79],a[148:151],v[72:75]
TR a[130:131],v212 offset:1280
M v[76:79],a[88:91],a[144:147],v[76:79]
M v[76:79],a[92:95],a[148:151],v[76:79]
M v[80:83],a[96:99],a[144:147],v[80:83]
M v[80:83],a[100:103],a[148:151],v[80:83]
TR a[132:133],v212 offset:9216
M v[84:87],a[112:115],a[144:147],v[84:87]
M v[84:87],a[116:119],a[148:151],v[84:87]
M v[88:91],a[104:107],a[144:147],v[88:91]
M v[88:91],a[108:111],a[148:151],v[88:91]
TR a[134:135],v212 offset:9472
M v[92:95],a[120:123],a[144:147],v[92:95]
M v[92:95],a[124:127],a[148:151],v[92:95]
W lgkmcnt(6)
M v[96:99],a[64:67],a[152:155],v[96:99]
W lgkmcnt(4)
M v[96:99],a[68:71],a[156:159],v[96:99]
TR a[136:137],v213 offset:1024
M v[100:103],a[80:83],a[152:155],v[100:103]
M v[100:103],a[84:87],a[156:159],v[100:103]
M v[104:107],a[72:75],a[152:155],v[104:107]
M v[104:107],a[76:79],a[156:159],v[104:107]
TR a[138:139],v213 offset:1280
M v[108:111],a[88:91],a[152:155],v[108:111]
M v[108:111],a[92:95],a[156:159],v[108:111]
M v[112:115],a[96:99],a[152:155],v[112:115]
M v[112:115],a[100:103],a[156:159],v[112:115]
TR a[140:141],v213 offset:9216
M v[116:119],a[112:115],a[152:155],v[116:119]
M v[116:119],a[116:119],a[156:159],v[116:119]
M v[120:123],a[104:107],a[152:155],v[120:123]
M v[120:123],a[108:111],a[156:159],v[120:123]
TR a[142:143],v213 offset:9472
M v[124:127],a[120:123],a[152:155],v[124:127]
M v[124:127],a[124:127],a[156:159],v[124:127]
W lgkmcnt(6)
M v[128:131],a[64:67],a[128:131],v[128:131]
W lgkmcnt(4)
M v[128:131],a[68:71],a[132:135],v[128:131]
TR a[144:145],v212 offset:1536
M v[132:135],a[80:83],a[128:131],v[132:135]
M v[132:135],a[84:87],a[132:135],v[132:135]
M v[136:139],a[72:75],a[128:131],v[136:139]
M v[136:139],a[76:79],a[132:135],v[136:139]
TR a[146:147],v212 offset:1792
M v[140:143],a[88:91],a[128:131],v[140:143]
M v[140:143],a[92:95],a[132:135],v[140:143]
M v[144:147],a[96:99],a[128:131],v[144:147]
M v[144:147],a[100:103],a[132:135],v[144:147]
TR a[148:149],v212 offset:9728
M v[148:151],a[112:115],a[128:131],v[148:151]
M v[148:151],a[116:119],a[132:135],v[148:151]
M v[152:155],a[104:107],a[128:131],v[152:155]
M v[152:155],a[108:111],a[132:135],v[152:155]
TR a[150:151],v212 offset:9984
M v[156:159],a[120:123],a[128:131],v[156:159]
M v[156:159],a[124:127],a[132:135],v[156:159]
W lgkmcnt(6)
M v[160:163],a[64:67],a[136:139],v[160:163]
W lgkmcnt(4)
M v[160:163],a[68:71],a[140:143],v[160:163]
TR a[152:153],v213 offset:1536
M v[164:167],a[80:83],a[136:139],v[164:167]
M v[164:167],a[84:87],a[140:143],v[164:167]
M v[168:171],a[72:75],a[136:139],v[168:171]
M v[168:171],a[76:79],a[140:143],v[168:171]
TR a[154:155],v213 offset:1792
M v[172:175],a[88:91],a[136:139],v[172:175]
M v[172:175],a[92:95],a[140:143],v[172:175]
M v[176:179],a[96:99],a[136:139],v[176:179]
M v[176:179],a[100:103],a[140:143],v[176:179]
TR a[156:157],v213 offset:9728
M v[180:183],a[112:115],a[136:139],v[180:183]
M v[180:183],a[116:119],a[140:143],v[180:183]
M v[184:187],a[104:107],a[136:139],v[184:187]
M v[184:187],a[108:111],a[140:143],v[184:187]
TR a[158:159],v213 offset:9984
M v[188:191],a[120:123],a[136:139],v[188:191]
M v[188:191],a[124:127],a[140:143],v[188:191]
W lgkmcnt(6)
M a[0:3],a[64:67],a[144:147],a[0:3]
W lgkmcnt(4)
M a[0:3],a[68:71],a[148:151],a[0:3]
M a[4:7],a[80:83],a[144:147],a[4:7]
M a[4:7],a[84:87],a[148:151],a[4:7]
M a[8:11],a[72:75],a[144:147],a[8:11]
M a[8:11],a[76:79],a[148:151],a[8:11]
M a[12:15],a[88:91],a[144:147],a[12:15]
M a[12:15],a[92:95],a[148:151],a[12:15]
M a[16:19],a[96:99],a[144:147],a[16:19]
M a[16:19],a[100:103],a[148:151],a[16:19]
M a[20:23],a[112:115],a[144:147],a[20:23]
M a[20:23],a[116:119],a[148:151],a[20:23]
M a[24:27],a[104:107],a[144:147],a[24:27]
M a[24:27],a[108:111],a[148:151],a[24:27]
M a[28:31],a[120:123],a[144:147],a[28:31]
M a[28:31],a[124:127],a[148:151],a[28:31]
W vmcnt(2) lgkmcnt(0)
s_barrier
M a[32:35],a[64:67],a[152:155],a[32:35]
M a[32:35],a[68:71],a[156:159],a[32:35]
TR a[128:129],v214
M a[36:39],a[80:83],a[152:155],a[36:39]
RD a[64:67],v210
RD a[68:71],v211
M a[36:39],a[84:87],a[156:159],a[36:39]
TR a[136:137],v215
M a[40:43],a[72:75],a[152:155],a[40:43]
RD a[80:83],v210 offset:4096
RD a[84:87],v211 offset:4096
M a[40:43],a[76:79],a[156:159],a[40:43]
TR a[130:131],v214 offset:256
M a[44:47],a[88:91],a[152:155],a[44:47]
RD a[72:75],v210 offset:2048
RD a[76:79],v211 offset:2048
M a[44:47],a[92:95],a[156:159],a[44:47]
TR a[138:139],v215 offset:256
M a[48:51],a[96:99],a[152:155],a[48:51]
RD a[88:91],v210 offset:6144
RD a[92:95],v211 offset:6144
M a[48:51],a[100:103],a[156:159],a[48:51]
TR a[132:133],v214 offset:8192
M a[52:55],a[112:115],a[152:155],a[52:55]
RD a[96:99],v210 offset:8192
)~"
R"~(RD a[100:103],v211 offset:8192
M a[52:55],a[116:119],a[156:159],a[52:55]
TR a[140:141],v215 offset:8192
M a[56:59],a[104:107],a[152:155],a[56:59]
RD a[112:115],v210 offset:12288
RD a[116:119],v211 offset:12288
M a[56:59],a[108:111],a[156:159],a[56:59]
TR a[134:135],v214 offset:8448
M a[60:63],a[120:123],a[152:155],a[60:63]
RD a[104:107],v210 offset:10240
RD a[108:111],v211 offset:10240
M a[60:63],a[124:127],a[156:159],a[60:63]
TR a[142:143],v215 offset:8448
RD a[120:123],v210 offset:14336
RD a[124:127],v211 offset:14336
W lgkmcnt(15)
M v[0:3],a[64:67],a[128:131],v[0:3]
W lgkmcnt(5)
M v[0:3],a[68:71],a[132:135],v[0:3]
TR a[144:145],v214 offset:512
M v[4:7],a[80:83],a[128:131],v[4:7]
SA m0,s62,0
M v[4:7],a[84:87],a[132:135],v[4:7]
BL v192,s[48:51],s60 offen lds
M v[8:11],a[72:75],a[128:131],v[8:11]
M v[8:11],a[76:79],a[132:135],v[8:11]
TR a[146:147],v214 offset:768
M v[12:15],a[88:91],a[128:131],v[12:15]
M v[12:15],a[92:95],a[132:135],v[12:15]
SA m0,s63,0
M v[16:19],a[96:99],a[128:131],v[16:19]
BL v200,s[52:55],s61 offen lds
M v[16:19],a[100:103],a[132:135],v[16:19]
TR a[148:149],v214 offset:8704
M v[20:23],a[112:115],a[128:131],v[20:23]
M v[20:23],a[116:119],a[132:135],v[20:23]
W lgkmcnt(7)
M v[24:27],a[104:107],a[128:131],v[24:27]
W lgkmcnt(6)
M v[24:27],a[108:111],a[132:135],v[24:27]
SA m0,s62,0x400
TR a[150:151],v214 offset:8960
W lgkmcnt(5)
M v[28:31],a[120:123],a[128:131],v[28:31]
BL v193,s[48:51],s60 offen lds
W lgkmcnt(4)
M v[28:31],a[124:127],a[132:135],v[28:31]
M v[32:35],a[64:67],a[136:139],v[32:35]
M v[32:35],a[68:71],a[140:143],v[32:35]
TR a[152:153],v215 offset:512
M v[36:39],a[80:83],a[136:139],v[36:39]
SA m0,s63,0x400
M v[36:39],a[84:87],a[140:143],v[36:39]
BL v201,s[52:55],s61 offen lds
M v[40:43],a[72:75],a[136:139],v[40:43]
M v[40:43],a[76:79],a[140:143],v[40:43]
TR a[154:155],v215 offset:768
M v[44:47],a[88:91],a[136:139],v[44:47]
M v[44:47],a[92:95],a[140:143],v[44:47]
SA m0,s62,0x800
M v[48:51],a[96:99],a[136:139],v[48:51]
BL v194,s[48:51],s60 offen lds
M v[48:51],a[100:103],a[140:143],v[48:51]
TR a[156:157],v215 offset:8704
M v[52:55],a[112:115],a[136:139],v[52:55]
M v[52:55],a[116:119],a[140:143],v[52:55]
M v[56:59],a[104:107],a[136:139],v[56:59]
M v[56:59],a[108:111],a[140:143],v[56:59]
SA m0,s63,0x800
TR a[158:159],v215 offset:8960
M v[60:63],a[120:123],a[136:139],v[60:63]
BL v202,s[52:55],s61 offen lds
M v[60:63],a[124:127],a[140:143],v[60:63]
W lgkmcnt(6)
M v[64:67],a[64:67],a[144:147],v[64:67]
W lgkmcnt(4)
M v[64:67],a[68:71],a[148:151],v[64:67]
TR a[128:129],v214 offset:1024
M v[68:71],a[80:83],a[144:147],v[68:71]
SA m0,s62,0xc00
M v[68:71],a[84:87],a[148:151],v[68:71]
BL v195,s[48:51],s60 offen lds
M v[72:75],a[72:75],a[144:147],v[72:75]
M v[72:75],a[76:79],a[148:151],v[72:75]
TR a[130:131],v214 offset:1280
M v[76:79],a[88:91],a[144:147],v[76:79]
M v[76:79],a[92:95],a[148:151],v[76:79]
SA m0,s63,0xc00
M v[80:83],a[96:99],a[144:147],v[80:83]
BL v203,s[52:55],s61 offen lds
M v[80:83],a[100:103],a[148:151],v[80:83]
TR a[132:133],v214 offset:9216
M v[84:87],a[112:115],a[144:147],v[84:87]
M v[84:87],a[116:119],a[148:151],v[84:87]
M v[88:91],a[104:107],a[144:147],v[88:91]
M v[88:91],a[108:111],a[148:151],v[88:91]
SA m0,s62,0x1000
TR a[134:135],v214 offset:9472
M v[92:95],a[120:123],a[144:147],v[92:95]
BL v196,s[48:51],s60 offen lds
M v[92:95],a[124:127],a[148:151],v[92:95]
W lgkmcnt(6)
M v[96:99],a[64:67],a[152:155],v[96:99]
W lgkmcnt(4)
M v[96:99],a[68:71],a[156:159],v[96:99]
TR a[136:137],v215 offset:1024
M v[100:103],a[80:83],a[152:155],v[100:103]
SA m0,s63,0x1000
M v[100:103],a[84:87],a[156:159],v[100:103]
BL v204,s[52:55],s61 offen lds
M v[104:107],a[72:75],a[152:155],v[104:107]
M v[104:107],a[76:79],a[156:159],v[104:107]
TR a[138:139],v215 offset:1280
M v[108:111],a[88:91],a[152:155],v[108:111]
M v[108:111],a[92:95],a[156:159],v[108:111]
SA m0,s62,0x1400
M v[112:115],a[96:99],a[152:155],v[112:115]
BL v197,s[48:51],s60 offen lds
M v[112:115],a[100:103],a[156:159],v[112:115]
TR a[140:141],v215 offset:9216
M v[116:119],a[112:115],a[152:155],v[116:119]
M v[116:119],a[116:119],a[156:159],v[116:119]
M v[120:123],a[104:107],a[152:155],v[120:123]
M v[120:123],a[108:111],a[156:159],v[120:123]
SA m0,s63,0x1400
TR a[142:143],v215 offset:9472
M v[124:127],a[120:123],a[152:155],v[124:127]
BL v205,s[52:55],s61 offen lds
M v[124:127],a[124:127],a[156:159],v[124:127]
W lgkmcnt(6)
M v[128:131],a[64:67],a[128:131],v[128:131]
W lgkmcnt(4)
M v[128:131],a[68:71],a[132:135],v[128:131]
TR a[144:145],v214 offset:1536
M v[132:135],a[80:83],a[128:131],v[132:135]
SA m0,s62,0x1800
M v[132:135],a[84:87],a[132:135],v[132:135]
BL v198,s[48:51],s60 offen lds
M v[136:139],a[72:75],a[128:131],v[136:139]
M v[136:139],a[76:79],a[132:135],v[136:139]
TR a[146:147],v214 offset:1792
M v[140:143],a[88:91],a[128:131],v[140:143]
M v[140:143],a[92:95],a[132:135],v[140:143]
SA m0,s63,0x1800
M v[144:147],a[96:99],a[128:131],v[144:147]
BL v206,s[52:55],s61 offen lds
M v[144:147],a[100:103],a[132:135],v[144:147]
TR a[148:149],v214 offset:9728
M v[148:151],a[112:115],a[128:131],v[148:151]
M v[148:151],a[116:119],a[132:135],v[148:151]
M v[152:155],a[104:107],a[128:131],v[152:155]
M v[152:155],a[108:111],a[132:135],v[152:155]
SA m0,s62,0x1c00
TR a[150:151],v214 offset:9984
M v[156:159],a[120:123],a[128:131],v[156:159]
BL v199,s[48:51],s60 offen lds
M v[156:159],a[124:127],a[132:135],v[156:159]
W lgkmcnt(6)
M v[160:163],a[64:67],a[136:139],v[160:163]
W lgkmcnt(4)
M v[160:163],a[68:71],a[140:143],v[160:163]
TR a[152:153],v215 offset:1536
M v[164:167],a[80:83],a[136:139],v[164:167]
SA m0,s63,0x1c00
M v[164:167],a[84:87],a[140:143],v[164:167]
BL v207,s[52:55],s61 offen lds
M v[168:171],a[72:75],a[136:139],v[168:171]
SA s60,s60,0x80
M v[168:171],a[76:79],a[140:143],v[168:171]
SA s61,s61,0x80000
TR a[154:155],v215 offset:1792
M v[172:175],a[88:91],a[136:139],v[172:175]
M v[172:175],a[92:95],a[140:143],v[172:175]
M v[176:179],a[96:99],a[136:139],v[176:179]
M v[176:179],a[100:103],a[140:143],v[176:179]
TR a[156:157],v215 offset:9728
M v[180:183],a[112:115],a[136:139],v[180:183]
M v[180:183],a[116:119],a[140:143],v[180:183]
M v[184:187],a[104:107],a[136:139],v[184:187]
M v[184:187],a[108:111],a[140:143],v[184:187]
TR a[158:159],v215 offset:9984
M v[188:191],a[120:123],a[136:139],v[188:191]
M v[188:191],a[124:127],a[140:143],v[188:191]
W lgkmcnt(6)
M a[0:3],a[64:67],a[144:147],a[0:3]
W lgkmcnt(4)
M a[0:3],a[68:71],a[148:151],a[0:3]
M a[4:7],a[80:83],a[144:147],a[4:7]
M a[4:7],a[84:87],a[148:151],a[4:7]
M a[8:11],a[72:75],a[144:147],a[8:11]
M a[8:11],a[76:79],a[148:151],a[8:11]
M a[12:15],a[88:91],a[144:147],a[12:15]
M a[12:15],a[92:95],a[148:151],a[12:15]
M a[16:19],a[96:99],a[144:147],a[16:19]
M a[16:19],a[100:103],a[148:151],a[16:19]
M a[20:23],a[112:115],a[144:147],a[20:23]
M a[20:23],a[116:119],a[148:151],a[20:23]
M a[24:27],a[104:107],a[144:147],a[24:27]
M a[24:27],a[108:111],a[148:151],a[24:27]
M a[28:31],a[120:123],a[144:147],a[28:31]
M a[28:31],a[124:127],a[148:151],a[28:31]
W vmcnt(0) lgkmcnt(0)
s_barrier
M a[32:35],a[64:67],a[152:155],a[32:35]
M a[32:35],a[68:71],a[156:159],a[32:35]
TR a[128:129],v212
M a[36:39],a[80:83],a[152:155],a[36:39]
RD a[64:67],v208
RD a[68:71],v209
M a[36:39],a[84:87],a[156:159],a[36:39]
TR a[136:137],v213
M a[40:43],a[72:75],a[152:155],a[40:43]
RD a[80:83],v208 offset:4096
RD a[84:87],v209 offset:4096
M a[40:43],a[76:79],a[156:159],a[40:43]
TR a[130:131],v212 offset:256
M a[44:47],a[88:91],a[152:155],a[44:47]
RD a[72:75],v208 offset:2048
RD a[76:79],v209 offset:2048
M a[44:47],a[92:95],a[156:159],a[44:47]
TR a[138:139],v213 offset:256
M a[48:51],a[96:99],a[152:155],a[48:51]
RD a[88:91],v208 offset:6144
RD a[92:95],v209 offset:6144
M a[48:51],a[100:103],a[156:159],a[48:51]
TR a[132:133],v212 offset:8192
M a[52:55],a[112:115],a[152:155],a[52:55]
RD a[96:99],v208 offset:8192
RD a[100:103],v209 offset:8192
M a[52:55],a[116:119],a[156:159],a[52:55]
TR a[140:141],v213 offset:8192
M a[56:59],a[104:107],a[152:155],a[56:59]
RD a[112:115],v208 offset:12288
RD a[116:119],v209 offset:12288
M a[56:59],a[108:111],a[156:159],a[56:59]
TR a[134:135],v212 offset:8448
M a[60:63],a[120:123],a[152:155],a[60:63]
RD a[104:107],v208 offset:10240
RD a[108:111],v209 offset:10240
M a[60:63],a[124:127],a[156:159],a[60:63]
TR a[142:143],v213 offset:8448
RD a[120:123],v208 offset:14336
RD a[124:127],v209 offset:14336
W lgkmcnt(15)
M v[0:3],a[64:67],a[128:131],v[0:3]
W lgkmcnt(5)
M v[0:3],a[68:71],a[132:135],v[0:3]
TR a[144:145],v212 offset:512
M v[4:7],a[80:83],a[128:131],v[4:7]
SA m0,s62,0x10000
M v[4:7],a[84:87],a[132:135],v[4:7]
BL v192,s[48:51],s60 offen lds
M v[8:11],a[72:75],a[128:131],v[8:11]
M v[8:11],a[76:79],a[132:135],v[8:11]
TR a[146:147],v212 offset:768
M v[12:15],a[88:91],a[128:131],v[12:15]
M v[12:15],a[92:95],a[132:135],v[12:15]
SA m0,s63,0x10000
M v[16:19],a[96:99],a[128:131],v[16:19]
BL v200,s[52:55],s61 offen lds
M v[16:19],a[100:103],a[132:135],v[16:19]
TR a[148:149],v212 offset:8704
M v[20:23],a[112:115],a[128:131],v[20:23]
M v[20:23],a[116:119],a[132:135],v[20:23]
W lgkmcnt(7)
M v[24:27],a[104:107],a[128:131],v[24:27]
W lgkmcnt(6)
M v[24:27],a[108:111],a[132:135],v[24:27]
SA m0,s62,0x10400
TR a[150:151],v212 offset:8960
W lgkmcnt(5)
M v[28:31],a[120:123],a[128:131],v[28:31]
BL v193,s[48:51],s60 offen lds
W lgkmcnt(4)
M v[28:31],a[124:127],a[132:135],v[28:31]
M v[32:35],a[64:67],a[136:139],v[32:35]
M v[32:35],a[68:71],a[140:143],v[32:35]
TR a[152:153],v213 offset:512
M v[36:39],a[80:83],a[136:139],v[36:39]
SA m0,s63,0x10400
M v[36:39],a[84:87],a[140:143],v[36:39]
BL v201,s[52:55],s61 offen lds
M v[40:43],a[72:75],a[136:139],v[40:43]
M v[40:43],a[76:79],a[140:143],v[40:43]
TR a[154:155],v213 offset:768
M v[44:47],a[88:91],a[136:139],v[44:47]
M v[44:47],a[92:95],a[140:143],v[44:47]
SA m0,s62,0x10800
M v[48:51],a[96:99],a[136:139],v[48:51]
BL v194,s[48:51],s60 offen lds
M v[48:51],a[100:103],a[140:143],v[48:51]
TR a[156:157],v213 offset:8704
M v[52:55],a[112:115],a[136:139],v[52:55]
M v[52:55],a[116:119],a[140:143],v[52:55]
M v[56:59],a[104:107],a[136:139],v[56:59]
M v[56:59],a[108:111],a[140:143],v[56:59]
SA m0,s63,0x10800
TR a[158:159],v213 offset:8960
M v[60:63],a[120:123],a[136:139],v[60:63]
BL v202,s[52:55],s61 offen lds
M v[60:63],a[124:127],a[140:143],v[60:63]
W lgkmcnt(6)
M v[64:67],a[64:67],a[144:147],v[64:67]
W lgkmcnt(4)
M v[64:67],a[68:71],a[148:151],v[64:67]
TR a[128:129],v212 offset:1024
M v[68:71],a[80:83],a[144:147],v[68:71]
SA m0,s62,0x10c00
M v[68:71],a[84:87],a[148:151],v[68:71]
BL v195,s[48:51],s60 offen lds
M v[72:75],a[72:75],a[144:147],v[72:75]
M v[72:75],a[76:79],a[148:151],v[72:75]
TR a[130:131],v212 offset:1280
M v[76:79],a[88:91],a[144:147],v[76:79]
M v[76:79],a[92:95],a[148:151],v[76:79]
SA m0,s63,0x10c00
M v[80:83],a[96:99],a[144:147],v[80:83]
BL v203,s[52:55],s61 offen lds
M v[80:83],a[100:103],a[148:151],v[80:83]
TR a[132:133],v212 offset:9216
M v[84:87],a[112:115],a[144:147],v[84:87]
M v[84:87],a[116:119],a[148:151],v[84:87]
M v[88:91],a[104:107],a[144:147],v[88:91]
M v[88:91],a[108:111],a[148:151],v[88:91]
SA m0,s62,0x11000
TR a[134:135],v212 offset:9472
M v[92:95],a[120:123],a[144:147],v[92:95]
BL v196,s[48:51],s60 offen lds
M v[92:95],a[124:127],a[148:151],v[92:95]
W lgkmcnt(6)
M v[96:99],a[64:67],a[152:155],v[96:99]
W lgkmcnt(4)
M v[96:99],a[68:71],a[156:159],v[96:99]
TR a[136:137],v213 offset:1024
M v[100:103],a[80:83],a[152:155],v[100:103]
SA m0,s63,0x11000
M v[100:103],a[84:87],a[156:159],v[100:103]
BL v204,s[52:55],s61 offen lds
M v[104:107],a[72:75],a[152:155],v[104:107]
M v[104:107],a[76:79],a[156:159],v[104:107]
TR a[138:139],v213 offset:1280
M v[108:111],a[88:91],a[152:155],v[108:111]
M v[108:111],a[92:95],a[156:159],v[108:111]
SA m0,s62,0x11400
M v[112:115],a[96:99],a[152:155],v[112:115]
BL v197,s[48:51],s60 offen lds
M v[112:115],a[100:103],a[156:159],v[112:115]
TR a[140:141],v213 offset:9216
M v[116:119],a[112:115],a[152:155],v[116:119]
M v[116:119],a[116:119],a[156:159],v[116:119]
M v[120:123],a[104:107],a[152:155],v[120:123]
M v[120:123],a[108:111],a[156:159],v[120:123]
SA m0,s63,0x11400
TR a[142:143],v213 offset:9472
M v[124:127],a[120:123],a[152:155],v[124:127]
BL v205,s[52:55],s61 offen lds
M v[124:127],a[124:127],a[156:159],v[124:127]
W lgkmcnt(6)
M v[128:131],a[64:67],a[128:131],v[128:131]
W lgkmcnt(4)
M v[128:131],a[68:71],a[132:135],v[128:131]
TR a[144:145],v212 offset:1536
M v[132:135],a[80:83],a[128:131],v[132:135]
SA m0,s62,0x11800
M v[132:135],a[84:87],a[132:135],v[132:135]
BL v198,s[48:51],s60 offen lds
M v[136:139],a[72:75],a[128:131],v[136:139]
M v[136:139],a[76:79],a[132:135],v[136:139]
TR a[146:147],v212 offset:1792
M v[140:143],a[88:91],a[128:131],v[140:143]
M v[140:143],a[92:95],a[132:135],v[140:143]
SA m0,s63,0x11800
M v[144:147],a[96:99],a[128:131],v[144:147]
BL v206,s[52:55],s61 offen lds
M v[144:147],a[100:103],a[132:135],v[144:147]
TR a[148:149],v212 offset:9728
M v[148:151],a[112:115],a[128:131],v[148:151]
M v[148:151],a[116:119],a[132:135],v[148:151]
M v[152:155],a[104:107],a[128:131],v[152:155]
M v[152:155],a[108:111],a[132:135],v[152:155]
SA m0,s62,0x11c00
TR a[150:151],v212 offset:9984
M v[156:159],a[120:123],a[128:131],v[156:159]
BL v199,s[48:51],s60 offen lds
M v[156:159],a[124:127],a[132:135],v[156:159]
W lgkmcnt(6)
M v[160:163],a[64:67],a[136:139],v[160:163]
W lgkmcnt(4)
M v[160:163],a[68:71],a[140:143],v[160:163]
TR a[152:153],v213 offset:1536
M v[164:167],a[80:83],a[136:139],v[164:167]
SA m0,s63,0x11c00
M v[164:167],a[84:87],a[140:143],v[164:167]
BL v207,s[52:55],s61 offen lds
M v[168:171],a[72:75],a[136:139],v[168:171]
SA s60,s60,0x80
M v[168:171],a[76:79],a[140:143],v[168:171]
SA s61,s61,0x80000
TR a[154:155],v213 offset:1792
M v[172:175],a[88:91],a[136:139],v[172:175]
M v[172:175],a[92:95],a[140:143],v[172:175]
M v[176:179],a[96:99],a[136:139],v[176:179]
M v[176:179],a[100:103],a[140:143],v[176:179]
TR a[156:157],v213 offset:9728
M v[180:183],a[112:115],a[136:139],v[180:183]
M v[180:183],a[116:119],a[140:143],v[180:183]
M v[184:187],a[104:107],a[136:139],v[184:187]
)~"
R"~(M v[184:187],a[108:111],a[140:143],v[184:187]
TR a[158:159],v213 offset:9984
M v[188:191],a[120:123],a[136:139],v[188:191]
M v[188:191],a[124:127],a[140:143],v[188:191]
W lgkmcnt(6)
M a[0:3],a[64:67],a[144:147],a[0:3]
W lgkmcnt(4)
M a[0:3],a[68:71],a[148:151],a[0:3]
M a[4:7],a[80:83],a[144:147],a[4:7]
M a[4:7],a[84:87],a[148:151],a[4:7]
M a[8:11],a[72:75],a[144:147],a[8:11]
M a[8:11],a[76:79],a[148:151],a[8:11]
M a[12:15],a[88:91],a[144:147],a[12:15]
M a[12:15],a[92:95],a[148:151],a[12:15]
M a[16:19],a[96:99],a[144:147],a[16:19]
M a[16:19],a[100:103],a[148:151],a[16:19]
M a[20:23],a[112:115],a[144:147],a[20:23]
M a[20:23],a[116:119],a[148:151],a[20:23]
M a[24:27],a[104:107],a[144:147],a[24:27]
M a[24:27],a[108:111],a[148:151],a[24:27]
M a[28:31],a[120:123],a[144:147],a[28:31]
M a[28:31],a[124:127],a[148:151],a[28:31]
W vmcnt(0) lgkmcnt(0)
s_barrier
M a[32:35],a[64:67],a[152:155],a[32:35]
M a[32:35],a[68:71],a[156:159],a[32:35]
TR a[128:129],v214
M a[36:39],a[80:83],a[152:155],a[36:39]
RD a[64:67],v210
RD a[68:71],v211
M a[36:39],a[84:87],a[156:159],a[36:39]
TR a[136:137],v215
M a[40:43],a[72:75],a[152:155],a[40:43]
RD a[80:83],v210 offset:4096
RD a[84:87],v211 offset:4096
M a[40:43],a[76:79],a[156:159],a[40:43]
TR a[130:131],v214 offset:256
M a[44:47],a[88:91],a[152:155],a[44:47]
RD a[72:75],v210 offset:2048
RD a[76:79],v211 offset:2048
M a[44:47],a[92:95],a[156:159],a[44:47]
TR a[138:139],v215 offset:256
M a[48:51],a[96:99],a[152:155],a[48:51]
RD a[88:91],v210 offset:6144
RD a[92:95],v211 offset:6144
M a[48:51],a[100:103],a[156:159],a[48:51]
TR a[132:133],v214 offset:8192
M a[52:55],a[112:115],a[152:155],a[52:55]
RD a[96:99],v210 offset:8192
RD a[100:103],v211 offset:8192
M a[52:55],a[116:119],a[156:159],a[52:55]
TR a[140:141],v215 offset:8192
M a[56:59],a[104:107],a[152:155],a[56:59]
RD a[112:115],v210 offset:12288
RD a[116:119],v211 offset:12288
M a[56:59],a[108:111],a[156:159],a[56:59]
TR a[134:135],v214 offset:8448
M a[60:63],a[120:123],a[152:155],a[60:63]
RD a[104:107],v210 offset:10240
RD a[108:111],v211 offset:10240
M a[60:63],a[124:127],a[156:159],a[60:63]
TR a[142:143],v215 offset:8448
RD a[120:123],v210 offset:14336
RD a[124:127],v211 offset:14336
W lgkmcnt(15)
M v[0:3],a[64:67],a[128:131],v[0:3]
W lgkmcnt(5)
M v[0:3],a[68:71],a[132:135],v[0:3]
TR a[144:145],v214 offset:512
M v[4:7],a[80:83],a[128:131],v[4:7]
SA m0,s62,0
M v[4:7],a[84:87],a[132:135],v[4:7]
BL v192,s[48:51],s60 offen lds
M v[8:11],a[72:75],a[128:131],v[8:11]
M v[8:11],a[76:79],a[132:135],v[8:11]
TR a[146:147],v214 offset:768
M v[12:15],a[88:91],a[128:131],v[12:15]
M v[12:15],a[92:95],a[132:135],v[12:15]
SA m0,s63,0
M v[16:19],a[96:99],a[128:131],v[16:19]
BL v200,s[52:55],s61 offen lds
M v[16:19],a[100:103],a[132:135],v[16:19]
TR a[148:149],v214 offset:8704
M v[20:23],a[112:115],a[128:131],v[20:23]
M v[20:23],a[116:119],a[132:135],v[20:23]
W lgkmcnt(7)
M v[24:27],a[104:107],a[128:131],v[24:27]
W lgkmcnt(6)
M v[24:27],a[108:111],a[132:135],v[24:27]
SA m0,s62,0x400
TR a[150:151],v214 offset:8960
W lgkmcnt(5)
M v[28:31],a[120:123],a[128:131],v[28:31]
BL v193,s[48:51],s60 offen lds
W lgkmcnt(4)
M v[28:31],a[124:127],a[132:135],v[28:31]
M v[32:35],a[64:67],a[136:139],v[32:35]
M v[32:35],a[68:71],a[140:143],v[32:35]
TR a[152:153],v215 offset:512
M v[36:39],a[80:83],a[136:139],v[36:39]
SA m0,s63,0x400
M v[36:39],a[84:87],a[140:143],v[36:39]
BL v201,s[52:55],s61 offen lds
M v[40:43],a[72:75],a[136:139],v[40:43]
M v[40:43],a[76:79],a[140:143],v[40:43]
TR a[154:155],v215 offset:768
M v[44:47],a[88:91],a[136:139],v[44:47]
M v[44:47],a[92:95],a[140:143],v[44:47]
SA m0,s62,0x800
M v[48:51],a[96:99],a[136:139],v[48:51]
BL v194,s[48:51],s60 offen lds
M v[48:51],a[100:103],a[140:143],v[48:51]
TR a[156:157],v215 offset:8704
M v[52:55],a[112:115],a[136:139],v[52:55]
M v[52:55],a[116:119],a[140:143],v[52:55]
M v[56:59],a[104:107],a[136:139],v[56:59]
M v[56:59],a[108:111],a[140:143],v[56:59]
SA m0,s63,0x800
TR a[158:159],v215 offset:8960
M v[60:63],a[120:123],a[136:139],v[60:63]
BL v202,s[52:55],s61 offen lds
M v[60:63],a[124:127],a[140:143],v[60:63]
W lgkmcnt(6)
M v[64:67],a[64:67],a[144:147],v[64:67]
W lgkmcnt(4)
M v[64:67],a[68:71],a[148:151],v[64:67]
TR a[128:129],v214 offset:1024
M v[68:71],a[80:83],a[144:147],v[68:71]
SA m0,s62,0xc00
M v[68:71],a[84:87],a[148:151],v[68:71]
BL v195,s[48:51],s60 offen lds
M v[72:75],a[72:75],a[144:147],v[72:75]
M v[72:75],a[76:79],a[148:151],v[72:75]
TR a[130:131],v214 offset:1280
M v[76:79],a[88:91],a[144:147],v[76:79]
M v[76:79],a[92:95],a[148:151],v[76:79]
SA m0,s63,0xc00
M v[80:83],a[96:99],a[144:147],v[80:83]
BL v203,s[52:55],s61 offen lds
M v[80:83],a[100:103],a[148:151],v[80:83]
TR a[132:133],v214 offset:9216
M v[84:87],a[112:115],a[144:147],v[84:87]
M v[84:87],a[116:119],a[148:151],v[84:87]
M v[88:91],a[104:107],a[144:147],v[88:91]
M v[88:91],a[108:111],a[148:151],v[88:91]
SA m0,s62,0x1000
TR a[134:135],v214 offset:9472
M v[92:95],a[120:123],a[144:147],v[92:95]
BL v196,s[48:51],s60 offen lds
M v[92:95],a[124:127],a[148:151],v[92:95]
W lgkmcnt(6)
M v[96:99],a[64:67],a[152:155],v[96:99]
W lgkmcnt(4)
M v[96:99],a[68:71],a[156:159],v[96:99]
TR a[136:137],v215 offset:1024
M v[100:103],a[80:83],a[152:155],v[100:103]
SA m0,s63,0x1000
M v[100:103],a[84:87],a[156:159],v[100:103]
BL v204,s[52:55],s61 offen lds
M v[104:107],a[72:75],a[152:155],v[104:107]
M v[104:107],a[76:79],a[156:159],v[104:107]
TR a[138:139],v215 offset:1280
M v[108:111],a[88:91],a[152:155],v[108:111]
M v[108:111],a[92:95],a[156:159],v[108:111]
SA m0,s62,0x1400
M v[112:115],a[96:99],a[152:155],v[112:115]
BL v197,s[48:51],s60 offen lds
M v[112:115],a[100:103],a[156:159],v[112:115]
TR a[140:141],v215 offset:9216
M v[116:119],a[112:115],a[152:155],v[116:119]
M v[116:119],a[116:119],a[156:159],v[116:119]
M v[120:123],a[104:107],a[152:155],v[120:123]
M v[120:123],a[108:111],a[156:159],v[120:123]
SA m0,s63,0x1400
TR a[142:143],v215 offset:9472
M v[124:127],a[120:123],a[152:155],v[124:127]
BL v205,s[52:55],s61 offen lds
M v[124:127],a[124:127],a[156:159],v[124:127]
W lgkmcnt(6)
M v[128:131],a[64:67],a[128:131],v[128:131]
W lgkmcnt(4)
M v[128:131],a[68:71],a[132:135],v[128:131]
TR a[144:145],v214 offset:1536
M v[132:135],a[80:83],a[128:131],v[132:135]
SA m0,s62,0x1800
M v[132:135],a[84:87],a[132:135],v[132:135]
BL v198,s[48:51],s60 offen lds
M v[136:139],a[72:75],a[128:131],v[136:139]
M v[136:139],a[76:79],a[132:135],v[136:139]
TR a[146:147],v214 offset:1792
M v[140:143],a[88:91],a[128:131],v[140:143]
M v[140:143],a[92:95],a[132:135],v[140:143]
SA m0,s63,0x1800
M v[144:147],a[96:99],a[128:131],v[144:147]
BL v206,s[52:55],s61 offen lds
M v[144:147],a[100:103],a[132:135],v[144:147]
TR a[148:149],v214 offset:9728
M v[148:151],a[112:115],a[128:131],v[148:151]
M v[148:151],a[116:119],a[132:135],v[148:151]
M v[152:155],a[104:107],a[128:131],v[152:155]
M v[152:155],a[108:111],a[132:135],v[152:155]
SA m0,s62,0x1c00
TR a[150:151],v214 offset:9984
M v[156:159],a[120:123],a[128:131],v[156:159]
BL v199,s[48:51],s60 offen lds
M v[156:159],a[124:127],a[132:135],v[156:159]
W lgkmcnt(6)
M v[160:163],a[64:67],a[136:139],v[160:163]
W lgkmcnt(4)
M v[160:163],a[68:71],a[140:143],v[160:163]
TR a[152:153],v215 offset:1536
M v[164:167],a[80:83],a[136:139],v[164:167]
SA m0,s63,0x1c00
M v[164:167],a[84:87],a[140:143],v[164:167]
BL v207,s[52:55],s61 offen lds
M v[168:171],a[72:75],a[136:139],v[168:171]
SA s60,s60,0x80
M v[168:171],a[76:79],a[140:143],v[168:171]
SA s61,s61,0x80000
TR a[154:155],v215 offset:1792
M v[172:175],a[88:91],a[136:139],v[172:175]
M v[172:175],a[92:95],a[140:143],v[172:175]
M v[176:179],a[96:99],a[136:139],v[176:179]
M v[176:179],a[100:103],a[140:143],v[176:179]
TR a[156:157],v215 offset:9728
M v[180:183],a[112:115],a[136:139],v[180:183]
M v[180:183],a[116:119],a[140:143],v[180:183]
M v[184:187],a[104:107],a[136:139],v[184:187]
M v[184:187],a[108:111],a[140:143],v[184:187]
TR a[158:159],v215 offset:9984
M v[188:191],a[120:123],a[136:139],v[188:191]
M v[188:191],a[124:127],a[140:143],v[188:191]
W lgkmcnt(6)
M a[0:3],a[64:67],a[144:147],a[0:3]
W lgkmcnt(4)
M a[0:3],a[68:71],a[148:151],a[0:3]
M a[4:7],a[80:83],a[144:147],a[4:7]
M a[4:7],a[84:87],a[148:151],a[4:7]
M a[8:11],a[72:75],a[144:147],a[8:11]
M a[8:11],a[76:79],a[148:151],a[8:11]
M a[12:15],a[88:91],a[144:147],a[12:15]
M a[12:15],a[92:95],a[148:151],a[12:15]
M a[16:19],a[96:99],a[144:147],a[16:19]
M a[16:19],a[100:103],a[148:151],a[16:19]
M a[20:23],a[112:115],a[144:147],a[20:23]
M a[20:23],a[116:119],a[148:151],a[20:23]
M a[24:27],a[104:107],a[144:147],a[24:27]
M a[24:27],a[108:111],a[148:151],a[24:27]
M a[28:31],a[120:123],a[144:147],a[28:31]
M a[28:31],a[124:127],a[148:151],a[28:31]
W vmcnt(0) lgkmcnt(0)
s_barrier
M a[32:35],a[64:67],a[152:155],a[32:35]
M a[32:35],a[68:71],a[156:159],a[32:35]
TR a[128:129],v212
M a[36:39],a[80:83],a[152:155],a[36:39]
RD a[64:67],v208
RD a[68:71],v209
M a[36:39],a[84:87],a[156:159],a[36:39]
TR a[136:137],v213
M a[40:43],a[72:75],a[152:155],a[40:43]
RD a[80:83],v208 offset:4096
RD a[84:87],v209 offset:4096
M a[40:43],a[76:79],a[156:159],a[40:43]
TR a[130:131],v212 offset:256
M a[44:47],a[88:91],a[152:155],a[44:47]
RD a[72:75],v208 offset:2048
RD a[76:79],v209 offset:2048
M a[44:47],a[92:95],a[156:159],a[44:47]
TR a[138:139],v213 offset:256
M a[48:51],a[96:99],a[152:155],a[48:51]
RD a[88:91],v208 offset:6144
RD a[92:95],v209 offset:6144
M a[48:51],a[100:103],a[156:159],a[48:51]
TR a[132:133],v212 offset:8192
M a[52:55],a[112:115],a[152:155],a[52:55]
RD a[96:99],v208 offset:8192
RD a[100:103],v209 offset:8192
M a[52:55],a[116:119],a[156:159],a[52:55]
TR a[140:141],v213 offset:8192
M a[56:59],a[104:107],a[152:155],a[56:59]
RD a[112:115],v208 offset:12288
RD a[116:119],v209 offset:12288
M a[56:59],a[108:111],a[156:159],a[56:59]
TR a[134:135],v212 offset:8448
M a[60:63],a[120:123],a[152:155],a[60:63]
RD a[104:107],v208 offset:10240
RD a[108:111],v209 offset:10240
M a[60:63],a[124:127],a[156:159],a[60:63]
TR a[142:143],v213 offset:8448
RD a[120:123],v208 offset:14336
RD a[124:127],v209 offset:14336
W lgkmcnt(15)
M v[0:3],a[64:67],a[128:131],v[0:3]
W lgkmcnt(5)
M v[0:3],a[68:71],a[132:135],v[0:3]
TR a[144:145],v212 offset:512
M v[4:7],a[80:83],a[128:131],v[4:7]
SA m0,s62,0x10000
M v[4:7],a[84:87],a[132:135],v[4:7]
BL v192,s[48:51],s60 offen lds
M v[8:11],a[72:75],a[128:131],v[8:11]
M v[8:11],a[76:79],a[132:135],v[8:11]
TR a[146:147],v212 offset:768
M v[12:15],a[88:91],a[128:131],v[12:15]
M v[12:15],a[92:95],a[132:135],v[12:15]
SA m0,s63,0x10000
M v[16:19],a[96:99],a[128:131],v[16:19]
BL v200,s[52:55],s61 offen lds
M v[16:19],a[100:103],a[132:135],v[16:19]
TR a[148:149],v212 offset:8704
M v[20:23],a[112:115],a[128:131],v[20:23]
M v[20:23],a[116:119],a[132:135],v[20:23]
W lgkmcnt(7)
M v[24:27],a[104:107],a[128:131],v[24:27]
W lgkmcnt(6)
M v[24:27],a[108:111],a[132:135],v[24:27]
SA m0,s62,0x10400
TR a[150:151],v212 offset:8960
W lgkmcnt(5)
M v[28:31],a[120:123],a[128:131],v[28:31]
BL v193,s[48:51],s60 offen lds
W lgkmcnt(4)
M v[28:31],a[124:127],a[132:135],v[28:31]
M v[32:35],a[64:67],a[136:139],v[32:35]
M v[32:35],a[68:71],a[140:143],v[32:35]
TR a[152:153],v213 offset:512
M v[36:39],a[80:83],a[136:139],v[36:39]
SA m0,s63,0x10400
M v[36:39],a[84:87],a[140:143],v[36:39]
BL v201,s[52:55],s61 offen lds
M v[40:43],a[72:75],a[136:139],v[40:43]
M v[40:43],a[76:79],a[140:143],v[40:43]
TR a[154:155],v213 offset:768
M v[44:47],a[88:91],a[136:139],v[44:47]
M v[44:47],a[92:95],a[140:143],v[44:47]
SA m0,s62,0x10800
M v[48:51],a[96:99],a[136:139],v[48:51]
BL v194,s[48:51],s60 offen lds
M v[48:51],a[100:103],a[140:143],v[48:51]
TR a[156:157],v213 offset:8704
M v[52:55],a[112:115],a[136:139],v[52:55]
M v[52:55],a[116:119],a[140:143],v[52:55]
M v[56:59],a[104:107],a[136:139],v[56:59]
M v[56:59],a[108:111],a[140:143],v[56:59]
SA m0,s63,0x10800
TR a[158:159],v213 offset:8960
M v[60:63],a[120:123],a[136:139],v[60:63]
BL v202,s[52:55],s61 offen lds
M v[60:63],a[124:127],a[140:143],v[60:63]
W lgkmcnt(6)
M v[64:67],a[64:67],a[144:147],v[64:67]
W lgkmcnt(4)
M v[64:67],a[68:71],a[148:151],v[64:67]
TR a[128:129],v212 offset:1024
M v[68:71],a[80:83],a[144:147],v[68:71]
SA m0,s62,0x10c00
M v[68:71],a[84:87],a[148:151],v[68:71]
BL v195,s[48:51],s60 offen lds
M v[72:75],a[72:75],a[144:147],v[72:75]
M v[72:75],a[76:79],a[148:151],v[72:75]
TR a[130:131],v212 offset:1280
M v[76:79],a[88:91],a[144:147],v[76:79]
M v[76:79],a[92:95],a[148:151],v[76:79]
SA m0,s63,0x10c00
M v[80:83],a[96:99],a[144:147],v[80:83]
BL v203,s[52:55],s61 offen lds
M v[80:83],a[100:103],a[148:151],v[80:83]
TR a[132:133],v212 offset:9216
M v[84:87],a[112:115],a[144:147],v[84:87]
M v[84:87],a[116:119],a[148:151],v[84:87]
M v[88:91],a[104:107],a[144:147],v[88:91]
M v[88:91],a[108:111],a[148:151],v[88:91]
SA m0,s62,0x11000
TR a[134:135],v212 offset:9472
M v[92:95],a[120:123],a[144:147],v[92:95]
BL v196,s[48:51],s60 offen lds
M v[92:95],a[124:127],a[148:151],v[92:95]
W lgkmcnt(6)
M v[96:99],a[64:67],a[152:155],v[96:99]
W lgkmcnt(4)
M v[96:99],a[68:71],a[156:159],v[96:99]
TR a[136:137],v213 offset:1024
M v[100:103],a[80:83],a[152:155],v[100:103]
SA m0,s63,0x11000
M v[100:103],a[84:87],a[156:159],v[100:103]
BL v204,s[52:55],s61 offen lds
M v[104:107],a[72:75],a[152:155],v[104:107]
M v[104:107],a[76:79],a[156:159],v[104:107]
TR a[138:139],v213 offset:1280
M v[108:111],a[88:91],a[152:155],v[108:111]
M v[108:111],a[92:95],a[156:159],v[108:111]
SA m0,s62,0x11400
M v[112:115],a[96:99],a[152:155],v[112:115]
BL v197,s[48:51],s60 offen lds
M v[112:115],a[100:103],a[156:159],v[112:115]
TR a[140:141],v213 offset:9216
M v[116:119],a[112:115],a[152:155],v[116:119]
M v[116:119],a[116:119],a[156:159],v[116:119]
M v[120:123],a[104:107],a[152:155],v[120:123]
M v[120:123],a[108:111],a[156:159],v[120:123]
SA m0,s63,0x11400
TR a[142:143],v213 offset:9472
M v[124:127],a[120:123],a[152:155],v[124:127]
BL v205,s[52:55],s61 offen lds
M v[124:127],a[124:127],a[156:159],v[124:127]
W lgkmcnt(6)
M v[128:131],a[64:67],a[128:131],v[128:131]
W lgkmcnt(4)
M v[128:131],a[68:71],a[132:135],v[128:131]
TR a[144:145],v212 offset:1536
)~"
R"~(M v[132:135],a[80:83],a[128:131],v[132:135]
SA m0,s62,0x11800
M v[132:135],a[84:87],a[132:135],v[132:135]
BL v198,s[48:51],s60 offen lds
M v[136:139],a[72:75],a[128:131],v[136:139]
M v[136:139],a[76:79],a[132:135],v[136:139]
TR a[146:147],v212 offset:1792
M v[140:143],a[88:91],a[128:131],v[140:143]
M v[140:143],a[92:95],a[132:135],v[140:143]
SA m0,s63,0x11800
M v[144:147],a[96:99],a[128:131],v[144:147]
BL v206,s[52:55],s61 offen lds
M v[144:147],a[100:103],a[132:135],v[144:147]
TR a[148:149],v212 offset:9728
M v[148:151],a[112:115],a[128:131],v[148:151]
M v[148:151],a[116:119],a[132:135],v[148:151]
M v[152:155],a[104:107],a[128:131],v[152:155]
M v[152:155],a[108:111],a[132:135],v[152:155]
SA m0,s62,0x11c00
TR a[150:151],v212 offset:9984
M v[156:159],a[120:123],a[128:131],v[156:159]
BL v199,s[48:51],s60 offen lds
M v[156:159],a[124:127],a[132:135],v[156:159]
W lgkmcnt(6)
M v[160:163],a[64:67],a[136:139],v[160:163]
W lgkmcnt(4)
M v[160:163],a[68:71],a[140:143],v[160:163]
TR a[152:153],v213 offset:1536
M v[164:167],a[80:83],a[136:139],v[164:167]
SA m0,s63,0x11c00
M v[164:167],a[84:87],a[140:143],v[164:167]
BL v207,s[52:55],s61 offen lds
M v[168:171],a[72:75],a[136:139],v[168:171]
SA s60,s60,0x80
M v[168:171],a[76:79],a[140:143],v[168:171]
SA s61,s61,0x80000
TR a[154:155],v213 offset:1792
M v[172:175],a[88:91],a[136:139],v[172:175]
M v[172:175],a[92:95],a[140:143],v[172:175]
M v[176:179],a[96:99],a[136:139],v[176:179]
M v[176:179],a[100:103],a[140:143],v[176:179]
TR a[156:157],v213 offset:9728
M v[180:183],a[112:115],a[136:139],v[180:183]
M v[180:183],a[116:119],a[140:143],v[180:183]
M v[184:187],a[104:107],a[136:139],v[184:187]
M v[184:187],a[108:111],a[140:143],v[184:187]
TR a[158:159],v213 offset:9984
M v[188:191],a[120:123],a[136:139],v[188:191]
M v[188:191],a[124:127],a[140:143],v[188:191]
W lgkmcnt(6)
M a[0:3],a[64:67],a[144:147],a[0:3]
W lgkmcnt(4)
M a[0:3],a[68:71],a[148:151],a[0:3]
M a[4:7],a[80:83],a[144:147],a[4:7]
M a[4:7],a[84:87],a[148:151],a[4:7]
M a[8:11],a[72:75],a[144:147],a[8:11]
M a[8:11],a[76:79],a[148:151],a[8:11]
M a[12:15],a[88:91],a[144:147],a[12:15]
M a[12:15],a[92:95],a[148:151],a[12:15]
M a[16:19],a[96:99],a[144:147],a[16:19]
M a[16:19],a[100:103],a[148:151],a[16:19]
M a[20:23],a[112:115],a[144:147],a[20:23]
M a[20:23],a[116:119],a[148:151],a[20:23]
M a[24:27],a[104:107],a[144:147],a[24:27]
M a[24:27],a[108:111],a[148:151],a[24:27]
M a[28:31],a[120:123],a[144:147],a[28:31]
M a[28:31],a[124:127],a[148:151],a[28:31]
W vmcnt(0) lgkmcnt(0)
s_barrier
M a[32:35],a[64:67],a[152:155],a[32:35]
M a[32:35],a[68:71],a[156:159],a[32:35]
TR a[128:129],v214
M a[36:39],a[80:83],a[152:155],a[36:39]
RD a[64:67],v210
RD a[68:71],v211
M a[36:39],a[84:87],a[156:159],a[36:39]
TR a[136:137],v215
M a[40:43],a[72:75],a[152:155],a[40:43]
RD a[80:83],v210 offset:4096
RD a[84:87],v211 offset:4096
M a[40:43],a[76:79],a[156:159],a[40:43]
TR a[130:131],v214 offset:256
M a[44:47],a[88:91],a[152:155],a[44:47]
RD a[72:75],v210 offset:2048
RD a[76:79],v211 offset:2048
M a[44:47],a[92:95],a[156:159],a[44:47]
TR a[138:139],v215 offset:256
M a[48:51],a[96:99],a[152:155],a[48:51]
RD a[88:91],v210 offset:6144
RD a[92:95],v211 offset:6144
M a[48:51],a[100:103],a[156:159],a[48:51]
TR a[132:133],v214 offset:8192
M a[52:55],a[112:115],a[152:155],a[52:55]
RD a[96:99],v210 offset:8192
RD a[100:103],v211 offset:8192
M a[52:55],a[116:119],a[156:159],a[52:55]
TR a[140:141],v215 offset:8192
M a[56:59],a[104:107],a[152:155],a[56:59]
RD a[112:115],v210 offset:12288
RD a[116:119],v211 offset:12288
M a[56:59],a[108:111],a[156:159],a[56:59]
TR a[134:135],v214 offset:8448
M a[60:63],a[120:123],a[152:155],a[60:63]
RD a[104:107],v210 offset:10240
RD a[108:111],v211 offset:10240
M a[60:63],a[124:127],a[156:159],a[60:63]
TR a[142:143],v215 offset:8448
RD a[120:123],v210 offset:14336
RD a[124:127],v211 offset:14336
W lgkmcnt(15)
M v[0:3],a[64:67],a[128:131],v[0:3]
W lgkmcnt(5)
M v[0:3],a[68:71],a[132:135],v[0:3]
TR a[144:145],v214 offset:512
M v[4:7],a[80:83],a[128:131],v[4:7]
SA m0,s62,0
M v[4:7],a[84:87],a[132:135],v[4:7]
BL v192,s[48:51],s60 offen lds
M v[8:11],a[72:75],a[128:131],v[8:11]
M v[8:11],a[76:79],a[132:135],v[8:11]
TR a[146:147],v214 offset:768
M v[12:15],a[88:91],a[128:131],v[12:15]
M v[12:15],a[92:95],a[132:135],v[12:15]
SA m0,s63,0
M v[16:19],a[96:99],a[128:131],v[16:19]
BL v200,s[52:55],s61 offen lds
M v[16:19],a[100:103],a[132:135],v[16:19]
TR a[148:149],v214 offset:8704
M v[20:23],a[112:115],a[128:131],v[20:23]
M v[20:23],a[116:119],a[132:135],v[20:23]
W lgkmcnt(7)
M v[24:27],a[104:107],a[128:131],v[24:27]
W lgkmcnt(6)
M v[24:27],a[108:111],a[132:135],v[24:27]
SA m0,s62,0x400
TR a[150:151],v214 offset:8960
W lgkmcnt(5)
M v[28:31],a[120:123],a[128:131],v[28:31]
BL v193,s[48:51],s60 offen lds
W lgkmcnt(4)
M v[28:31],a[124:127],a[132:135],v[28:31]
M v[32:35],a[64:67],a[136:139],v[32:35]
M v[32:35],a[68:71],a[140:143],v[32:35]
TR a[152:153],v215 offset:512
M v[36:39],a[80:83],a[136:139],v[36:39]
SA m0,s63,0x400
M v[36:39],a[84:87],a[140:143],v[36:39]
BL v201,s[52:55],s61 offen lds
M v[40:43],a[72:75],a[136:139],v[40:43]
M v[40:43],a[76:79],a[140:143],v[40:43]
TR a[154:155],v215 offset:768
M v[44:47],a[88:91],a[136:139],v[44:47]
M v[44:47],a[92:95],a[140:143],v[44:47]
SA m0,s62,0x800
M v[48:51],a[96:99],a[136:139],v[48:51]
BL v194,s[48:51],s60 offen lds
M v[48:51],a[100:103],a[140:143],v[48:51]
TR a[156:157],v215 offset:8704
M v[52:55],a[112:115],a[136:139],v[52:55]
M v[52:55],a[116:119],a[140:143],v[52:55]
M v[56:59],a[104:107],a[136:139],v[56:59]
M v[56:59],a[108:111],a[140:143],v[56:59]
SA m0,s63,0x800
TR a[158:159],v215 offset:8960
M v[60:63],a[120:123],a[136:139],v[60:63]
BL v202,s[52:55],s61 offen lds
M v[60:63],a[124:127],a[140:143],v[60:63]
W lgkmcnt(6)
M v[64:67],a[64:67],a[144:147],v[64:67]
W lgkmcnt(4)
M v[64:67],a[68:71],a[148:151],v[64:67]
TR a[128:129],v214 offset:1024
M v[68:71],a[80:83],a[144:147],v[68:71]
SA m0,s62,0xc00
M v[68:71],a[84:87],a[148:151],v[68:71]
BL v195,s[48:51],s60 offen lds
M v[72:75],a[72:75],a[144:147],v[72:75]
M v[72:75],a[76:79],a[148:151],v[72:75]
TR a[130:131],v214 offset:1280
M v[76:79],a[88:91],a[144:147],v[76:79]
M v[76:79],a[92:95],a[148:151],v[76:79]
SA m0,s63,0xc00
M v[80:83],a[96:99],a[144:147],v[80:83]
BL v203,s[52:55],s61 offen lds
M v[80:83],a[100:103],a[148:151],v[80:83]
TR a[132:133],v214 offset:9216
M v[84:87],a[112:115],a[144:147],v[84:87]
M v[84:87],a[116:119],a[148:151],v[84:87]
M v[88:91],a[104:107],a[144:147],v[88:91]
M v[88:91],a[108:111],a[148:151],v[88:91]
SA m0,s62,0x1000
TR a[134:135],v214 offset:9472
M v[92:95],a[120:123],a[144:147],v[92:95]
BL v196,s[48:51],s60 offen lds
M v[92:95],a[124:127],a[148:151],v[92:95]
W lgkmcnt(6)
M v[96:99],a[64:67],a[152:155],v[96:99]
W lgkmcnt(4)
M v[96:99],a[68:71],a[156:159],v[96:99]
TR a[136:137],v215 offset:1024
M v[100:103],a[80:83],a[152:155],v[100:103]
SA m0,s63,0x1000
M v[100:103],a[84:87],a[156:159],v[100:103]
BL v204,s[52:55],s61 offen lds
M v[104:107],a[72:75],a[152:155],v[104:107]
M v[104:107],a[76:79],a[156:159],v[104:107]
TR a[138:139],v215 offset:1280
M v[108:111],a[88:91],a[152:155],v[108:111]
M v[108:111],a[92:95],a[156:159],v[108:111]
SA m0,s62,0x1400
M v[112:115],a[96:99],a[152:155],v[112:115]
BL v197,s[48:51],s60 offen lds
M v[112:115],a[100:103],a[156:159],v[112:115]
TR a[140:141],v215 offset:9216
M v[116:119],a[112:115],a[152:155],v[116:119]
M v[116:119],a[116:119],a[156:159],v[116:119]
M v[120:123],a[104:107],a[152:155],v[120:123]
M v[120:123],a[108:111],a[156:159],v[120:123]
SA m0,s63,0x1400
TR a[142:143],v215 offset:9472
M v[124:127],a[120:123],a[152:155],v[124:127]
BL v205,s[52:55],s61 offen lds
M v[124:127],a[124:127],a[156:159],v[124:127]
W lgkmcnt(6)
M v[128:131],a[64:67],a[128:131],v[128:131]
W lgkmcnt(4)
M v[128:131],a[68:71],a[132:135],v[128:131]
TR a[144:145],v214 offset:1536
M v[132:135],a[80:83],a[128:131],v[132:135]
SA m0,s62,0x1800
M v[132:135],a[84:87],a[132:135],v[132:135]
BL v198,s[48:51],s60 offen lds
M v[136:139],a[72:75],a[128:131],v[136:139]
M v[136:139],a[76:79],a[132:135],v[136:139]
TR a[146:147],v214 offset:1792
M v[140:143],a[88:91],a[128:131],v[140:143]
M v[140:143],a[92:95],a[132:135],v[140:143]
SA m0,s63,0x1800
M v[144:147],a[96:99],a[128:131],v[144:147]
BL v206,s[52:55],s61 offen lds
M v[144:147],a[100:103],a[132:135],v[144:147]
TR a[148:149],v214 offset:9728
M v[148:151],a[112:115],a[128:131],v[148:151]
M v[148:151],a[116:119],a[132:135],v[148:151]
M v[152:155],a[104:107],a[128:131],v[152:155]
M v[152:155],a[108:111],a[132:135],v[152:155]
SA m0,s62,0x1c00
TR a[150:151],v214 offset:9984
M v[156:159],a[120:123],a[128:131],v[156:159]
BL v199,s[48:51],s60 offen lds
M v[156:159],a[124:127],a[132:135],v[156:159]
W lgkmcnt(6)
M v[160:163],a[64:67],a[136:139],v[160:163]
W lgkmcnt(4)
M v[160:163],a[68:71],a[140:143],v[160:163]
TR a[152:153],v215 offset:1536
M v[164:167],a[80:83],a[136:139],v[164:167]
SA m0,s63,0x1c00
M v[164:167],a[84:87],a[140:143],v[164:167]
BL v207,s[52:55],s61 offen lds
M v[168:171],a[72:75],a[136:139],v[168:171]
SA s60,s60,0x80
M v[168:171],a[76:79],a[140:143],v[168:171]
SA s61,s61,0x80000
TR a[154:155],v215 offset:1792
M v[172:175],a[88:91],a[136:139],v[172:175]
M v[172:175],a[92:95],a[140:143],v[172:175]
M v[176:179],a[96:99],a[136:139],v[176:179]
M v[176:179],a[100:103],a[140:143],v[176:179]
TR a[156:157],v215 offset:9728
M v[180:183],a[112:115],a[136:139],v[180:183]
M v[180:183],a[116:119],a[140:143],v[180:183]
M v[184:187],a[104:107],a[136:139],v[184:187]
M v[184:187],a[108:111],a[140:143],v[184:187]
TR a[158:159],v215 offset:9984
M v[188:191],a[120:123],a[136:139],v[188:191]
M v[188:191],a[124:127],a[140:143],v[188:191]
W lgkmcnt(6)
M a[0:3],a[64:67],a[144:147],a[0:3]
W lgkmcnt(4)
M a[0:3],a[68:71],a[148:151],a[0:3]
M a[4:7],a[80:83],a[144:147],a[4:7]
M a[4:7],a[84:87],a[148:151],a[4:7]
M a[8:11],a[72:75],a[144:147],a[8:11]
M a[8:11],a[76:79],a[148:151],a[8:11]
M a[12:15],a[88:91],a[144:147],a[12:15]
M a[12:15],a[92:95],a[148:151],a[12:15]
M a[16:19],a[96:99],a[144:147],a[16:19]
M a[16:19],a[100:103],a[148:151],a[16:19]
M a[20:23],a[112:115],a[144:147],a[20:23]
M a[20:23],a[116:119],a[148:151],a[20:23]
M a[24:27],a[104:107],a[144:147],a[24:27]
M a[24:27],a[108:111],a[148:151],a[24:27]
M a[28:31],a[120:123],a[144:147],a[28:31]
M a[28:31],a[124:127],a[148:151],a[28:31]
W vmcnt(0) lgkmcnt(0)
s_barrier
M a[32:35],a[64:67],a[152:155],a[32:35]
M a[32:35],a[68:71],a[156:159],a[32:35]
TR a[128:129],v212
M a[36:39],a[80:83],a[152:155],a[36:39]
RD a[64:67],v208
RD a[68:71],v209
M a[36:39],a[84:87],a[156:159],a[36:39]
TR a[136:137],v213
M a[40:43],a[72:75],a[152:155],a[40:43]
RD a[80:83],v208 offset:4096
RD a[84:87],v209 offset:4096
M a[40:43],a[76:79],a[156:159],a[40:43]
TR a[130:131],v212 offset:256
M a[44:47],a[88:91],a[152:155],a[44:47]
RD a[72:75],v208 offset:2048
RD a[76:79],v209 offset:2048
M a[44:47],a[92:95],a[156:159],a[44:47]
TR a[138:139],v213 offset:256
M a[48:51],a[96:99],a[152:155],a[48:51]
RD a[88:91],v208 offset:6144
RD a[92:95],v209 offset:6144
M a[48:51],a[100:103],a[156:159],a[48:51]
TR a[132:133],v212 offset:8192
M a[52:55],a[112:115],a[152:155],a[52:55]
RD a[96:99],v208 offset:8192
RD a[100:103],v209 offset:8192
M a[52:55],a[116:119],a[156:159],a[52:55]
TR a[140:141],v213 offset:8192
M a[56:59],a[104:107],a[152:155],a[56:59]
RD a[112:115],v208 offset:12288
RD a[116:119],v209 offset:12288
M a[56:59],a[108:111],a[156:159],a[56:59]
TR a[134:135],v212 offset:8448
M a[60:63],a[120:123],a[152:155],a[60:63]
RD a[104:107],v208 offset:10240
RD a[108:111],v209 offset:10240
M a[60:63],a[124:127],a[156:159],a[60:63]
TR a[142:143],v213 offset:8448
RD a[120:123],v208 offset:14336
RD a[124:127],v209 offset:14336
W lgkmcnt(15)
M v[0:3],a[64:67],a[128:131],v[0:3]
W lgkmcnt(5)
M v[0:3],a[68:71],a[132:135],v[0:3]
TR a[144:145],v212 offset:512
M v[4:7],a[80:83],a[128:131],v[4:7]
SA m0,s62,0x10000
M v[4:7],a[84:87],a[132:135],v[4:7]
BL v192,s[48:51],s60 offen lds
M v[8:11],a[72:75],a[128:131],v[8:11]
M v[8:11],a[76:79],a[132:135],v[8:11]
TR a[146:147],v212 offset:768
M v[12:15],a[88:91],a[128:131],v[12:15]
M v[12:15],a[92:95],a[132:135],v[12:15]
SA m0,s63,0x10000
M v[16:19],a[96:99],a[128:131],v[16:19]
BL v200,s[52:55],s61 offen lds
M v[16:19],a[100:103],a[132:135],v[16:19]
TR a[148:149],v212 offset:8704
M v[20:23],a[112:115],a[128:131],v[20:23]
M v[20:23],a[116:119],a[132:135],v[20:23]
W lgkmcnt(7)
M v[24:27],a[104:107],a[128:131],v[24:27]
W lgkmcnt(6)
M v[24:27],a[108:111],a[132:135],v[24:27]
SA m0,s62,0x10400
TR a[150:151],v212 offset:8960
W lgkmcnt(5)
M v[28:31],a[120:123],a[128:131],v[28:31]
BL v193,s[48:51],s60 offen lds
W lgkmcnt(4)
M v[28:31],a[124:127],a[132:135],v[28:31]
M v[32:35],a[64:67],a[136:139],v[32:35]
M v[32:35],a[68:71],a[140:143],v[32:35]
TR a[152:153],v213 offset:512
M v[36:39],a[80:83],a[136:139],v[36:39]
SA m0,s63,0x10400
M v[36:39],a[84:87],a[140:143],v[36:39]
BL v201,s[52:55],s61 offen lds
M v[40:43],a[72:75],a[136:139],v[40:43]
M v[40:43],a[76:79],a[140:143],v[40:43]
TR a[154:155],v213 offset:768
M v[44:47],a[88:91],a[136:139],v[44:47]
M v[44:47],a[92:95],a[140:143],v[44:47]
SA m0,s62,0x10800
M v[48:51],a[96:99],a[136:139],v[48:51]
BL v194,s[48:51],s60 offen lds
M v[48:51],a[100:103],a[140:143],v[48:51]
TR a[156:157],v213 offset:8704
M v[52:55],a[112:115],a[136:139],v[52:55]
M v[52:55],a[116:119],a[140:143],v[52:55]
M v[56:59],a[104:107],a[136:139],v[56:59]
M v[56:59],a[108:111],a[140:143],v[56:59]
SA m0,s63,0x10800
TR a[158:159],v213 offset:8960
M v[60:63],a[120:123],a[136:139],v[60:63]
BL v202,s[52:55],s61 offen lds
M v[60:63],a[124:127],a[140:143],v[60:63]
W lgkmcnt(6)
M v[64:67],a[64:67],a[144:147],v[64:67]
W lgkmcnt(4)
M v[64:67],a[68:71],a[148:151],v[64:67]
TR a[128:129],v212 offset:1024
M v[68:71],a[80:83],a[144:147],v[68:71]
SA m0,s62,0x10c00
M v[68:71],a[84:87],a[148:151],v[68:71]
BL v195,s[48:51],s60 offen lds
M v[72:75],a[72:75],a[144:147],v[72:75]
M v[72:75],a[76:79],a[148:151],v[72:75]
TR a[130:131],v212 offset:1280
M v[76:79],a[88:91],a[144:147],v[76:79]
M v[76:79],a[92:95],a[148:151],v[76:79]
)~"
R"~(SA m0,s63,0x10c00
M v[80:83],a[96:99],a[144:147],v[80:83]
BL v203,s[52:55],s61 offen lds
M v[80:83],a[100:103],a[148:151],v[80:83]
TR a[132:133],v212 offset:9216
M v[84:87],a[112:115],a[144:147],v[84:87]
M v[84:87],a[116:119],a[148:151],v[84:87]
M v[88:91],a[104:107],a[144:147],v[88:91]
M v[88:91],a[108:111],a[148:151],v[88:91]
SA m0,s62,0x11000
TR a[134:135],v212 offset:9472
M v[92:95],a[120:123],a[144:147],v[92:95]
BL v196,s[48:51],s60 offen lds
M v[92:95],a[124:127],a[148:151],v[92:95]
W lgkmcnt(6)
M v[96:99],a[64:67],a[152:155],v[96:99]
W lgkmcnt(4)
M v[96:99],a[68:71],a[156:159],v[96:99]
TR a[136:137],v213 offset:1024
M v[100:103],a[80:83],a[152:155],v[100:103]
SA m0,s63,0x11000
M v[100:103],a[84:87],a[156:159],v[100:103]
BL v204,s[52:55],s61 offen lds
M v[104:107],a[72:75],a[152:155],v[104:107]
M v[104:107],a[76:79],a[156:159],v[104:107]
TR a[138:139],v213 offset:1280
M v[108:111],a[88:91],a[152:155],v[108:111]
M v[108:111],a[92:95],a[156:159],v[108:111]
SA m0,s62,0x11400
M v[112:115],a[96:99],a[152:155],v[112:115]
BL v197,s[48:51],s60 offen lds
M v[112:115],a[100:103],a[156:159],v[112:115]
TR a[140:141],v213 offset:9216
M v[116:119],a[112:115],a[152:155],v[116:119]
M v[116:119],a[116:119],a[156:159],v[116:119]
M v[120:123],a[104:107],a[152:155],v[120:123]
M v[120:123],a[108:111],a[156:159],v[120:123]
SA m0,s63,0x11400
TR a[142:143],v213 offset:9472
M v[124:127],a[120:123],a[152:155],v[124:127]
BL v205,s[52:55],s61 offen lds
M v[124:127],a[124:127],a[156:159],v[124:127]
W lgkmcnt(6)
M v[128:131],a[64:67],a[128:131],v[128:131]
W lgkmcnt(4)
M v[128:131],a[68:71],a[132:135],v[128:131]
TR a[144:145],v212 offset:1536
M v[132:135],a[80:83],a[128:131],v[132:135]
SA m0,s62,0x11800
M v[132:135],a[84:87],a[132:135],v[132:135]
BL v198,s[48:51],s60 offen lds
M v[136:139],a[72:75],a[128:131],v[136:139]
M v[136:139],a[76:79],a[132:135],v[136:139]
TR a[146:147],v212 offset:1792
M v[140:143],a[88:91],a[128:131],v[140:143]
M v[140:143],a[92:95],a[132:135],v[140:143]
SA m0,s63,0x11800
M v[144:147],a[96:99],a[128:131],v[144:147]
BL v206,s[52:55],s61 offen lds
M v[144:147],a[100:103],a[132:135],v[144:147]
TR a[148:149],v212 offset:9728
M v[148:151],a[112:115],a[128:131],v[148:151]
M v[148:151],a[116:119],a[132:135],v[148:151]
M v[152:155],a[104:107],a[128:131],v[152:155]
M v[152:155],a[108:111],a[132:135],v[152:155]
SA m0,s62,0x11c00
TR a[150:151],v212 offset:9984
M v[156:159],a[120:123],a[128:131],v[156:159]
BL v199,s[48:51],s60 offen lds
M v[156:159],a[124:127],a[132:135],v[156:159]
W lgkmcnt(6)
M v[160:163],a[64:67],a[136:139],v[160:163]
W lgkmcnt(4)
M v[160:163],a[68:71],a[140:143],v[160:163]
TR a[152:153],v213 offset:1536
M v[164:167],a[80:83],a[136:139],v[164:167]
SA m0,s63,0x11c00
M v[164:167],a[84:87],a[140:143],v[164:167]
BL v207,s[52:55],s61 offen lds
M v[168:171],a[72:75],a[136:139],v[168:171]
SA s60,s60,0x80
M v[168:171],a[76:79],a[140:143],v[168:171]
s_lshl_b32 s90,s37,2
SA s90,s90,s36
s_mov_b64 s[56:57],s[8:9]
s_and_b32 s57,s57,0xffff
s_mov_b32 s58,0x200000
s_mov_b32 s59,0x20000
s_mul_i32 s93,s90,0x800
v_min_u32 v241,15,v255
v_lshlrev_b32 v241,7,v241
SA s61,s61,0x80000
TR a[154:155],v213 offset:1792
M v[172:175],a[88:91],a[136:139],v[172:175]
buffer_load_dword v242,v241,s[56:59],s93 offen
M v[172:175],a[92:95],a[140:143],v[172:175]
M v[176:179],a[96:99],a[136:139],v[176:179]
M v[176:179],a[100:103],a[140:143],v[176:179]
s_mov_b64 s[56:57],s[10:11]
s_and_b32 s57,s57,0xffff
s_mov_b32 s58,0x180000
s_mov_b32 s59,0x20000
s_mul_i32 s93,s90,0x600
v_min_u32 v241,11,v255
v_lshlrev_b32 v241,7,v241
TR a[156:157],v213 offset:9728
M v[180:183],a[112:115],a[136:139],v[180:183]
buffer_load_dword v243,v241,s[56:59],s93 offen
M v[180:183],a[116:119],a[140:143],v[180:183]
M v[184:187],a[104:107],a[136:139],v[184:187]
M v[184:187],a[108:111],a[140:143],v[184:187]
s_mov_b64 s[56:57],s[16:17]
s_and_b32 s57,s57,0xffff
s_mov_b32 s58,0x400000
s_mov_b32 s59,0x20000
s_mul_i32 s93,s90,0x1000
v_min_u32 v241,31,v255
v_lshlrev_b32 v241,7,v241
TR a[158:159],v213 offset:9984
M v[188:191],a[120:123],a[136:139],v[188:191]
buffer_load_dword v237,v241,s[56:59],s93 offen
M v[188:191],a[124:127],a[140:143],v[188:191]
W lgkmcnt(6)
M a[0:3],a[64:67],a[144:147],a[0:3]
W lgkmcnt(4)
M a[0:3],a[68:71],a[148:151],a[0:3]
s_mov_b64 s[56:57],s[22:23]
s_and_b32 s57,s57,0xffff
s_mov_b32 s58,0x400000
s_mov_b32 s59,0x20000
s_mul_i32 s93,s90,0x1000
v_min_u32 v241,31,v255
v_lshlrev_b32 v241,7,v241
M a[4:7],a[80:83],a[144:147],a[4:7]
buffer_load_dword v248,v241,s[56:59],s93 offen
M a[4:7],a[84:87],a[148:151],a[4:7]
M a[8:11],a[72:75],a[144:147],a[8:11]
M a[8:11],a[76:79],a[148:151],a[8:11]
s_mov_b64 s[56:57],s[12:13]
s_and_b32 s57,s57,0xffff
s_mov_b32 s58,0x80000
s_mov_b32 s59,0x20000
s_mul_i32 s93,s90,0x200
v_min_u32 v241,3,v255
v_lshlrev_b32 v241,7,v241
M a[12:15],a[88:91],a[144:147],a[12:15]
buffer_load_dword v249,v241,s[56:59],s93 offen
M a[12:15],a[92:95],a[148:151],a[12:15]
M a[16:19],a[96:99],a[144:147],a[16:19]
M a[16:19],a[100:103],a[148:151],a[16:19]
M a[20:23],a[112:115],a[144:147],a[20:23]
M a[20:23],a[116:119],a[148:151],a[20:23]
M a[24:27],a[104:107],a[144:147],a[24:27]
M a[24:27],a[108:111],a[148:151],a[24:27]
M a[28:31],a[120:123],a[144:147],a[28:31]
M a[28:31],a[124:127],a[148:151],a[28:31]
W vmcnt(5) lgkmcnt(0)
s_barrier
M a[32:35],a[64:67],a[152:155],a[32:35]
M a[32:35],a[68:71],a[156:159],a[32:35]
TR a[128:129],v214
M a[36:39],a[80:83],a[152:155],a[36:39]
RD a[64:67],v210
RD a[68:71],v211
M a[36:39],a[84:87],a[156:159],a[36:39]
TR a[136:137],v215
M a[40:43],a[72:75],a[152:155],a[40:43]
RD a[80:83],v210 offset:4096
RD a[84:87],v211 offset:4096
M a[40:43],a[76:79],a[156:159],a[40:43]
TR a[130:131],v214 offset:256
M a[44:47],a[88:91],a[152:155],a[44:47]
RD a[72:75],v210 offset:2048
RD a[76:79],v211 offset:2048
M a[44:47],a[92:95],a[156:159],a[44:47]
TR a[138:139],v215 offset:256
M a[48:51],a[96:99],a[152:155],a[48:51]
RD a[88:91],v210 offset:6144
RD a[92:95],v211 offset:6144
M a[48:51],a[100:103],a[156:159],a[48:51]
TR a[132:133],v214 offset:8192
M a[52:55],a[112:115],a[152:155],a[52:55]
RD a[96:99],v210 offset:8192
RD a[100:103],v211 offset:8192
M a[52:55],a[116:119],a[156:159],a[52:55]
TR a[140:141],v215 offset:8192
M a[56:59],a[104:107],a[152:155],a[56:59]
RD a[112:115],v210 offset:12288
RD a[116:119],v211 offset:12288
M a[56:59],a[108:111],a[156:159],a[56:59]
TR a[134:135],v214 offset:8448
M a[60:63],a[120:123],a[152:155],a[60:63]
RD a[104:107],v210 offset:10240
RD a[108:111],v211 offset:10240
M a[60:63],a[124:127],a[156:159],a[60:63]
TR a[142:143],v215 offset:8448
RD a[120:123],v210 offset:14336
RD a[124:127],v211 offset:14336
W lgkmcnt(15)
M v[0:3],a[64:67],a[128:131],v[0:3]
W lgkmcnt(5)
M v[0:3],a[68:71],a[132:135],v[0:3]
TR a[144:145],v214 offset:512
M v[4:7],a[80:83],a[128:131],v[4:7]
M v[4:7],a[84:87],a[132:135],v[4:7]
M v[8:11],a[72:75],a[128:131],v[8:11]
M v[8:11],a[76:79],a[132:135],v[8:11]
TR a[146:147],v214 offset:768
M v[12:15],a[88:91],a[128:131],v[12:15]
M v[12:15],a[92:95],a[132:135],v[12:15]
M v[16:19],a[96:99],a[128:131],v[16:19]
M v[16:19],a[100:103],a[132:135],v[16:19]
TR a[148:149],v214 offset:8704
M v[20:23],a[112:115],a[128:131],v[20:23]
M v[20:23],a[116:119],a[132:135],v[20:23]
W lgkmcnt(7)
M v[24:27],a[104:107],a[128:131],v[24:27]
W lgkmcnt(6)
M v[24:27],a[108:111],a[132:135],v[24:27]
TR a[150:151],v214 offset:8960
W lgkmcnt(5)
M v[28:31],a[120:123],a[128:131],v[28:31]
W lgkmcnt(4)
M v[28:31],a[124:127],a[132:135],v[28:31]
M v[32:35],a[64:67],a[136:139],v[32:35]
M v[32:35],a[68:71],a[140:143],v[32:35]
TR a[152:153],v215 offset:512
M v[36:39],a[80:83],a[136:139],v[36:39]
M v[36:39],a[84:87],a[140:143],v[36:39]
M v[40:43],a[72:75],a[136:139],v[40:43]
M v[40:43],a[76:79],a[140:143],v[40:43]
TR a[154:155],v215 offset:768
M v[44:47],a[88:91],a[136:139],v[44:47]
M v[44:47],a[92:95],a[140:143],v[44:47]
M v[48:51],a[96:99],a[136:139],v[48:51]
M v[48:51],a[100:103],a[140:143],v[48:51]
TR a[156:157],v215 offset:8704
M v[52:55],a[112:115],a[136:139],v[52:55]
M v[52:55],a[116:119],a[140:143],v[52:55]
M v[56:59],a[104:107],a[136:139],v[56:59]
M v[56:59],a[108:111],a[140:143],v[56:59]
TR a[158:159],v215 offset:8960
M v[60:63],a[120:123],a[136:139],v[60:63]
M v[60:63],a[124:127],a[140:143],v[60:63]
W lgkmcnt(6)
M v[64:67],a[64:67],a[144:147],v[64:67]
W lgkmcnt(4)
M v[64:67],a[68:71],a[148:151],v[64:67]
TR a[128:129],v214 offset:1024
M v[68:71],a[80:83],a[144:147],v[68:71]
M v[68:71],a[84:87],a[148:151],v[68:71]
M v[72:75],a[72:75],a[144:147],v[72:75]
M v[72:75],a[76:79],a[148:151],v[72:75]
TR a[130:131],v214 offset:1280
M v[76:79],a[88:91],a[144:147],v[76:79]
M v[76:79],a[92:95],a[148:151],v[76:79]
M v[80:83],a[96:99],a[144:147],v[80:83]
M v[80:83],a[100:103],a[148:151],v[80:83]
TR a[132:133],v214 offset:9216
M v[84:87],a[112:115],a[144:147],v[84:87]
M v[84:87],a[116:119],a[148:151],v[84:87]
M v[88:91],a[104:107],a[144:147],v[88:91]
M v[88:91],a[108:111],a[148:151],v[88:91]
TR a[134:135],v214 offset:9472
M v[92:95],a[120:123],a[144:147],v[92:95]
M v[92:95],a[124:127],a[148:151],v[92:95]
W lgkmcnt(6)
M v[96:99],a[64:67],a[152:155],v[96:99]
W lgkmcnt(4)
M v[96:99],a[68:71],a[156:159],v[96:99]
TR a[136:137],v215 offset:1024
M v[100:103],a[80:83],a[152:155],v[100:103]
M v[100:103],a[84:87],a[156:159],v[100:103]
M v[104:107],a[72:75],a[152:155],v[104:107]
M v[104:107],a[76:79],a[156:159],v[104:107]
TR a[138:139],v215 offset:1280
M v[108:111],a[88:91],a[152:155],v[108:111]
M v[108:111],a[92:95],a[156:159],v[108:111]
M v[112:115],a[96:99],a[152:155],v[112:115]
M v[112:115],a[100:103],a[156:159],v[112:115]
TR a[140:141],v215 offset:9216
M v[116:119],a[112:115],a[152:155],v[116:119]
M v[116:119],a[116:119],a[156:159],v[116:119]
M v[120:123],a[104:107],a[152:155],v[120:123]
M v[120:123],a[108:111],a[156:159],v[120:123]
TR a[142:143],v215 offset:9472
M v[124:127],a[120:123],a[152:155],v[124:127]
M v[124:127],a[124:127],a[156:159],v[124:127]
W lgkmcnt(6)
M v[128:131],a[64:67],a[128:131],v[128:131]
W lgkmcnt(4)
M v[128:131],a[68:71],a[132:135],v[128:131]
TR a[144:145],v214 offset:1536
M v[132:135],a[80:83],a[128:131],v[132:135]
M v[132:135],a[84:87],a[132:135],v[132:135]
M v[136:139],a[72:75],a[128:131],v[136:139]
M v[136:139],a[76:79],a[132:135],v[136:139]
TR a[146:147],v214 offset:1792
M v[140:143],a[88:91],a[128:131],v[140:143]
M v[140:143],a[92:95],a[132:135],v[140:143]
M v[144:147],a[96:99],a[128:131],v[144:147]
M v[144:147],a[100:103],a[132:135],v[144:147]
TR a[148:149],v214 offset:9728
M v[148:151],a[112:115],a[128:131],v[148:151]
M v[148:151],a[116:119],a[132:135],v[148:151]
M v[152:155],a[104:107],a[128:131],v[152:155]
M v[152:155],a[108:111],a[132:135],v[152:155]
TR a[150:151],v214 offset:9984
M v[156:159],a[120:123],a[128:131],v[156:159]
M v[156:159],a[124:127],a[132:135],v[156:159]
W lgkmcnt(6)
M v[160:163],a[64:67],a[136:139],v[160:163]
W lgkmcnt(4)
M v[160:163],a[68:71],a[140:143],v[160:163]
TR a[152:153],v215 offset:1536
M v[164:167],a[80:83],a[136:139],v[164:167]
M v[164:167],a[84:87],a[140:143],v[164:167]
M v[168:171],a[72:75],a[136:139],v[168:171]
M v[168:171],a[76:79],a[140:143],v[168:171]
TR a[154:155],v215 offset:1792
M v[172:175],a[88:91],a[136:139],v[172:175]
M v[172:175],a[92:95],a[140:143],v[172:175]
M v[176:179],a[96:99],a[136:139],v[176:179]
M v[176:179],a[100:103],a[140:143],v[176:179]
TR a[156:157],v215 offset:9728
M v[180:183],a[112:115],a[136:139],v[180:183]
M v[180:183],a[116:119],a[140:143],v[180:183]
M v[184:187],a[104:107],a[136:139],v[184:187]
M v[184:187],a[108:111],a[140:143],v[184:187]
TR a[158:159],v215 offset:9984
M v[188:191],a[120:123],a[136:139],v[188:191]
M v[188:191],a[124:127],a[140:143],v[188:191]
W lgkmcnt(6)
M a[0:3],a[64:67],a[144:147],a[0:3]
W lgkmcnt(4)
M a[0:3],a[68:71],a[148:151],a[0:3]
M a[4:7],a[80:83],a[144:147],a[4:7]
M a[4:7],a[84:87],a[148:151],a[4:7]
M a[8:11],a[72:75],a[144:147],a[8:11]
M a[8:11],a[76:79],a[148:151],a[8:11]
M a[12:15],a[88:91],a[144:147],a[12:15]
M a[12:15],a[92:95],a[148:151],a[12:15]
M a[16:19],a[96:99],a[144:147],a[16:19]
M a[16:19],a[100:103],a[148:151],a[16:19]
M a[20:23],a[112:115],a[144:147],a[20:23]
M a[20:23],a[116:119],a[148:151],a[20:23]
M a[24:27],a[104:107],a[144:147],a[24:27]
M a[24:27],a[108:111],a[148:151],a[24:27]
M a[28:31],a[120:123],a[144:147],a[28:31]
M a[28:31],a[124:127],a[148:151],a[28:31]
W lgkmcnt(2)
M a[32:35],a[64:67],a[152:155],a[32:35]
W lgkmcnt(0)
M a[32:35],a[68:71],a[156:159],a[32:35]
M a[36:39],a[80:83],a[152:155],a[36:39]
M a[36:39],a[84:87],a[156:159],a[36:39]
M a[40:43],a[72:75],a[152:155],a[40:43]
M a[40:43],a[76:79],a[156:159],a[40:43]
M a[44:47],a[88:91],a[152:155],a[44:47]
M a[44:47],a[92:95],a[156:159],a[44:47]
M a[48:51],a[96:99],a[152:155],a[48:51]
M a[48:51],a[100:103],a[156:159],a[48:51]
M a[52:55],a[112:115],a[152:155],a[52:55]
M a[52:55],a[116:119],a[156:159],a[52:55]
M a[56:59],a[104:107],a[152:155],a[56:59]
M a[56:59],a[108:111],a[156:159],a[56:59]
M a[60:63],a[120:123],a[152:155],a[60:63]
M a[60:63],a[124:127],a[156:159],a[60:63]
fk_kv_done:
W lgkmcnt(0)
s_barrier
s_mov_b64 s[48:49],s[8:9]
s_and_b32 s49,s49,0xffff
s_mov_b32 s50,0x200000
s_mov_b32 s51,0x20000
s_mov_b64 s[52:53],s[10:11]
s_and_b32 s53,s53,0xffff
s_mov_b32 s54,0x180000
s_mov_b32 s55,0x20000
CV v0,v0,v1
CV v1,v2,v3
CV v2,v4,v5
CV v3,v6,v7
CV v4,v8,v9
CV v5,v10,v11
CV v6,v12,v13
CV v7,v14,v15
SA s88,s68,0
SA m0,s71,0
s_nop 0
BL v216,s[48:51],s88 offen lds
CV v8,v32,v33
CV v9,v34,v35
CV v10,v36,v37
CV v11,v38,v39
CV v12,v40,v41
CV v13,v42,v43
CV v14,v44,v45
CV v15,v46,v47
SA s88,s69,0
SA m0,s72,0
s_nop 0
BL v217,s[48:51],s88 offen lds
CV v16,v16,v17
CV v17,v18,v19
CV v18,v20,v21
CV v19,v22,v23
CV v20,v24,v25
CV v21,v26,v27
CV v22,v28,v29
CV v23,v30,v31
SA s88,s70,0
SA m0,s73,0
s_nop 0
BL v218,s[48:51],s88 offen lds
CV v24,v48,v49
CV v25,v50,v51
CV v26,v52,v53
CV v27,v54,v55
CV v28,v56,v57
CV v29,v58,v59
CV v30,v60,v61
CV v31,v62,v63
SA s89,s74,0
SA m0,s75,0
s_nop 0
BL v219,s[52:55],s89 offen lds
CV v32,v64,v65
CV v33,v66,v67
CV v34,v68,v69
CV v35,v70,v71
CV v36,v72,v73
CV v37,v74,v75
)~"
R"~(CV v38,v76,v77
CV v39,v78,v79
SA m0,s75,0x400
s_nop 0
BL v220,s[52:55],s89 offen lds
CV v40,v96,v97
CV v41,v98,v99
CV v42,v100,v101
CV v43,v102,v103
CV v44,v104,v105
CV v45,v106,v107
CV v46,v108,v109
CV v47,v110,v111
SA m0,s75,0x800
s_nop 0
BL v221,s[52:55],s89 offen lds
CV v48,v80,v81
CV v49,v82,v83
CV v50,v84,v85
CV v51,v86,v87
CV v52,v88,v89
CV v53,v90,v91
CV v54,v92,v93
CV v55,v94,v95
SA m0,s75,0xc00
s_nop 0
BL v222,s[52:55],s89 offen lds
CV v56,v112,v113
CV v57,v114,v115
CV v58,v116,v117
CV v59,v118,v119
CV v60,v120,v121
CV v61,v122,v123
CV v62,v124,v125
CV v63,v126,v127
SA m0,s75,0x1000
s_nop 0
BL v223,s[52:55],s89 offen lds
CV v64,v128,v129
CV v65,v130,v131
CV v66,v132,v133
CV v67,v134,v135
CV v68,v136,v137
CV v69,v138,v139
CV v70,v140,v141
CV v71,v142,v143
SA m0,s75,0x1400
s_nop 0
BL v224,s[52:55],s89 offen lds
CV v72,v160,v161
CV v73,v162,v163
CV v74,v164,v165
CV v75,v166,v167
CV v76,v168,v169
CV v77,v170,v171
CV v78,v172,v173
CV v79,v174,v175
SA s88,s68,0x80
SA m0,s71,0x9000
s_nop 0
BL v216,s[48:51],s88 offen lds
CV v80,v144,v145
CV v81,v146,v147
CV v82,v148,v149
CV v83,v150,v151
CV v84,v152,v153
CV v85,v154,v155
CV v86,v156,v157
CV v87,v158,v159
SA s88,s69,0x80
SA m0,s72,0x9000
s_nop 0
BL v217,s[48:51],s88 offen lds
CV v88,v176,v177
CV v89,v178,v179
CV v90,v180,v181
CV v91,v182,v183
CV v92,v184,v185
CV v93,v186,v187
CV v94,v188,v189
CV v95,v190,v191
SA s88,s70,0x80
SA m0,s73,0x9000
s_nop 0
BL v218,s[48:51],s88 offen lds
AR v192,a0
AR v193,a1
AR v194,a2
AR v195,a3
AR v196,a4
AR v197,a5
AR v198,a6
AR v199,a7
SA s89,s74,0x30000
SA m0,s75,0x9000
s_nop 0
BL v219,s[52:55],s89 offen lds
CV v96,v192,v193
CV v97,v194,v195
CV v98,v196,v197
CV v99,v198,v199
AR v200,a8
AR v201,a9
AR v202,a10
AR v203,a11
SA m0,s75,0x9400
s_nop 0
BL v220,s[52:55],s89 offen lds
AR v204,a12
AR v205,a13
AR v206,a14
AR v207,a15
CV v100,v200,v201
CV v101,v202,v203
CV v102,v204,v205
CV v103,v206,v207
SA m0,s75,0x9800
s_nop 0
BL v221,s[52:55],s89 offen lds
AR v192,a32
AR v193,a33
AR v194,a34
AR v195,a35
AR v196,a36
AR v197,a37
AR v198,a38
AR v199,a39
SA m0,s75,0x9c00
s_nop 0
BL v222,s[52:55],s89 offen lds
CV v104,v192,v193
CV v105,v194,v195
CV v106,v196,v197
CV v107,v198,v199
AR v200,a40
AR v201,a41
AR v202,a42
AR v203,a43
SA m0,s75,0xa000
s_nop 0
BL v223,s[52:55],s89 offen lds
AR v204,a44
AR v205,a45
AR v206,a46
AR v207,a47
CV v108,v200,v201
CV v109,v202,v203
CV v110,v204,v205
CV v111,v206,v207
SA m0,s75,0xa400
s_nop 0
BL v224,s[52:55],s89 offen lds
AR v192,a16
AR v193,a17
AR v194,a18
AR v195,a19
AR v196,a20
AR v197,a21
AR v198,a22
AR v199,a23
SA s88,s68,0x100
SA m0,s71,0x12000
s_nop 0
BL v216,s[48:51],s88 offen lds
CV v112,v192,v193
CV v113,v194,v195
CV v114,v196,v197
CV v115,v198,v199
AR v200,a24
AR v201,a25
AR v202,a26
AR v203,a27
SA s88,s69,0x100
SA m0,s72,0x12000
s_nop 0
BL v217,s[48:51],s88 offen lds
AR v204,a28
AR v205,a29
AR v206,a30
AR v207,a31
CV v116,v200,v201
CV v117,v202,v203
CV v118,v204,v205
CV v119,v206,v207
SA s88,s70,0x100
SA m0,s73,0x12000
s_nop 0
BL v218,s[48:51],s88 offen lds
AR v192,a48
AR v193,a49
AR v194,a50
AR v195,a51
AR v196,a52
AR v197,a53
AR v198,a54
AR v199,a55
SA s89,s74,0x60000
SA m0,s75,0x12000
s_nop 0
BL v219,s[52:55],s89 offen lds
CV v120,v192,v193
CV v121,v194,v195
CV v122,v196,v197
CV v123,v198,v199
AR v200,a56
AR v201,a57
AR v202,a58
AR v203,a59
SA m0,s75,0x12400
s_nop 0
BL v220,s[52:55],s89 offen lds
AR v204,a60
AR v205,a61
AR v206,a62
AR v207,a63
CV v124,v200,v201
CV v125,v202,v203
CV v126,v204,v205
CV v127,v206,v207
SA m0,s75,0x12800
s_nop 0
BL v221,s[52:55],s89 offen lds
AW a128,v64
AW a129,v65
AW a130,v66
AW a131,v67
AW a132,v68
AW a133,v69
AW a134,v70
AW a135,v71
SA m0,s75,0x12c00
s_nop 0
BL v222,s[52:55],s89 offen lds
AW a136,v72
AW a137,v73
AW a138,v74
AW a139,v75
AW a140,v76
AW a141,v77
AW a142,v78
AW a143,v79
SA m0,s75,0x13000
s_nop 0
BL v223,s[52:55],s89 offen lds
AW a144,v80
AW a145,v81
AW a146,v82
AW a147,v83
AW a148,v84
AW a149,v85
AW a150,v86
AW a151,v87
SA m0,s75,0x13400
s_nop 0
BL v224,s[52:55],s89 offen lds
AW a152,v88
AW a153,v89
AW a154,v90
AW a155,v91
AW a156,v92
AW a157,v93
AW a158,v94
AW a159,v95
AW a160,v96
AW a161,v97
AW a162,v98
AW a163,v99
AW a164,v100
AW a165,v101
AW a166,v102
AW a167,v103
AW a168,v104
AW a169,v105
AW a170,v106
AW a171,v107
AW a172,v108
AW a173,v109
AW a174,v110
AW a175,v111
AW a176,v112
AW a177,v113
AW a178,v114
AW a179,v115
AW a180,v116
AW a181,v117
AW a182,v118
AW a183,v119
AW a184,v120
AW a185,v121
AW a186,v122
AW a187,v123
AW a188,v124
AW a189,v125
AW a190,v126
AW a191,v127
v_mov_b32 v188,v48
v_mov_b32 v189,v49
v_mov_b32 v190,v50
v_mov_b32 v191,v51
v_mov_b32 v192,v52
v_mov_b32 v193,v53
v_mov_b32 v194,v54
v_mov_b32 v195,v55
v_mov_b32 v196,v56
v_mov_b32 v197,v57
v_mov_b32 v198,v58
v_mov_b32 v199,v59
v_mov_b32 v200,v60
v_mov_b32 v201,v61
v_mov_b32 v202,v62
v_mov_b32 v203,v63
v_readfirstlane_b32 s84,v251
s_nop 3
s_mul_i32 s42,s84,0x9e3779b1
SA s42,s42,0x7f4a7c15
s_xor_b32 s84,s84,0x5bd1e995
s_mul_i32 s43,s84,0x85ebca6b
SA s43,s43,0x27d4eb2f
s_mov_b64 s[56:57],s[24:25]
s_and_b32 s57,s57,0xffff
s_mov_b32 s58,0x400
s_mov_b32 s59,0x20000
s_lshl_b32 s76,s66,6
s_lshl_b32 s84,s67,3
SA s76,s76,s84
s_lshl_b32 s84,s36,1
SA s76,s76,s84
s_lshl_b32 s85,s76,10
BL v[48:51],v254,s[48:51],s85 offen
BL v[52:55],v254,s[48:51],s85 offen offset:1024
BL v[244:247],v254,s[56:59],0 offen
W vmcnt(21)
s_barrier
TR a[88:89],v233 offset:8192
TR a[90:91],v233 offset:8448
TR a[92:93],v233 offset:12288
TR a[94:95],v233 offset:12544
TR a[96:97],v233 offset:16384
TR a[98:99],v233 offset:16640
TR a[100:101],v233 offset:20480
TR a[102:103],v233 offset:20736
TR a[80:81],v233
TR a[82:83],v233 offset:256
TR a[84:85],v233 offset:4096
TR a[86:87],v233 offset:4352
RD a[0:3],v225
RD a[4:7],v226
RD a[8:11],v225 offset:2048
RD a[12:15],v226 offset:2048
RD a[16:19],v225 offset:4096
RD a[20:23],v226 offset:4096
RD a[24:27],v225 offset:6144
RD a[28:31],v226 offset:6144
RD a[32:35],v225 offset:8192
RD a[36:39],v226 offset:8192
W vmcnt(12) lgkmcnt(0)
s_barrier
M v[80:83],a[88:91],a[0:3],0
SA s88,s68,0x180
SA m0,s71,0x1b000
TR a[112:113],v234 offset:8192
TR a[114:115],v234 offset:8448
v_and_b32 v120,15,v255
v_lshrrev_b32 v121,4,v255
M v[80:83],a[92:95],a[4:7],v[80:83]
BL v216,s[48:51],s88 offen lds
M v[100:103],a[0:3],a[96:99],0
SA s88,s69,0x180
SA m0,s72,0x1b000
TR a[116:117],v234 offset:12288
TR a[118:119],v234 offset:12544
v_lshlrev_b32 v121,2,v121
v_sub_u32 v120,v120,v121
M v[100:103],a[4:7],a[100:103],v[100:103]
BL v217,s[48:51],s88 offen lds
M v[84:87],a[88:91],a[8:11],0
SA s88,s70,0x180
SA m0,s73,0x1b000
TR a[120:121],v234 offset:16384
TR a[122:123],v234 offset:16640
v_mov_b32 v122,0xff800000
v_subrev_u32 v123,0,v120
M v[84:87],a[92:95],a[12:15],v[84:87]
BL v218,s[48:51],s88 offen lds
M v[104:107],a[8:11],a[96:99],0
SA s89,s74,0x90000
SA m0,s75,0x1b000
TR a[124:125],v234 offset:20480
TR a[126:127],v234 offset:20736
v_and_b32 v124,1,v123
v_cmp_le_i32 s[90:91],0,v123
M v[104:107],a[12:15],a[100:103],v[104:107]
BL v219,s[52:55],s89 offen lds
M v[64:67],a[80:83],a[8:11],0
SA m0,s75,0x1b400
TR a[104:105],v234
TR a[106:107],v234 offset:256
v_cmp_eq_u32 s[92:93],0,v124
v_cmp_ge_i32 s[94:95],-2,v123
M v[64:67],a[84:87],a[12:15],v[64:67]
BL v220,s[52:55],s89 offen lds
M v[88:91],a[88:91],a[16:19],0
SA m0,s75,0x1b800
TR a[108:109],v234 offset:4096
TR a[110:111],v234 offset:4352
s_nop 4
s_and_b64 s[90:91],s[90:91],s[92:93]
M v[88:91],a[92:95],a[20:23],v[88:91]
BL v221,s[52:55],s89 offen lds
M v[108:111],a[16:19],a[96:99],0
SA m0,s75,0x1bc00
RD a[40:43],v227
RD a[44:47],v228
s_and_b64 s[94:95],s[94:95],s[92:93]
M v[108:111],a[20:23],a[100:103],v[108:111]
BL v222,s[52:55],s89 offen lds
M v[68:71],a[80:83],a[16:19],0
SA m0,s75,0x1c000
RD a[48:51],v227 offset:2048
RD a[52:55],v228 offset:2048
v_cndmask_b32 v204,v122,0,s[90:91]
v_cndmask_b32 v208,v122,0,s[94:95]
M v[68:71],a[84:87],a[20:23],v[68:71]
BL v223,s[52:55],s89 offen lds
M v[92:95],a[88:91],a[24:27],0
SA m0,s75,0x1c400
RD a[56:59],v227 offset:4096
RD a[60:63],v228 offset:4096
v_subrev_u32 v123,1,v120
v_and_b32 v124,1,v123
M v[92:95],a[92:95],a[28:31],v[92:95]
BL v224,s[52:55],s89 offen lds
M v[112:115],a[24:27],a[96:99],0
SA s82,s64,0
RD a[64:67],v227 offset:6144
RD a[68:71],v228 offset:6144
v_cmp_le_i32 s[90:91],0,v123
v_cmp_eq_u32 s[92:93],0,v124
M v[112:115],a[28:31],a[100:103],v[112:115]
BS v[0:3],v254,s[44:47],s82 offen sc1
M v[72:75],a[80:83],a[24:27],0
RD a[72:75],v227 offset:8192
RD a[76:79],v228 offset:8192
v_cmp_ge_i32 s[94:95],-2,v123
s_nop 4
M v[72:75],a[84:87],a[28:31],v[72:75]
M v[96:99],a[88:91],a[32:35],0
s_and_b64 s[90:91],s[90:91],s[92:93]
s_and_b64 s[94:95],s[94:95],s[92:93]
M v[96:99],a[92:95],a[36:39],v[96:99]
M v[116:119],a[32:35],a[96:99],0
v_cndmask_b32 v205,v122,0,s[90:91]
v_cndmask_b32 v209,v122,0,s[94:95]
M v[116:119],a[36:39],a[100:103],v[116:119]
M v[76:79],a[80:83],a[32:35],0
v_subrev_u32 v123,2,v120
M v[76:79],a[84:87],a[36:39],v[76:79]
W vmcnt(13) lgkmcnt(0)
s_barrier
M v[80:83],a[112:115],a[40:43],v[80:83]
SA s88,s68,0x200
SA m0,s71,0
TR a[88:89],v235 offset:8192
TR a[90:91],v235 offset:8448
v_and_b32 v124,1,v123
v_cmp_le_i32 s[90:91],0,v123
M v[80:83],a[116:119],a[44:47],v[80:83]
BL v216,s[48:51],s88 offen lds
M v[100:103],a[40:43],a[120:123],v[100:103]
SA s88,s69,0x200
SA m0,s72,0
TR a[92:93],v235 offset:12288
TR a[94:95],v235 offset:12544
v_cmp_eq_u32 s[92:93],0,v124
v_cmp_ge_i32 s[94:95],-2,v123
M v[100:103],a[44:47],a[124:127],v[100:103]
BL v217,s[48:51],s88 offen lds
M v[84:87],a[112:115],a[48:51],v[84:87]
SA s88,s70,0x200
SA m0,s73,0
TR a[96:97],v235 offset:16384
TR a[98:99],v235 offset:16640
s_nop 4
s_and_b64 s[90:91],s[90:91],s[92:93]
M v[84:87],a[116:119],a[52:55],v[84:87]
BL v218,s[48:51],s88 offen lds
M v[104:107],a[48:51],a[120:123],v[104:107]
SA s89,s74,0xc0000
SA m0,s75,0
TR a[100:101],v235 offset:20480
TR a[102:103],v235 offset:20736
s_and_b64 s[94:95],s[94:95],s[92:93]
v_cndmask_b32 v206,v122,0,s[90:91]
M v[104:107],a[52:55],a[124:127],v[104:107]
BL v219,s[52:55],s89 offen lds
M v[64:67],a[104:107],a[48:51],v[64:67]
SA m0,s75,0x400
TR a[80:81],v235
TR a[82:83],v235 offset:256
v_cndmask_b32 v210,v122,0,s[94:95]
v_subrev_u32 v123,3,v120
M v[64:67],a[108:111],a[52:55],v[64:67]
BL v220,s[52:55],s89 offen lds
M v[88:91],a[112:115],a[56:59],v[88:91]
SA m0,s75,0x800
TR a[84:85],v235 offset:4096
TR a[86:87],v235 offset:4352
v_and_b32 v124,1,v123
v_cmp_le_i32 s[90:91],0,v123
M v[88:91],a[116:119],a[60:63],v[88:91]
BL v221,s[52:55],s89 offen lds
M v[108:111],a[56:59],a[120:123],v[108:111]
SA m0,s75,0xc00
RD a[0:3],v229
RD a[4:7],v230
v_cmp_eq_u32 s[92:93],0,v124
M v[108:111],a[60:63],a[124:127],v[108:111]
BL v222,s[52:55],s89 offen lds
M v[68:71],a[104:107],a[56:59],v[68:71]
SA m0,s75,0x1000
RD a[8:11],v229 offset:2048
RD a[12:15],v230 offset:2048
v_cmp_ge_i32 s[94:95],-2,v123
s_nop 4
M v[68:71],a[108:111],a[60:63],v[68:71]
BL v223,s[52:55],s89 offen lds
M v[92:95],a[112:115],a[64:67],v[92:95]
SA m0,s75,0x1400
RD a[16:19],v229 offset:4096
RD a[20:23],v230 offset:4096
s_and_b64 s[90:91],s[90:91],s[92:93]
s_and_b64 s[94:95],s[94:95],s[92:93]
M v[92:95],a[116:119],a[68:71],v[92:95]
BL v224,s[52:55],s89 offen lds
M v[112:115],a[64:67],a[120:123],v[112:115]
SA s82,s64,0
RD a[24:27],v229 offset:6144
RD a[28:31],v230 offset:6144
v_cndmask_b32 v207,v122,0,s[90:91]
v_cndmask_b32 v211,v122,0,s[94:95]
M v[112:115],a[68:71],a[124:127],v[112:115]
BS v[4:7],v254,s[44:47],s82 offen offset:1024 sc1
M v[72:75],a[104:107],a[64:67],v[72:75]
RD a[32:35],v229 offset:8192
RD a[36:39],v230 offset:8192
s_min_u32 s96,s66,1
s_sub_u32 s96,s96,1
M v[72:75],a[108:111],a[68:71],v[72:75]
M v[96:99],a[112:115],a[72:75],v[96:99]
s_and_b32 s96,s96,0xff800000
v_or_b32 v212,s96,v208
M v[96:99],a[116:119],a[76:79],v[96:99]
M v[116:119],a[72:75],a[120:123],v[116:119]
v_or_b32 v213,s96,v209
v_or_b32 v214,s96,v210
M v[116:119],a[76:79],a[124:127],v[116:119]
M v[76:79],a[104:107],a[72:75],v[76:79]
v_or_b32 v215,s96,v211
M v[76:79],a[108:111],a[76:79],v[76:79]
W vmcnt(11) lgkmcnt(0)
s_barrier
M v[80:83],a[88:91],a[0:3],v[80:83]
SA s88,s68,0x280
SA m0,s71,0x9000
TR a[112:113],v236 offset:8192
TR a[114:115],v236 offset:8448
W vmcnt(20)
v_lshlrev_b32 v180,16,v244
M v[80:83],a[92:95],a[4:7],v[80:83]
BL v216,s[48:51],s88 offen lds
M v[100:103],a[0:3],a[96:99],v[100:103]
SA s88,s69,0x280
SA m0,s72,0x9000
TR a[116:117],v236 offset:12288
TR a[118:119],v236 offset:12544
v_and_b32 v181,0xffff0000,v244
v_lshlrev_b32 v182,16,v245
M v[100:103],a[4:7],a[100:103],v[100:103]
BL v217,s[48:51],s88 offen lds
M v[84:87],a[88:91],a[8:11],v[84:87]
SA s88,s70,0x280
SA m0,s73,0x9000
TR a[120:121],v236 offset:16384
TR a[122:123],v236 offset:16640
v_and_b32 v183,0xffff0000,v245
v_lshlrev_b32 v184,16,v246
M v[84:87],a[92:95],a[12:15],v[84:87]
BL v218,s[48:51],s88 offen lds
M v[104:107],a[8:11],a[96:99],v[104:107]
SA s89,s74,0xf0000
SA m0,s75,0x9000
TR a[124:125],v236 offset:20480
TR a[126:127],v236 offset:20736
v_and_b32 v185,0xffff0000,v246
v_lshlrev_b32 v186,16,v247
M v[104:107],a[12:15],a[100:103],v[104:107]
BL v219,s[52:55],s89 offen lds
M v[64:67],a[80:83],a[8:11],v[64:67]
SA m0,s75,0x9400
TR a[104:105],v236
TR a[106:107],v236 offset:256
v_and_b32 v187,0xffff0000,v247
v_lshlrev_b32 v176,16,v48
M v[64:67],a[84:87],a[12:15],v[64:67]
BL v220,s[52:55],s89 offen lds
M v[88:91],a[88:91],a[16:19],v[88:91]
SA m0,s75,0x9800
TR a[108:109],v236 offset:4096
TR a[110:111],v236 offset:4352
v_mul_f32 v126,v176,v180
v_and_b32 v178,0xffff0000,v48
M v[88:91],a[92:95],a[20:23],v[88:91]
BL v221,s[52:55],s89 offen lds
M v[108:111],a[16:19],a[96:99],v[108:111]
SA m0,s75,0x9c00
RD a[40:43],v231
RD a[44:47],v232
v_fmac_f32 v126,v178,v181
M v[108:111],a[20:23],a[100:103],v[108:111]
BL v222,s[52:55],s89 offen lds
M v[68:71],a[80:83],a[16:19],v[68:71]
SA m0,s75,0xa000
RD a[48:51],v231 offset:2048
RD a[52:55],v232 offset:2048
v_lshlrev_b32 v176,16,v49
v_fmac_f32 v126,v176,v182
M v[68:71],a[84:87],a[20:23],v[68:71]
BL v223,s[52:55],s89 offen lds
M v[92:95],a[88:91],a[24:27],v[92:95]
SA m0,s75,0xa400
RD a[56:59],v231 offset:4096
)~"
R"~(RD a[60:63],v232 offset:4096
v_and_b32 v178,0xffff0000,v49
v_fmac_f32 v126,v178,v183
M v[92:95],a[92:95],a[28:31],v[92:95]
BL v224,s[52:55],s89 offen lds
M v[112:115],a[24:27],a[96:99],v[112:115]
SA s82,s64,0
RD a[64:67],v231 offset:6144
RD a[68:71],v232 offset:6144
v_lshlrev_b32 v176,16,v50
v_fmac_f32 v126,v176,v184
M v[112:115],a[28:31],a[100:103],v[112:115]
BS v[8:11],v254,s[44:47],s82 offen offset:2048 sc1
M v[72:75],a[80:83],a[24:27],v[72:75]
RD a[72:75],v231 offset:8192
RD a[76:79],v232 offset:8192
v_and_b32 v178,0xffff0000,v50
v_fmac_f32 v126,v178,v185
M v[72:75],a[84:87],a[28:31],v[72:75]
M v[96:99],a[88:91],a[32:35],v[96:99]
v_lshlrev_b32 v176,16,v51
v_fmac_f32 v126,v176,v186
M v[96:99],a[92:95],a[36:39],v[96:99]
M v[116:119],a[32:35],a[96:99],v[116:119]
v_and_b32 v178,0xffff0000,v51
v_fmac_f32 v126,v178,v187
M v[116:119],a[36:39],a[100:103],v[116:119]
M v[76:79],a[80:83],a[32:35],v[76:79]
v_lshlrev_b32 v177,16,v52
M v[76:79],a[84:87],a[36:39],v[76:79]
W vmcnt(11) lgkmcnt(0)
s_barrier
M v[80:83],a[112:115],a[40:43],v[80:83]
SA s88,s68,0x300
SA m0,s71,0x12000
TR a[88:89],v233 offset:8192
TR a[90:91],v233 offset:8448
v_mul_f32 v127,v177,v180
v_and_b32 v179,0xffff0000,v52
M v[80:83],a[116:119],a[44:47],v[80:83]
BL v216,s[48:51],s88 offen lds
M v[100:103],a[40:43],a[120:123],v[100:103]
SA s88,s69,0x300
SA m0,s72,0x12000
TR a[92:93],v233 offset:12288
TR a[94:95],v233 offset:12544
v_fmac_f32 v127,v179,v181
v_lshlrev_b32 v177,16,v53
M v[100:103],a[44:47],a[124:127],v[100:103]
BL v217,s[48:51],s88 offen lds
M v[84:87],a[112:115],a[48:51],v[84:87]
SA s88,s70,0x300
SA m0,s73,0x12000
TR a[96:97],v233 offset:16384
TR a[98:99],v233 offset:16640
v_fmac_f32 v127,v177,v182
v_and_b32 v179,0xffff0000,v53
M v[84:87],a[116:119],a[52:55],v[84:87]
BL v218,s[48:51],s88 offen lds
M v[104:107],a[48:51],a[120:123],v[104:107]
SA s89,s74,0x120000
SA m0,s75,0x12000
TR a[100:101],v233 offset:20480
TR a[102:103],v233 offset:20736
v_fmac_f32 v127,v179,v183
v_lshlrev_b32 v177,16,v54
M v[104:107],a[52:55],a[124:127],v[104:107]
BL v219,s[52:55],s89 offen lds
M v[64:67],a[104:107],a[48:51],v[64:67]
SA m0,s75,0x12400
TR a[80:81],v233
TR a[82:83],v233 offset:256
v_fmac_f32 v127,v177,v184
v_and_b32 v179,0xffff0000,v54
M v[64:67],a[108:111],a[52:55],v[64:67]
BL v220,s[52:55],s89 offen lds
M v[88:91],a[112:115],a[56:59],v[88:91]
SA m0,s75,0x12800
TR a[84:85],v233 offset:4096
TR a[86:87],v233 offset:4352
v_fmac_f32 v127,v179,v185
v_lshlrev_b32 v177,16,v55
M v[88:91],a[116:119],a[60:63],v[88:91]
BL v221,s[52:55],s89 offen lds
M v[108:111],a[56:59],a[120:123],v[108:111]
SA m0,s75,0x12c00
RD a[0:3],v225
RD a[4:7],v226
v_fmac_f32 v127,v177,v186
M v[108:111],a[60:63],a[124:127],v[108:111]
BL v222,s[52:55],s89 offen lds
M v[68:71],a[104:107],a[56:59],v[68:71]
SA m0,s75,0x13000
RD a[8:11],v225 offset:2048
RD a[12:15],v226 offset:2048
v_and_b32 v179,0xffff0000,v55
v_fmac_f32 v127,v179,v187
M v[68:71],a[108:111],a[60:63],v[68:71]
BL v223,s[52:55],s89 offen lds
M v[92:95],a[112:115],a[64:67],v[92:95]
SA m0,s75,0x13400
RD a[16:19],v225 offset:4096
RD a[20:23],v226 offset:4096
v_add_f32_dpp v126,v126,v126 row_ror:8 row_mask:0xf bank_mask:0xf
v_add_f32_dpp v127,v127,v127 row_ror:8 row_mask:0xf bank_mask:0xf
M v[92:95],a[116:119],a[68:71],v[92:95]
BL v224,s[52:55],s89 offen lds
M v[112:115],a[64:67],a[120:123],v[112:115]
SA s82,s64,0
RD a[24:27],v225 offset:6144
RD a[28:31],v226 offset:6144
v_add_f32_dpp v126,v126,v126 row_ror:4 row_mask:0xf bank_mask:0xf
v_add_f32_dpp v127,v127,v127 row_ror:4 row_mask:0xf bank_mask:0xf
M v[112:115],a[68:71],a[124:127],v[112:115]
BS v[12:15],v254,s[44:47],s82 offen offset:3072 sc1
M v[72:75],a[104:107],a[64:67],v[72:75]
RD a[32:35],v225 offset:8192
RD a[36:39],v226 offset:8192
v_add_f32_dpp v126,v126,v126 row_ror:2 row_mask:0xf bank_mask:0xf
v_add_f32_dpp v127,v127,v127 row_ror:2 row_mask:0xf bank_mask:0xf
M v[72:75],a[108:111],a[68:71],v[72:75]
M v[96:99],a[112:115],a[72:75],v[96:99]
v_add_f32_dpp v126,v126,v126 row_ror:1 row_mask:0xf bank_mask:0xf
v_add_f32_dpp v127,v127,v127 row_ror:1 row_mask:0xf bank_mask:0xf
M v[96:99],a[116:119],a[76:79],v[96:99]
M v[116:119],a[72:75],a[120:123],v[116:119]
v_mov_b32 v176,v126
v_mov_b32 v177,v127
M v[116:119],a[76:79],a[124:127],v[116:119]
M v[76:79],a[104:107],a[72:75],v[76:79]
v_permlane16_swap_b32 v126,v176
M v[76:79],a[108:111],a[76:79],v[76:79]
W vmcnt(11) lgkmcnt(0)
s_barrier
M v[80:83],a[88:91],a[0:3],v[80:83]
SA s88,s68,0x380
SA m0,s71,0x1b000
TR a[112:113],v234 offset:8192
TR a[114:115],v234 offset:8448
v_permlane16_swap_b32 v127,v177
v_add_f32 v126,v126,v176
M v[80:83],a[92:95],a[4:7],v[80:83]
BL v216,s[48:51],s88 offen lds
M v[100:103],a[0:3],a[96:99],v[100:103]
SA s88,s69,0x380
SA m0,s72,0x1b000
TR a[116:117],v234 offset:12288
TR a[118:119],v234 offset:12544
v_add_f32 v127,v127,v177
M v[100:103],a[4:7],a[100:103],v[100:103]
BL v217,s[48:51],s88 offen lds
M v[84:87],a[88:91],a[8:11],v[84:87]
SA s88,s70,0x380
SA m0,s73,0x1b000
TR a[120:121],v234 offset:16384
TR a[122:123],v234 offset:16640
v_mov_b32 v176,v126
v_mov_b32 v177,v127
M v[84:87],a[92:95],a[12:15],v[84:87]
BL v218,s[48:51],s88 offen lds
M v[104:107],a[8:11],a[96:99],v[104:107]
SA s89,s74,0x150000
SA m0,s75,0x1b000
TR a[124:125],v234 offset:20480
TR a[126:127],v234 offset:20736
v_permlane32_swap_b32 v126,v176
M v[104:107],a[12:15],a[100:103],v[104:107]
BL v219,s[52:55],s89 offen lds
M v[64:67],a[80:83],a[8:11],v[64:67]
SA m0,s75,0x1b400
TR a[104:105],v234
TR a[106:107],v234 offset:256
v_permlane32_swap_b32 v127,v177
v_add_f32 v126,v126,v176
M v[64:67],a[84:87],a[12:15],v[64:67]
BL v220,s[52:55],s89 offen lds
M v[88:91],a[88:91],a[16:19],v[88:91]
SA m0,s75,0x1b800
TR a[108:109],v234 offset:4096
TR a[110:111],v234 offset:4352
v_add_f32 v127,v127,v177
M v[88:91],a[92:95],a[20:23],v[88:91]
BL v221,s[52:55],s89 offen lds
M v[108:111],a[16:19],a[96:99],v[108:111]
SA m0,s75,0x1bc00
RD a[40:43],v227
RD a[44:47],v228
v_mul_f32 v126,0xbfb8aa3b,v126
v_mul_f32 v127,0xbfb8aa3b,v127
M v[108:111],a[20:23],a[100:103],v[108:111]
BL v222,s[52:55],s89 offen lds
M v[68:71],a[80:83],a[16:19],v[68:71]
SA m0,s75,0x1c000
RD a[48:51],v227 offset:2048
RD a[52:55],v228 offset:2048
EX v126,v126
M v[68:71],a[84:87],a[20:23],v[68:71]
BL v223,s[52:55],s89 offen lds
M v[92:95],a[88:91],a[24:27],v[92:95]
SA m0,s75,0x1c400
RD a[56:59],v227 offset:4096
RD a[60:63],v228 offset:4096
EX v127,v127
s_lshl_b32 s96,s76,2
M v[92:95],a[92:95],a[28:31],v[92:95]
BL v224,s[52:55],s89 offen lds
M v[112:115],a[24:27],a[96:99],v[112:115]
SA s82,s64,0x4000
RD a[64:67],v227 offset:6144
RD a[68:71],v228 offset:6144
SA s96,s96,0x3400000
M v[112:115],a[28:31],a[100:103],v[112:115]
BS v[16:19],v254,s[44:47],s82 offen sc1
M v[72:75],a[80:83],a[24:27],v[72:75]
RD a[72:75],v227 offset:8192
RD a[76:79],v228 offset:8192
v_add_f32 v126,1.0,v126
v_add_f32 v127,1.0,v127
M v[72:75],a[84:87],a[28:31],v[72:75]
M v[96:99],a[88:91],a[32:35],v[96:99]
v_rcp_f32 v126,v126
M v[96:99],a[92:95],a[36:39],v[96:99]
M v[116:119],a[32:35],a[96:99],v[116:119]
v_rcp_f32 v127,v127
s_nop 0
M v[116:119],a[36:39],a[100:103],v[116:119]
M v[76:79],a[80:83],a[32:35],v[76:79]
buffer_store_dwordx2 v[126:127],v250,s[44:47],s96 offen sc1
M v[76:79],a[84:87],a[36:39],v[76:79]
s_lshl_b32 s84,s41,1
SA s84,s84,s40
s_lshl_b32 s62,s84,13
s_sub_u32 s85,15,s39
s_cmp_eq_u32 s41,0
s_cselect_b32 s85,s85,s39
s_lshl_b32 s85,s85,7
s_lshl_b32 s86,s40,6
SA s85,s85,s86
s_lshl_b32 s60,s85,10
s_lshl_b32 s63,s36,12
SA s63,s63,0x8000
s_lshl_b32 s61,s36,17
s_lshl_b32 s86,s38,8
SA s61,s61,s86
s_mov_b64 s[52:53],s[16:17]
s_and_b32 s53,s53,0xffff
s_mov_b32 s54,0x400000
s_mov_b32 s55,0x20000
v_lshrrev_b32 v140,3,v255
v_and_b32 v141,7,v255
v_lshrrev_b32 v142,1,v140
v_xor_b32 v141,v141,v142
v_mul_u32_u24 v140,0x400,v140
v_xor_b32 v143,0,v141
v_lshl_add_u32 v143,v143,4,v140
v_add_u32 v128,0,v143
v_xor_b32 v143,4,v141
v_lshl_add_u32 v143,v143,4,v140
v_add_u32 v129,0x2000,v143
v_xor_b32 v143,0,v141
v_lshl_add_u32 v143,v143,4,v140
v_add_u32 v130,0x4000,v143
v_xor_b32 v143,4,v141
v_lshl_add_u32 v143,v143,4,v140
v_add_u32 v131,0x6000,v143
v_xor_b32 v143,0,v141
v_lshl_add_u32 v143,v143,4,v140
v_add_u32 v132,0x8000,v143
v_xor_b32 v143,4,v141
v_lshl_add_u32 v143,v143,4,v140
v_add_u32 v133,0xa000,v143
v_xor_b32 v143,0,v141
v_lshl_add_u32 v143,v143,4,v140
v_add_u32 v134,0xc000,v143
v_xor_b32 v143,4,v141
v_lshl_add_u32 v143,v143,4,v140
v_add_u32 v135,0xe000,v143
v_bfe_u32 v140,v255,2,3
v_mul_u32_u24 v140,0x2000,v140
v_lshrrev_b32 v141,5,v255
v_lshl_add_u32 v140,v141,6,v140
v_and_b32 v141,3,v255
v_xor_b32 v142,0,v141
v_lshl_add_u32 v142,v142,4,v140
v_add_u32 v136,0,v142
v_xor_b32 v142,0,v141
v_lshl_add_u32 v142,v142,4,v140
v_add_u32 v137,0x80,v142
v_xor_b32 v142,2,v141
v_lshl_add_u32 v142,v142,4,v140
v_add_u32 v138,0x10000,v142
v_xor_b32 v142,2,v141
v_lshl_add_u32 v142,v142,4,v140
v_add_u32 v139,0x10080,v142
W vmcnt(12) lgkmcnt(0)
s_barrier
M v[80:83],a[112:115],a[40:43],v[80:83]
SA s77,s60,0
SA m0,s62,0
TR a[88:89],v235 offset:8192
TR a[90:91],v235 offset:8448
M v[80:83],a[116:119],a[44:47],v[80:83]
BL v128,s[48:51],s77 offen lds
M v[100:103],a[40:43],a[120:123],v[100:103]
SA m0,s62,0x400
TR a[92:93],v235 offset:12288
TR a[94:95],v235 offset:12544
M v[100:103],a[44:47],a[124:127],v[100:103]
BL v129,s[48:51],s77 offen lds
M v[84:87],a[112:115],a[48:51],v[84:87]
SA m0,s62,0x800
TR a[96:97],v235 offset:16384
TR a[98:99],v235 offset:16640
M v[84:87],a[116:119],a[52:55],v[84:87]
BL v130,s[48:51],s77 offen lds
M v[104:107],a[48:51],a[120:123],v[104:107]
SA m0,s62,0xc00
TR a[100:101],v235 offset:20480
TR a[102:103],v235 offset:20736
M v[104:107],a[52:55],a[124:127],v[104:107]
BL v131,s[48:51],s77 offen lds
M v[64:67],a[104:107],a[48:51],v[64:67]
SA m0,s62,0x1000
TR a[80:81],v235
TR a[82:83],v235 offset:256
M v[64:67],a[108:111],a[52:55],v[64:67]
BL v132,s[48:51],s77 offen lds
M v[88:91],a[112:115],a[56:59],v[88:91]
SA m0,s62,0x1400
TR a[84:85],v235 offset:4096
TR a[86:87],v235 offset:4352
M v[88:91],a[116:119],a[60:63],v[88:91]
BL v133,s[48:51],s77 offen lds
M v[108:111],a[56:59],a[120:123],v[108:111]
SA m0,s62,0x1800
RD a[0:3],v229
RD a[4:7],v230
M v[108:111],a[60:63],a[124:127],v[108:111]
BL v134,s[48:51],s77 offen lds
M v[68:71],a[104:107],a[56:59],v[68:71]
SA m0,s62,0x1c00
RD a[8:11],v229 offset:2048
RD a[12:15],v230 offset:2048
M v[68:71],a[108:111],a[60:63],v[68:71]
BL v135,s[48:51],s77 offen lds
M v[92:95],a[112:115],a[64:67],v[92:95]
SA s78,s61,0
SA m0,s63,0
RD a[16:19],v229 offset:4096
RD a[20:23],v230 offset:4096
M v[92:95],a[116:119],a[68:71],v[92:95]
BL v136,s[52:55],s78 offen lds
M v[112:115],a[64:67],a[120:123],v[112:115]
SA m0,s63,0x400
RD a[24:27],v229 offset:6144
RD a[28:31],v230 offset:6144
M v[112:115],a[68:71],a[124:127],v[112:115]
BL v137,s[52:55],s78 offen lds
M v[72:75],a[104:107],a[64:67],v[72:75]
SA m0,s63,0x800
RD a[32:35],v229 offset:8192
RD a[36:39],v230 offset:8192
M v[72:75],a[108:111],a[68:71],v[72:75]
BL v138,s[52:55],s78 offen lds
M v[96:99],a[112:115],a[72:75],v[96:99]
SA m0,s63,0xc00
M v[96:99],a[116:119],a[76:79],v[96:99]
BL v139,s[52:55],s78 offen lds
M v[116:119],a[72:75],a[120:123],v[116:119]
SA s82,s64,0x4000
M v[116:119],a[76:79],a[124:127],v[116:119]
BS v[20:23],v254,s[44:47],s82 offen offset:1024 sc1
M v[76:79],a[104:107],a[72:75],v[76:79]
M v[76:79],a[108:111],a[76:79],v[76:79]
W vmcnt(15) lgkmcnt(0)
s_barrier
M v[80:83],a[88:91],a[0:3],v[80:83]
SA s77,s60,0x80
SA m0,s62,0xc000
TR a[112:113],v236 offset:8192
TR a[114:115],v236 offset:8448
M v[80:83],a[92:95],a[4:7],v[80:83]
BL v128,s[48:51],s77 offen lds
M v[100:103],a[0:3],a[96:99],v[100:103]
SA m0,s62,0xc400
TR a[116:117],v236 offset:12288
TR a[118:119],v236 offset:12544
M v[100:103],a[4:7],a[100:103],v[100:103]
BL v129,s[48:51],s77 offen lds
M v[84:87],a[88:91],a[8:11],v[84:87]
SA m0,s62,0xc800
TR a[120:121],v236 offset:16384
TR a[122:123],v236 offset:16640
M v[84:87],a[92:95],a[12:15],v[84:87]
BL v130,s[48:51],s77 offen lds
M v[104:107],a[8:11],a[96:99],v[104:107]
SA m0,s62,0xcc00
TR a[124:125],v236 offset:20480
TR a[126:127],v236 offset:20736
M v[104:107],a[12:15],a[100:103],v[104:107]
BL v131,s[48:51],s77 offen lds
M v[64:67],a[80:83],a[8:11],v[64:67]
SA m0,s62,0xd000
TR a[104:105],v236
TR a[106:107],v236 offset:256
M v[64:67],a[84:87],a[12:15],v[64:67]
BL v132,s[48:51],s77 offen lds
M v[88:91],a[88:91],a[16:19],v[88:91]
SA m0,s62,0xd400
TR a[108:109],v236 offset:4096
TR a[110:111],v236 offset:4352
M v[88:91],a[92:95],a[20:23],v[88:91]
BL v133,s[48:51],s77 offen lds
M v[108:111],a[16:19],a[96:99],v[108:111]
SA m0,s62,0xd800
RD a[40:43],v231
RD a[44:47],v232
M v[108:111],a[20:23],a[100:103],v[108:111]
BL v134,s[48:51],s77 offen lds
M v[68:71],a[80:83],a[16:19],v[68:71]
SA m0,s62,0xdc00
RD a[48:51],v231 offset:2048
RD a[52:55],v232 offset:2048
M v[68:71],a[84:87],a[20:23],v[68:71]
BL v135,s[48:51],s77 offen lds
M v[92:95],a[88:91],a[24:27],v[92:95]
SA s78,s61,0x80000
SA m0,s63,0xc000
RD a[56:59],v231 offset:4096
RD a[60:63],v232 offset:4096
M v[92:95],a[92:95],a[28:31],v[92:95]
BL v136,s[52:55],s78 offen lds
M v[112:115],a[24:27],a[96:99],v[112:115]
SA m0,s63,0xc400
RD a[64:67],v231 offset:6144
RD a[68:71],v232 offset:6144
M v[112:115],a[28:31],a[100:103],v[112:115]
BL v137,s[52:55],s78 offen lds
M v[72:75],a[80:83],a[24:27],v[72:75]
SA m0,s63,0xc800
RD a[72:75],v231 offset:8192
RD a[76:79],v232 offset:8192
M v[72:75],a[84:87],a[28:31],v[72:75]
BL v138,s[52:55],s78 offen lds
M v[96:99],a[88:91],a[32:35],v[96:99]
SA m0,s63,0xcc00
M v[96:99],a[92:95],a[36:39],v[96:99]
BL v139,s[52:55],s78 offen lds
M v[116:119],a[32:35],a[96:99],v[116:119]
SA s82,s64,0x4000
M v[116:119],a[36:39],a[100:103],v[116:119]
BS v[24:27],v254,s[44:47],s82 offen offset:2048 sc1
M v[76:79],a[80:83],a[32:35],v[76:79]
M v[76:79],a[84:87],a[36:39],v[76:79]
W lgkmcnt(0)
s_barrier
M v[80:83],a[112:115],a[40:43],v[80:83]
SA s77,s60,0x100
SA m0,s62,0x18000
M v[80:83],a[116:119],a[44:47],v[80:83]
BL v128,s[48:51],s77 offen lds
M v[100:103],a[40:43],a[120:123],v[100:103]
SA m0,s62,0x18400
M v[100:103],a[44:47],a[124:127],v[100:103]
)~"
R"~(BL v129,s[48:51],s77 offen lds
M v[84:87],a[112:115],a[48:51],v[84:87]
SA m0,s62,0x18800
M v[84:87],a[116:119],a[52:55],v[84:87]
BL v130,s[48:51],s77 offen lds
M v[104:107],a[48:51],a[120:123],v[104:107]
SA m0,s62,0x18c00
M v[104:107],a[52:55],a[124:127],v[104:107]
BL v131,s[48:51],s77 offen lds
M v[64:67],a[104:107],a[48:51],v[64:67]
SA m0,s62,0x19000
M v[64:67],a[108:111],a[52:55],v[64:67]
BL v132,s[48:51],s77 offen lds
M v[88:91],a[112:115],a[56:59],v[88:91]
SA m0,s62,0x19400
M v[88:91],a[116:119],a[60:63],v[88:91]
BL v133,s[48:51],s77 offen lds
M v[108:111],a[56:59],a[120:123],v[108:111]
SA m0,s62,0x19800
M v[108:111],a[60:63],a[124:127],v[108:111]
BL v134,s[48:51],s77 offen lds
M v[68:71],a[104:107],a[56:59],v[68:71]
SA m0,s62,0x19c00
M v[68:71],a[108:111],a[60:63],v[68:71]
BL v135,s[48:51],s77 offen lds
M v[92:95],a[112:115],a[64:67],v[92:95]
SA s78,s61,0x100000
SA m0,s63,0x18000
M v[92:95],a[116:119],a[68:71],v[92:95]
BL v136,s[52:55],s78 offen lds
M v[112:115],a[64:67],a[120:123],v[112:115]
SA m0,s63,0x18400
M v[112:115],a[68:71],a[124:127],v[112:115]
BL v137,s[52:55],s78 offen lds
M v[72:75],a[104:107],a[64:67],v[72:75]
SA m0,s63,0x18800
M v[72:75],a[108:111],a[68:71],v[72:75]
BL v138,s[52:55],s78 offen lds
M v[96:99],a[112:115],a[72:75],v[96:99]
SA m0,s63,0x18c00
M v[96:99],a[116:119],a[76:79],v[96:99]
BL v139,s[52:55],s78 offen lds
M v[116:119],a[72:75],a[120:123],v[116:119]
SA s82,s64,0x4000
M v[116:119],a[76:79],a[124:127],v[116:119]
BS v[28:31],v254,s[44:47],s82 offen offset:3072 sc1
M v[76:79],a[104:107],a[72:75],v[76:79]
M v[76:79],a[108:111],a[76:79],v[76:79]
s_mov_b32 s90,0x3eb8aa3b
s_nop 5
CV v152,v80,v81
CV v162,v100,v101
CV v153,v82,v83
CV v163,v102,v103
CV v154,v84,v85
CV v164,v104,v105
CV v155,v86,v87
CV v165,v106,v107
CV v156,v88,v89
CV v166,v108,v109
CV v157,v90,v91
CV v167,v110,v111
CV v158,v92,v93
CV v168,v112,v113
CV v159,v94,v95
CV v169,v114,v115
CV v160,v96,v97
CV v170,v116,v117
CV v161,v98,v99
CV v171,v118,v119
v_mul_f32 v64,s90,v64
v_mul_f32 v65,s90,v65
v_mul_f32 v66,s90,v66
v_mul_f32 v67,s90,v67
CV v172,v64,v65
CV v173,v66,v67
v_mul_f32 v68,s90,v68
v_mul_f32 v69,s90,v69
v_mul_f32 v70,s90,v70
v_mul_f32 v71,s90,v71
CV v174,v68,v69
CV v175,v70,v71
v_mul_f32 v72,s90,v72
v_mul_f32 v73,s90,v73
v_mul_f32 v74,s90,v74
v_mul_f32 v75,s90,v75
CV v176,v72,v73
CV v177,v74,v75
v_mul_f32 v76,s90,v76
v_mul_f32 v77,s90,v77
v_mul_f32 v78,s90,v78
v_mul_f32 v79,s90,v79
CV v178,v76,v77
CV v179,v78,v79
v_mov_b32 v216,0x3f803f80
v_mov_b32 v217,0x3f803f80
v_mov_b32 v218,0x3f803f80
v_mov_b32 v219,0x3f803f80
v_mfma_f32_16x16x16_bf16 v[0:3],v[152:153],v[172:173],v[212:215]
v_mfma_f32_16x16x16_bf16 v[4:7],v[154:155],v[172:173],v[204:207]
v_mfma_f32_16x16x16_bf16 v[8:11],v[154:155],v[174:175],v[208:211]
v_mfma_f32_16x16x16_bf16 v[12:15],v[156:157],v[174:175],v[204:207]
v_mfma_f32_16x16x16_bf16 v[16:19],v[156:157],v[176:177],v[208:211]
v_mfma_f32_16x16x16_bf16 v[20:23],v[158:159],v[176:177],v[204:207]
SA s82,s64,0x1000
BS v[32:35],v254,s[44:47],s82 offen sc1
v_mfma_f32_16x16x16_bf16 v[24:27],v[158:159],v[178:179],v[208:211]
v_mfma_f32_16x16x16_bf16 v[28:31],v[160:161],v[178:179],v[204:207]
v_max3_f32 v220,v0,v1,v2
v_max3_f32 v224,v3,v4,v5
v_max3_f32 v221,v8,v9,v10
v_max3_f32 v225,v11,v12,v13
v_max3_f32 v222,v16,v17,v18
v_max3_f32 v226,v19,v20,v21
s_nop 0
v_max3_f32 v223,v24,v25,v26
v_max3_f32 v227,v27,v28,v29
SA s82,s64,0x1000
BS v[36:39],v254,s[44:47],s82 offen offset:1024 sc1
v_max3_f32 v220,v220,v6,v7
v_max_f32 v220,v220,v224
v_max3_f32 v221,v221,v14,v15
v_max_f32 v221,v221,v225
v_max3_f32 v222,v222,v22,v23
v_max_f32 v222,v222,v226
v_max3_f32 v223,v223,v30,v31
v_max_f32 v223,v223,v227
v_mov_b32 v224,v220
v_mov_b32 v225,v221
SA s82,s64,0x1000
BS v[40:43],v254,s[44:47],s82 offen offset:2048 sc1
v_mov_b32 v226,v222
v_mov_b32 v227,v223
v_permlane16_swap_b32 v220,v224
v_permlane16_swap_b32 v221,v225
v_permlane16_swap_b32 v222,v226
v_permlane16_swap_b32 v223,v227
v_max_f32 v220,v220,v224
v_max_f32 v221,v221,v225
v_max_f32 v222,v222,v226
v_max_f32 v223,v223,v227
SA s82,s64,0x1000
BS v[44:47],v254,s[44:47],s82 offen offset:3072 sc1
v_mov_b32 v224,v220
v_mov_b32 v225,v221
v_mov_b32 v226,v222
v_mov_b32 v227,v223
v_permlane32_swap_b32 v220,v224
v_permlane32_swap_b32 v221,v225
v_permlane32_swap_b32 v222,v226
v_permlane32_swap_b32 v223,v227
v_max_f32 v220,v220,v224
v_max_f32 v221,v221,v225
v_max_f32 v222,v222,v226
v_max_f32 v223,v223,v227
v_sub_f32 v0,v0,v220
v_sub_f32 v1,v1,v220
v_sub_f32 v2,v2,v220
v_sub_f32 v3,v3,v220
v_sub_f32 v4,v4,v220
v_sub_f32 v5,v5,v220
v_sub_f32 v6,v6,v220
v_sub_f32 v7,v7,v220
v_sub_f32 v8,v8,v221
v_sub_f32 v9,v9,v221
v_sub_f32 v10,v10,v221
v_sub_f32 v11,v11,v221
v_sub_f32 v12,v12,v221
v_sub_f32 v13,v13,v221
v_sub_f32 v14,v14,v221
v_sub_f32 v15,v15,v221
v_sub_f32 v16,v16,v222
v_sub_f32 v17,v17,v222
v_sub_f32 v18,v18,v222
v_sub_f32 v19,v19,v222
v_sub_f32 v20,v20,v222
v_sub_f32 v21,v21,v222
v_sub_f32 v22,v22,v222
v_sub_f32 v23,v23,v222
v_sub_f32 v24,v24,v223
v_sub_f32 v25,v25,v223
v_sub_f32 v26,v26,v223
v_sub_f32 v27,v27,v223
v_sub_f32 v28,v28,v223
v_sub_f32 v29,v29,v223
v_sub_f32 v30,v30,v223
v_sub_f32 v31,v31,v223
EX v0,v0
EX v1,v1
EX v2,v2
EX v3,v3
EX v4,v4
EX v5,v5
EX v6,v6
EX v7,v7
EX v8,v8
EX v9,v9
EX v10,v10
EX v11,v11
EX v12,v12
EX v13,v13
EX v14,v14
EX v15,v15
EX v16,v16
EX v17,v17
EX v18,v18
EX v19,v19
EX v20,v20
EX v21,v21
EX v22,v22
EX v23,v23
EX v24,v24
EX v25,v25
EX v26,v26
EX v27,v27
EX v28,v28
EX v29,v29
EX v30,v30
EX v31,v31
CV v32,v0,v1
CV v33,v2,v3
CV v34,v4,v5
CV v35,v6,v7
CV v36,v8,v9
CV v37,v10,v11
CV v38,v12,v13
CV v39,v14,v15
CV v40,v16,v17
CV v41,v18,v19
CV v42,v20,v21
CV v43,v22,v23
CV v44,v24,v25
CV v45,v26,v27
CV v46,v28,v29
CV v47,v30,v31
M v[48:51],v[162:165],v[32:35],0
M v[64:67],v[216:219],v[32:35],0
M v[52:55],v[164:167],v[36:39],0
M v[68:71],v[216:219],v[36:39],0
M v[56:59],v[166:169],v[40:43],0
M v[72:75],v[216:219],v[40:43],0
M v[60:63],v[168:171],v[44:47],0
M v[76:79],v[216:219],v[44:47],0
s_nop 1
v_rcp_f32 v64,v64
s_nop 0
v_rcp_f32 v68,v68
s_nop 0
v_rcp_f32 v72,v72
s_nop 0
v_rcp_f32 v76,v76
s_lshl_b32 s84,s66,2
s_mul_i32 s84,s84,0x24000
s_lshl_b32 s85,s67,1
SA s85,s85,s40
SA s85,s85,0x80
s_lshl_b32 s85,s85,10
SA s84,s84,s85
SA s84,s84,0x2000000
s_lshl_b32 s85,s41,3
v_add_u32 v120,s85,v254
v_mul_f32 v48,v48,v64
v_mul_f32 v49,v49,v64
v_mul_f32 v50,v50,v64
v_mul_f32 v51,v51,v64
CV v48,v48,v49
CV v49,v50,v51
buffer_store_dwordx2 v[48:49],v120,s[44:47],s84 offen sc1
SA s84,s84,0x24000
v_mul_f32 v52,v52,v68
v_mul_f32 v53,v53,v68
v_mul_f32 v54,v54,v68
v_mul_f32 v55,v55,v68
CV v52,v52,v53
CV v53,v54,v55
buffer_store_dwordx2 v[52:53],v120,s[44:47],s84 offen sc1
SA s84,s84,0x24000
v_mul_f32 v56,v56,v72
v_mul_f32 v57,v57,v72
v_mul_f32 v58,v58,v72
v_mul_f32 v59,v59,v72
CV v56,v56,v57
CV v57,v58,v59
buffer_store_dwordx2 v[56:57],v120,s[44:47],s84 offen sc1
SA s84,s84,0x24000
v_mul_f32 v60,v60,v76
v_mul_f32 v61,v61,v76
v_mul_f32 v62,v62,v76
v_mul_f32 v63,v63,v76
CV v60,v60,v61
CV v61,v62,v63
buffer_store_dwordx2 v[60:61],v120,s[44:47],s84 offen sc1
v_and_b32 v224,15,v255
v_lshrrev_b32 v225,1,v224
v_lshrrev_b32 v226,4,v255
v_xor_b32 v225,v225,v226
v_lshlrev_b32 v224,7,v224
v_xor_b32 v226,0,v225
v_lshl_add_u32 v140,v226,4,v224
v_xor_b32 v226,4,v225
v_lshl_add_u32 v141,v226,4,v224
v_add_u32 v140,s62,v140
v_add_u32 v141,s62,v141
v_add_u32 v142,0xc000,v140
v_add_u32 v143,0xc000,v141
v_add_u32 v144,0x18000,v140
v_add_u32 v145,0x18000,v141
v_lshrrev_b32 v224,4,v255
v_lshlrev_b32 v224,11,v224
v_bfe_u32 v225,v255,2,2
v_lshl_add_u32 v224,v225,6,v224
v_and_b32 v225,3,v255
v_lshl_add_u32 v224,v225,3,v224
v_bfe_u32 v225,v255,4,1
v_lshl_add_u32 v146,v225,5,v224
v_xor_b32 v225,1,v225
v_lshl_add_u32 v147,v225,5,v224
v_add_u32 v150,0x20000,v146
v_add_u32 v151,0x20000,v147
v_add_u32 v148,0x14000,v146
v_add_u32 v149,0x14000,v147
v_add_u32 v146,0x8000,v146
v_add_u32 v147,0x8000,v147
s_lshl_b32 s84,s36,13
v_add_u32 v208,s84,v254
v_add_u32 v209,0x1000,v208
s_sub_u32 s79,s40,s41
s_lshl_b32 s79,s79,4
s_mov_b32 s56,0
s_mov_b32 s57,0
W vmcnt(35)
fk_qg:
s_barrier
RD a[224:227],v140
RD a[228:231],v141
RD a[232:235],v140 offset:2048
RD a[236:239],v141 offset:2048
RD a[240:243],v140 offset:4096
RD a[244:247],v141 offset:4096
RD a[248:251],v140 offset:6144
RD a[252:255],v141 offset:6144
TR a[192:193],v146
TR a[194:195],v146 offset:256
TR a[196:197],v146 offset:8192
TR a[198:199],v146 offset:8448
TR a[200:201],v147
TR a[202:203],v147 offset:256
TR a[204:205],v147 offset:8192
TR a[206:207],v147 offset:8448
W lgkmcnt(6)
M v[0:3],a[192:195],a[224:227],0
TR a[208:209],v146 offset:512
W lgkmcnt(5)
M v[0:3],a[196:199],a[228:231],v[0:3]
M v[4:7],a[192:195],a[232:235],0
SA s82,s64,0x5000
TR a[210:211],v146 offset:768
M v[4:7],a[196:199],a[236:239],v[4:7]
BS v[188:191],v254,s[44:47],s82 offen sc1
M v[8:11],a[192:195],a[240:243],0
TR a[212:213],v146 offset:8704
M v[8:11],a[196:199],a[244:247],v[8:11]
M v[12:15],a[192:195],a[248:251],0
TR a[214:215],v146 offset:8960
M v[12:15],a[196:199],a[252:255],v[12:15]
W lgkmcnt(6)
M v[16:19],a[200:203],a[224:227],0
SA s82,s64,0x5000
TR a[216:217],v147 offset:512
W lgkmcnt(5)
M v[16:19],a[204:207],a[228:231],v[16:19]
BS v[192:195],v254,s[44:47],s82 offen offset:1024 sc1
M v[20:23],a[200:203],a[232:235],0
TR a[218:219],v147 offset:768
M v[20:23],a[204:207],a[236:239],v[20:23]
M v[24:27],a[200:203],a[240:243],0
TR a[220:221],v147 offset:8704
M v[24:27],a[204:207],a[244:247],v[24:27]
M v[28:31],a[200:203],a[248:251],0
SA s82,s64,0x5000
TR a[222:223],v147 offset:8960
M v[28:31],a[204:207],a[252:255],v[28:31]
BS v[196:199],v254,s[44:47],s82 offen offset:2048 sc1
W lgkmcnt(6)
M v[32:35],a[208:211],a[224:227],0
TR a[192:193],v146 offset:1024
W lgkmcnt(5)
M v[32:35],a[212:215],a[228:231],v[32:35]
M v[36:39],a[208:211],a[232:235],0
TR a[194:195],v146 offset:1280
M v[36:39],a[212:215],a[236:239],v[36:39]
M v[40:43],a[208:211],a[240:243],0
SA s82,s64,0x5000
TR a[196:197],v146 offset:9216
M v[40:43],a[212:215],a[244:247],v[40:43]
BS v[200:203],v254,s[44:47],s82 offen offset:3072 sc1
M v[44:47],a[208:211],a[248:251],0
TR a[198:199],v146 offset:9472
M v[44:47],a[212:215],a[252:255],v[44:47]
W lgkmcnt(6)
M v[48:51],a[216:219],a[224:227],0
TR a[200:201],v147 offset:1024
W lgkmcnt(5)
M v[48:51],a[220:223],a[228:231],v[48:51]
M v[52:55],a[216:219],a[232:235],0
SA s82,s64,0x2000
TR a[202:203],v147 offset:1280
M v[52:55],a[220:223],a[236:239],v[52:55]
BS a[128:131],v254,s[44:47],s82 offen sc1
M v[56:59],a[216:219],a[240:243],0
TR a[204:205],v147 offset:9216
M v[56:59],a[220:223],a[244:247],v[56:59]
M v[60:63],a[216:219],a[248:251],0
TR a[206:207],v147 offset:9472
M v[60:63],a[220:223],a[252:255],v[60:63]
W lgkmcnt(6)
M v[64:67],a[192:195],a[224:227],0
TR a[208:209],v146 offset:1536
W lgkmcnt(5)
M v[64:67],a[196:199],a[228:231],v[64:67]
M v[68:71],a[192:195],a[232:235],0
TR a[210:211],v146 offset:1792
M v[68:71],a[196:199],a[236:239],v[68:71]
M v[72:75],a[192:195],a[240:243],0
TR a[212:213],v146 offset:9728
M v[72:75],a[196:199],a[244:247],v[72:75]
M v[76:79],a[192:195],a[248:251],0
TR a[214:215],v146 offset:9984
M v[76:79],a[196:199],a[252:255],v[76:79]
W lgkmcnt(6)
M v[80:83],a[200:203],a[224:227],0
TR a[216:217],v147 offset:1536
W lgkmcnt(5)
M v[80:83],a[204:207],a[228:231],v[80:83]
TR a[218:219],v147 offset:1792
M v[84:87],a[200:203],a[232:235],0
TR a[220:221],v147 offset:9728
M v[84:87],a[204:207],a[236:239],v[84:87]
TR a[222:223],v147 offset:9984
M v[88:91],a[200:203],a[240:243],0
M v[88:91],a[204:207],a[244:247],v[88:91]
M v[92:95],a[200:203],a[248:251],0
M v[92:95],a[204:207],a[252:255],v[92:95]
W vmcnt(27) lgkmcnt(0)
s_barrier
M v[96:99],a[208:211],a[224:227],0
SA s77,s60,0x180
SA m0,s62,0
TR a[192:193],v148
M v[96:99],a[212:215],a[228:231],v[96:99]
BL v128,s[48:51],s77 offen lds
M v[100:103],a[208:211],a[232:235],0
TR a[194:195],v148 offset:256
M v[100:103],a[212:215],a[236:239],v[100:103]
RD a[0:3],v142
RD a[4:7],v143
M v[104:107],a[208:211],a[240:243],0
SA m0,s62,0x400
TR a[196:197],v148 offset:8192
M v[104:107],a[212:215],a[244:247],v[104:107]
BL v129,s[48:51],s77 offen lds
M v[108:111],a[208:211],a[248:251],0
TR a[198:199],v148 offset:8448
M v[108:111],a[212:215],a[252:255],v[108:111]
SA m0,s62,0x800
RD a[8:11],v142 offset:2048
RD a[12:15],v143 offset:2048
M v[112:115],a[216:219],a[224:227],0
BL v130,s[48:51],s77 offen lds
M v[112:115],a[220:223],a[228:231],v[112:115]
TR a[200:201],v149
M v[116:119],a[216:219],a[232:235],0
RD a[16:19],v142 offset:4096
RD a[20:23],v143 offset:4096
M v[116:119],a[220:223],a[236:239],v[116:119]
TR a[202:203],v149 offset:256
M v[120:123],a[216:219],a[240:243],0
RD a[24:27],v142 offset:6144
RD a[28:31],v143 offset:6144
M v[120:123],a[220:223],a[244:247],v[120:123]
TR a[204:205],v149 offset:8192
M v[124:127],a[216:219],a[248:251],0
M v[124:127],a[220:223],a[252:255],v[124:127]
TR a[206:207],v149 offset:8448
W lgkmcnt(13)
M v[0:3],a[192:195],a[0:3],v[0:3]
SA m0,s62,0xc00
TR a[208:209],v148 offset:512
v_and_b32 v228,15,v255
W lgkmcnt(11)
M v[0:3],a[196:199],a[4:7],v[0:3]
BL v131,s[48:51],s77 offen lds
W lgkmcnt(10)
M v[4:7],a[192:195],a[8:11],v[4:7]
TR a[210:211],v148 offset:768
v_lshrrev_b32 v229,4,v255
W lgkmcnt(10)
M v[4:7],a[196:199],a[12:15],v[4:7]
W lgkmcnt(8)
M v[8:11],a[192:195],a[16:19],v[8:11]
SA m0,s62,0x1000
TR a[212:213],v148 offset:8704
v_lshlrev_b32 v229,2,v229
W lgkmcnt(8)
M v[8:11],a[196:199],a[20:23],v[8:11]
BL v132,s[48:51],s77 offen lds
W lgkmcnt(6)
M v[12:15],a[192:195],a[24:27],v[12:15]
TR a[214:215],v148 offset:8960
v_sub_u32 v228,v228,v229
W lgkmcnt(6)
M v[12:15],a[196:199],a[28:31],v[12:15]
M v[16:19],a[200:203],a[0:3],v[16:19]
SA m0,s62,0x1400
TR a[216:217],v149 offset:512
v_add_u32 v228,s79,v228
W lgkmcnt(5)
M v[16:19],a[204:207],a[4:7],v[16:19]
BL v133,s[48:51],s77 offen lds
M v[20:23],a[200:203],a[8:11],v[20:23]
TR a[218:219],v149 offset:768
v_mov_b32 v229,0xff800000
M v[20:23],a[204:207],a[12:15],v[20:23]
M v[24:27],a[200:203],a[16:19],v[24:27]
SA m0,s62,0x1800
TR a[220:221],v149 offset:8704
v_cmp_le_i32 s[90:91],0,v228
M v[24:27],a[204:207],a[20:23],v[24:27]
)~"
R"~(BL v134,s[48:51],s77 offen lds
M v[28:31],a[200:203],a[24:27],v[28:31]
TR a[222:223],v149 offset:8960
v_cmp_le_i32 s[92:93],1,v228
M v[28:31],a[204:207],a[28:31],v[28:31]
W lgkmcnt(6)
M v[32:35],a[208:211],a[0:3],v[32:35]
SA m0,s62,0x1c00
TR a[192:193],v148 offset:1024
v_cmp_le_i32 s[94:95],2,v228
W lgkmcnt(5)
M v[32:35],a[212:215],a[4:7],v[32:35]
BL v135,s[48:51],s77 offen lds
M v[36:39],a[208:211],a[8:11],v[36:39]
TR a[194:195],v148 offset:1280
v_cmp_le_i32 s[96:97],3,v228
M v[36:39],a[212:215],a[12:15],v[36:39]
M v[40:43],a[208:211],a[16:19],v[40:43]
SA s78,s61,0x180000
SA m0,s63,0
TR a[196:197],v148 offset:9216
v_cndmask_b32 v160,v229,0,s[90:91]
M v[40:43],a[212:215],a[20:23],v[40:43]
BL v136,s[52:55],s78 offen lds
M v[44:47],a[208:211],a[24:27],v[44:47]
TR a[198:199],v148 offset:9472
v_cndmask_b32 v161,v229,0,s[92:93]
M v[44:47],a[212:215],a[28:31],v[44:47]
W lgkmcnt(6)
M v[48:51],a[216:219],a[0:3],v[48:51]
SA m0,s63,0x400
TR a[200:201],v149 offset:1024
v_cndmask_b32 v162,v229,0,s[94:95]
W lgkmcnt(5)
M v[48:51],a[220:223],a[4:7],v[48:51]
BL v137,s[52:55],s78 offen lds
M v[52:55],a[216:219],a[8:11],v[52:55]
TR a[202:203],v149 offset:1280
v_cndmask_b32 v163,v229,0,s[96:97]
M v[52:55],a[220:223],a[12:15],v[52:55]
M v[56:59],a[216:219],a[16:19],v[56:59]
SA m0,s63,0x800
TR a[204:205],v149 offset:9216
v_mov_b32 v204,0x3f803f80
M v[56:59],a[220:223],a[20:23],v[56:59]
BL v138,s[52:55],s78 offen lds
M v[60:63],a[216:219],a[24:27],v[60:63]
TR a[206:207],v149 offset:9472
v_mov_b32 v205,0x3f803f80
M v[60:63],a[220:223],a[28:31],v[60:63]
W lgkmcnt(6)
M v[64:67],a[192:195],a[0:3],v[64:67]
SA m0,s63,0xc00
TR a[208:209],v148 offset:1536
v_mov_b32 v206,0x3f803f80
W lgkmcnt(5)
M v[64:67],a[196:199],a[4:7],v[64:67]
BL v139,s[52:55],s78 offen lds
M v[68:71],a[192:195],a[8:11],v[68:71]
SA s82,s64,0x2000
TR a[210:211],v148 offset:1792
v_mov_b32 v207,0x3f803f80
M v[68:71],a[196:199],a[12:15],v[68:71]
BS a[132:135],v254,s[44:47],s82 offen offset:1024 sc1
M v[72:75],a[192:195],a[16:19],v[72:75]
SA s82,s64,0x2000
TR a[212:213],v148 offset:9728
M v[72:75],a[196:199],a[20:23],v[72:75]
BS a[136:139],v254,s[44:47],s82 offen offset:2048 sc1
M v[76:79],a[192:195],a[24:27],v[76:79]
SA s82,s64,0x2000
TR a[214:215],v148 offset:9984
M v[76:79],a[196:199],a[28:31],v[76:79]
BS a[140:143],v254,s[44:47],s82 offen offset:3072 sc1
W lgkmcnt(6)
M v[80:83],a[200:203],a[0:3],v[80:83]
TR a[216:217],v149 offset:1536
W lgkmcnt(5)
M v[80:83],a[204:207],a[4:7],v[80:83]
TR a[218:219],v149 offset:1792
M v[84:87],a[200:203],a[8:11],v[84:87]
TR a[220:221],v149 offset:9728
M v[84:87],a[204:207],a[12:15],v[84:87]
TR a[222:223],v149 offset:9984
M v[88:91],a[200:203],a[16:19],v[88:91]
SA s82,s64,0x6000
M v[88:91],a[204:207],a[20:23],v[88:91]
BS a[144:147],v254,s[44:47],s82 offen sc1
M v[92:95],a[200:203],a[24:27],v[92:95]
SA s82,s64,0x6000
M v[92:95],a[204:207],a[28:31],v[92:95]
BS a[148:151],v254,s[44:47],s82 offen offset:1024 sc1
W vmcnt(31) lgkmcnt(0)
s_barrier
M v[96:99],a[208:211],a[0:3],v[96:99]
SA s77,s60,0x200
SA m0,s62,0xc000
TR a[192:193],v150
M v[96:99],a[212:215],a[4:7],v[96:99]
BL v128,s[48:51],s77 offen lds
M v[100:103],a[208:211],a[8:11],v[100:103]
TR a[194:195],v150 offset:256
M v[100:103],a[212:215],a[12:15],v[100:103]
RD a[224:227],v144
RD a[228:231],v145
M v[104:107],a[208:211],a[16:19],v[104:107]
SA m0,s62,0xc400
TR a[196:197],v150 offset:8192
M v[104:107],a[212:215],a[20:23],v[104:107]
BL v129,s[48:51],s77 offen lds
M v[108:111],a[208:211],a[24:27],v[108:111]
TR a[198:199],v150 offset:8448
M v[108:111],a[212:215],a[28:31],v[108:111]
SA m0,s62,0xc800
RD a[232:235],v144 offset:2048
RD a[236:239],v145 offset:2048
M v[112:115],a[216:219],a[0:3],v[112:115]
BL v130,s[48:51],s77 offen lds
M v[112:115],a[220:223],a[4:7],v[112:115]
TR a[200:201],v151
M v[116:119],a[216:219],a[8:11],v[116:119]
RD a[240:243],v144 offset:4096
RD a[244:247],v145 offset:4096
M v[116:119],a[220:223],a[12:15],v[116:119]
TR a[202:203],v151 offset:256
M v[120:123],a[216:219],a[16:19],v[120:123]
RD a[248:251],v144 offset:6144
RD a[252:255],v145 offset:6144
M v[120:123],a[220:223],a[20:23],v[120:123]
TR a[204:205],v151 offset:8192
M v[124:127],a[216:219],a[24:27],v[124:127]
M v[124:127],a[220:223],a[28:31],v[124:127]
TR a[206:207],v151 offset:8448
W lgkmcnt(13)
M v[0:3],a[192:195],a[224:227],v[0:3]
SA m0,s62,0xcc00
TR a[208:209],v150 offset:512
W lgkmcnt(11)
M v[0:3],a[196:199],a[228:231],v[0:3]
BL v131,s[48:51],s77 offen lds
W lgkmcnt(10)
M v[4:7],a[192:195],a[232:235],v[4:7]
TR a[210:211],v150 offset:768
W lgkmcnt(10)
M v[4:7],a[196:199],a[236:239],v[4:7]
W lgkmcnt(8)
M v[8:11],a[192:195],a[240:243],v[8:11]
SA m0,s62,0xd000
TR a[212:213],v150 offset:8704
W lgkmcnt(8)
M v[8:11],a[196:199],a[244:247],v[8:11]
BL v132,s[48:51],s77 offen lds
W lgkmcnt(6)
M v[12:15],a[192:195],a[248:251],v[12:15]
TR a[214:215],v150 offset:8960
W lgkmcnt(6)
M v[12:15],a[196:199],a[252:255],v[12:15]
M v[16:19],a[200:203],a[224:227],v[16:19]
SA m0,s62,0xd400
TR a[216:217],v151 offset:512
W lgkmcnt(5)
M v[16:19],a[204:207],a[228:231],v[16:19]
BL v133,s[48:51],s77 offen lds
M v[20:23],a[200:203],a[232:235],v[20:23]
TR a[218:219],v151 offset:768
M v[20:23],a[204:207],a[236:239],v[20:23]
M v[24:27],a[200:203],a[240:243],v[24:27]
SA m0,s62,0xd800
TR a[220:221],v151 offset:8704
M v[24:27],a[204:207],a[244:247],v[24:27]
BL v134,s[48:51],s77 offen lds
M v[28:31],a[200:203],a[248:251],v[28:31]
TR a[222:223],v151 offset:8960
M v[28:31],a[204:207],a[252:255],v[28:31]
W lgkmcnt(6)
M v[32:35],a[208:211],a[224:227],v[32:35]
SA m0,s62,0xdc00
TR a[192:193],v150 offset:1024
W lgkmcnt(5)
M v[32:35],a[212:215],a[228:231],v[32:35]
BL v135,s[48:51],s77 offen lds
M v[36:39],a[208:211],a[232:235],v[36:39]
TR a[194:195],v150 offset:1280
M v[36:39],a[212:215],a[236:239],v[36:39]
M v[40:43],a[208:211],a[240:243],v[40:43]
SA s78,s61,0x200000
SA m0,s63,0xc000
TR a[196:197],v150 offset:9216
M v[40:43],a[212:215],a[244:247],v[40:43]
BL v136,s[52:55],s78 offen lds
M v[44:47],a[208:211],a[248:251],v[44:47]
TR a[198:199],v150 offset:9472
M v[44:47],a[212:215],a[252:255],v[44:47]
W lgkmcnt(6)
M v[48:51],a[216:219],a[224:227],v[48:51]
SA m0,s63,0xc400
TR a[200:201],v151 offset:1024
W lgkmcnt(5)
M v[48:51],a[220:223],a[228:231],v[48:51]
BL v137,s[52:55],s78 offen lds
M v[52:55],a[216:219],a[232:235],v[52:55]
TR a[202:203],v151 offset:1280
M v[52:55],a[220:223],a[236:239],v[52:55]
M v[56:59],a[216:219],a[240:243],v[56:59]
SA m0,s63,0xc800
TR a[204:205],v151 offset:9216
M v[56:59],a[220:223],a[244:247],v[56:59]
BL v138,s[52:55],s78 offen lds
M v[60:63],a[216:219],a[248:251],v[60:63]
TR a[206:207],v151 offset:9472
M v[60:63],a[220:223],a[252:255],v[60:63]
W lgkmcnt(6)
M v[64:67],a[192:195],a[224:227],v[64:67]
SA m0,s63,0xcc00
TR a[208:209],v150 offset:1536
W lgkmcnt(5)
M v[64:67],a[196:199],a[228:231],v[64:67]
BL v139,s[52:55],s78 offen lds
M v[68:71],a[192:195],a[232:235],v[68:71]
SA s82,s64,0x6000
TR a[210:211],v150 offset:1792
M v[68:71],a[196:199],a[236:239],v[68:71]
BS a[152:155],v254,s[44:47],s82 offen offset:2048 sc1
M v[72:75],a[192:195],a[240:243],v[72:75]
SA s82,s64,0x6000
TR a[212:213],v150 offset:9728
M v[72:75],a[196:199],a[244:247],v[72:75]
BS a[156:159],v254,s[44:47],s82 offen offset:3072 sc1
M v[76:79],a[192:195],a[248:251],v[76:79]
SA s82,s64,0x3000
TR a[214:215],v150 offset:9984
M v[76:79],a[196:199],a[252:255],v[76:79]
BS a[160:163],v254,s[44:47],s82 offen sc1
W lgkmcnt(6)
M v[80:83],a[200:203],a[224:227],v[80:83]
TR a[216:217],v151 offset:1536
W lgkmcnt(5)
M v[80:83],a[204:207],a[228:231],v[80:83]
TR a[218:219],v151 offset:1792
M v[84:87],a[200:203],a[232:235],v[84:87]
TR a[220:221],v151 offset:9728
M v[84:87],a[204:207],a[236:239],v[84:87]
TR a[222:223],v151 offset:9984
M v[88:91],a[200:203],a[240:243],v[88:91]
SA s82,s64,0x3000
M v[88:91],a[204:207],a[244:247],v[88:91]
BS a[164:167],v254,s[44:47],s82 offen offset:1024 sc1
M v[92:95],a[200:203],a[248:251],v[92:95]
SA s82,s64,0x3000
M v[92:95],a[204:207],a[252:255],v[92:95]
BS a[168:171],v254,s[44:47],s82 offen offset:2048 sc1
W vmcnt(22) lgkmcnt(0)
s_barrier
M v[96:99],a[208:211],a[224:227],v[96:99]
SA s77,s60,0x280
SA m0,s62,0x18000
TR a[192:193],v146
M v[96:99],a[212:215],a[228:231],v[96:99]
BL v128,s[48:51],s77 offen lds
M v[100:103],a[208:211],a[232:235],v[100:103]
TR a[194:195],v146 offset:256
M v[100:103],a[212:215],a[236:239],v[100:103]
RD a[0:3],v140
RD a[4:7],v141
M v[104:107],a[208:211],a[240:243],v[104:107]
SA m0,s62,0x18400
TR a[196:197],v146 offset:8192
M v[104:107],a[212:215],a[244:247],v[104:107]
BL v129,s[48:51],s77 offen lds
M v[108:111],a[208:211],a[248:251],v[108:111]
TR a[198:199],v146 offset:8448
M v[108:111],a[212:215],a[252:255],v[108:111]
SA m0,s62,0x18800
RD a[8:11],v140 offset:2048
RD a[12:15],v141 offset:2048
M v[112:115],a[216:219],a[224:227],v[112:115]
BL v130,s[48:51],s77 offen lds
M v[112:115],a[220:223],a[228:231],v[112:115]
TR a[200:201],v147
M v[116:119],a[216:219],a[232:235],v[116:119]
RD a[16:19],v140 offset:4096
RD a[20:23],v141 offset:4096
M v[116:119],a[220:223],a[236:239],v[116:119]
TR a[202:203],v147 offset:256
M v[120:123],a[216:219],a[240:243],v[120:123]
RD a[24:27],v140 offset:6144
RD a[28:31],v141 offset:6144
M v[120:123],a[220:223],a[244:247],v[120:123]
TR a[204:205],v147 offset:8192
M v[124:127],a[216:219],a[248:251],v[124:127]
M v[124:127],a[220:223],a[252:255],v[124:127]
TR a[206:207],v147 offset:8448
W lgkmcnt(13)
M v[0:3],a[192:195],a[0:3],v[0:3]
SA m0,s62,0x18c00
TR a[208:209],v146 offset:512
W lgkmcnt(11)
M v[0:3],a[196:199],a[4:7],v[0:3]
BL v131,s[48:51],s77 offen lds
W lgkmcnt(10)
M v[4:7],a[192:195],a[8:11],v[4:7]
TR a[210:211],v146 offset:768
W lgkmcnt(10)
M v[4:7],a[196:199],a[12:15],v[4:7]
W lgkmcnt(8)
M v[8:11],a[192:195],a[16:19],v[8:11]
SA m0,s62,0x19000
TR a[212:213],v146 offset:8704
W lgkmcnt(8)
M v[8:11],a[196:199],a[20:23],v[8:11]
BL v132,s[48:51],s77 offen lds
W lgkmcnt(6)
M v[12:15],a[192:195],a[24:27],v[12:15]
TR a[214:215],v146 offset:8960
W lgkmcnt(6)
M v[12:15],a[196:199],a[28:31],v[12:15]
M v[16:19],a[200:203],a[0:3],v[16:19]
SA m0,s62,0x19400
TR a[216:217],v147 offset:512
W lgkmcnt(5)
M v[16:19],a[204:207],a[4:7],v[16:19]
BL v133,s[48:51],s77 offen lds
M v[20:23],a[200:203],a[8:11],v[20:23]
TR a[218:219],v147 offset:768
M v[20:23],a[204:207],a[12:15],v[20:23]
M v[24:27],a[200:203],a[16:19],v[24:27]
SA m0,s62,0x19800
TR a[220:221],v147 offset:8704
M v[24:27],a[204:207],a[20:23],v[24:27]
BL v134,s[48:51],s77 offen lds
M v[28:31],a[200:203],a[24:27],v[28:31]
TR a[222:223],v147 offset:8960
M v[28:31],a[204:207],a[28:31],v[28:31]
W lgkmcnt(6)
M v[32:35],a[208:211],a[0:3],v[32:35]
SA m0,s62,0x19c00
TR a[192:193],v146 offset:1024
W lgkmcnt(5)
M v[32:35],a[212:215],a[4:7],v[32:35]
BL v135,s[48:51],s77 offen lds
M v[36:39],a[208:211],a[8:11],v[36:39]
TR a[194:195],v146 offset:1280
M v[36:39],a[212:215],a[12:15],v[36:39]
M v[40:43],a[208:211],a[16:19],v[40:43]
SA s78,s61,0x280000
SA m0,s63,0x18000
TR a[196:197],v146 offset:9216
M v[40:43],a[212:215],a[20:23],v[40:43]
BL v136,s[52:55],s78 offen lds
M v[44:47],a[208:211],a[24:27],v[44:47]
TR a[198:199],v146 offset:9472
M v[44:47],a[212:215],a[28:31],v[44:47]
W lgkmcnt(6)
M v[48:51],a[216:219],a[0:3],v[48:51]
SA m0,s63,0x18400
TR a[200:201],v147 offset:1024
W lgkmcnt(5)
M v[48:51],a[220:223],a[4:7],v[48:51]
BL v137,s[52:55],s78 offen lds
M v[52:55],a[216:219],a[8:11],v[52:55]
TR a[202:203],v147 offset:1280
M v[52:55],a[220:223],a[12:15],v[52:55]
M v[56:59],a[216:219],a[16:19],v[56:59]
SA m0,s63,0x18800
TR a[204:205],v147 offset:9216
M v[56:59],a[220:223],a[20:23],v[56:59]
BL v138,s[52:55],s78 offen lds
M v[60:63],a[216:219],a[24:27],v[60:63]
TR a[206:207],v147 offset:9472
M v[60:63],a[220:223],a[28:31],v[60:63]
W lgkmcnt(6)
M v[64:67],a[192:195],a[0:3],v[64:67]
SA m0,s63,0x18c00
TR a[208:209],v146 offset:1536
W lgkmcnt(5)
M v[64:67],a[196:199],a[4:7],v[64:67]
BL v139,s[52:55],s78 offen lds
M v[68:71],a[192:195],a[8:11],v[68:71]
SA s82,s64,0x3000
TR a[210:211],v146 offset:1792
M v[68:71],a[196:199],a[12:15],v[68:71]
BS a[172:175],v254,s[44:47],s82 offen offset:3072 sc1
M v[72:75],a[192:195],a[16:19],v[72:75]
SA s82,s64,0x7000
TR a[212:213],v146 offset:9728
M v[72:75],a[196:199],a[20:23],v[72:75]
BS a[176:179],v254,s[44:47],s82 offen sc1
M v[76:79],a[192:195],a[24:27],v[76:79]
SA s82,s64,0x7000
TR a[214:215],v146 offset:9984
M v[76:79],a[196:199],a[28:31],v[76:79]
BS a[180:183],v254,s[44:47],s82 offen offset:1024 sc1
W lgkmcnt(6)
M v[80:83],a[200:203],a[0:3],v[80:83]
TR a[216:217],v147 offset:1536
W lgkmcnt(5)
M v[80:83],a[204:207],a[4:7],v[80:83]
TR a[218:219],v147 offset:1792
M v[84:87],a[200:203],a[8:11],v[84:87]
TR a[220:221],v147 offset:9728
M v[84:87],a[204:207],a[12:15],v[84:87]
TR a[222:223],v147 offset:9984
M v[88:91],a[200:203],a[16:19],v[88:91]
SA s82,s64,0x7000
M v[88:91],a[204:207],a[20:23],v[88:91]
BS a[184:187],v254,s[44:47],s82 offen offset:2048 sc1
M v[92:95],a[200:203],a[24:27],v[92:95]
SA s82,s64,0x7000
M v[92:95],a[204:207],a[28:31],v[92:95]
BS a[188:191],v254,s[44:47],s82 offen offset:3072 sc1
W vmcnt(22) lgkmcnt(0)
s_barrier
M v[96:99],a[208:211],a[0:3],v[96:99]
SA s77,s60,0x300
SA m0,s62,0
TR a[192:193],v148
M v[96:99],a[212:215],a[4:7],v[96:99]
BL v128,s[48:51],s77 offen lds
M v[100:103],a[208:211],a[8:11],v[100:103]
TR a[194:195],v148 offset:256
M v[100:103],a[212:215],a[12:15],v[100:103]
RD a[224:227],v142
RD a[228:231],v143
M v[104:107],a[208:211],a[16:19],v[104:107]
SA m0,s62,0x400
TR a[196:197],v148 offset:8192
M v[104:107],a[212:215],a[20:23],v[104:107]
BL v129,s[48:51],s77 offen lds
M v[108:111],a[208:211],a[24:27],v[108:111]
TR a[198:199],v148 offset:8448
M v[108:111],a[212:215],a[28:31],v[108:111]
SA m0,s62,0x800
RD a[232:235],v142 offset:2048
RD a[236:239],v143 offset:2048
M v[112:115],a[216:219],a[0:3],v[112:115]
BL v130,s[48:51],s77 offen lds
M v[112:115],a[220:223],a[4:7],v[112:115]
TR a[200:201],v149
M v[116:119],a[216:219],a[8:11],v[116:119]
RD a[240:243],v142 offset:4096
RD a[244:247],v143 offset:4096
M v[116:119],a[220:223],a[12:15],v[116:119]
)~"
R"~(TR a[202:203],v149 offset:256
M v[120:123],a[216:219],a[16:19],v[120:123]
RD a[248:251],v142 offset:6144
RD a[252:255],v143 offset:6144
M v[120:123],a[220:223],a[20:23],v[120:123]
TR a[204:205],v149 offset:8192
M v[124:127],a[216:219],a[24:27],v[124:127]
M v[124:127],a[220:223],a[28:31],v[124:127]
TR a[206:207],v149 offset:8448
W lgkmcnt(13)
M v[0:3],a[192:195],a[224:227],v[0:3]
SA m0,s62,0xc00
TR a[208:209],v148 offset:512
W lgkmcnt(11)
M v[0:3],a[196:199],a[228:231],v[0:3]
BL v131,s[48:51],s77 offen lds
W lgkmcnt(10)
M v[4:7],a[192:195],a[232:235],v[4:7]
TR a[210:211],v148 offset:768
W lgkmcnt(10)
M v[4:7],a[196:199],a[236:239],v[4:7]
W lgkmcnt(8)
M v[8:11],a[192:195],a[240:243],v[8:11]
SA m0,s62,0x1000
TR a[212:213],v148 offset:8704
W lgkmcnt(8)
M v[8:11],a[196:199],a[244:247],v[8:11]
BL v132,s[48:51],s77 offen lds
W lgkmcnt(6)
M v[12:15],a[192:195],a[248:251],v[12:15]
TR a[214:215],v148 offset:8960
W lgkmcnt(6)
M v[12:15],a[196:199],a[252:255],v[12:15]
M v[16:19],a[200:203],a[224:227],v[16:19]
SA m0,s62,0x1400
TR a[216:217],v149 offset:512
W lgkmcnt(5)
M v[16:19],a[204:207],a[228:231],v[16:19]
BL v133,s[48:51],s77 offen lds
M v[20:23],a[200:203],a[232:235],v[20:23]
TR a[218:219],v149 offset:768
M v[20:23],a[204:207],a[236:239],v[20:23]
M v[24:27],a[200:203],a[240:243],v[24:27]
SA m0,s62,0x1800
TR a[220:221],v149 offset:8704
M v[24:27],a[204:207],a[244:247],v[24:27]
BL v134,s[48:51],s77 offen lds
M v[28:31],a[200:203],a[248:251],v[28:31]
TR a[222:223],v149 offset:8960
M v[28:31],a[204:207],a[252:255],v[28:31]
W lgkmcnt(6)
M v[32:35],a[208:211],a[224:227],v[32:35]
SA m0,s62,0x1c00
TR a[192:193],v148 offset:1024
W lgkmcnt(5)
M v[32:35],a[212:215],a[228:231],v[32:35]
BL v135,s[48:51],s77 offen lds
M v[36:39],a[208:211],a[232:235],v[36:39]
TR a[194:195],v148 offset:1280
M v[36:39],a[212:215],a[236:239],v[36:39]
M v[40:43],a[208:211],a[240:243],v[40:43]
SA s78,s61,0x300000
SA m0,s63,0
TR a[196:197],v148 offset:9216
M v[40:43],a[212:215],a[244:247],v[40:43]
BL v136,s[52:55],s78 offen lds
M v[44:47],a[208:211],a[248:251],v[44:47]
TR a[198:199],v148 offset:9472
M v[44:47],a[212:215],a[252:255],v[44:47]
W lgkmcnt(6)
M v[48:51],a[216:219],a[224:227],v[48:51]
SA m0,s63,0x400
TR a[200:201],v149 offset:1024
W lgkmcnt(5)
M v[48:51],a[220:223],a[228:231],v[48:51]
BL v137,s[52:55],s78 offen lds
M v[52:55],a[216:219],a[232:235],v[52:55]
TR a[202:203],v149 offset:1280
M v[52:55],a[220:223],a[236:239],v[52:55]
M v[56:59],a[216:219],a[240:243],v[56:59]
SA m0,s63,0x800
TR a[204:205],v149 offset:9216
M v[56:59],a[220:223],a[244:247],v[56:59]
BL v138,s[52:55],s78 offen lds
M v[60:63],a[216:219],a[248:251],v[60:63]
TR a[206:207],v149 offset:9472
M v[60:63],a[220:223],a[252:255],v[60:63]
W lgkmcnt(6)
M v[64:67],a[192:195],a[224:227],v[64:67]
SA m0,s63,0xc00
TR a[208:209],v148 offset:1536
W lgkmcnt(5)
M v[64:67],a[196:199],a[228:231],v[64:67]
BL v139,s[52:55],s78 offen lds
M v[68:71],a[192:195],a[232:235],v[68:71]
TR a[210:211],v148 offset:1792
M v[68:71],a[196:199],a[236:239],v[68:71]
M v[72:75],a[192:195],a[240:243],v[72:75]
TR a[212:213],v148 offset:9728
M v[72:75],a[196:199],a[244:247],v[72:75]
M v[76:79],a[192:195],a[248:251],v[76:79]
TR a[214:215],v148 offset:9984
M v[76:79],a[196:199],a[252:255],v[76:79]
W lgkmcnt(6)
M v[80:83],a[200:203],a[224:227],v[80:83]
TR a[216:217],v149 offset:1536
W lgkmcnt(5)
M v[80:83],a[204:207],a[228:231],v[80:83]
TR a[218:219],v149 offset:1792
M v[84:87],a[200:203],a[232:235],v[84:87]
TR a[220:221],v149 offset:9728
M v[84:87],a[204:207],a[236:239],v[84:87]
TR a[222:223],v149 offset:9984
M v[88:91],a[200:203],a[240:243],v[88:91]
M v[88:91],a[204:207],a[244:247],v[88:91]
M v[92:95],a[200:203],a[248:251],v[92:95]
M v[92:95],a[204:207],a[252:255],v[92:95]
W vmcnt(12) lgkmcnt(0)
s_barrier
s_lshl_b32 s84,s38,3
SA s84,s84,s39
s_lshl_b32 s84,s84,6
SA s84,s84,0x3501000
s_lshl_b32 s85,s66,3
SA s85,s85,s67
s_lshl_b32 s85,s85,6
SA s85,s85,0x3508000
s_cmp_eq_u32 s36,0
s_cbranch_scc0 fk_pub_kv
v_mov_b32 v234,s42
v_mov_b32 v235,s43
buffer_store_dwordx2 v[234:235],v250,s[44:47],s84 offen sc1
buffer_store_dwordx2 v[234:235],v250,s[44:47],s85 offen sc1
fk_pub_kv:
M v[96:99],a[208:211],a[224:227],v[96:99]
SA s77,s60,0x380
SA m0,s62,0xc000
TR a[192:193],v150
M v[96:99],a[212:215],a[228:231],v[96:99]
BL v128,s[48:51],s77 offen lds
M v[100:103],a[208:211],a[232:235],v[100:103]
TR a[194:195],v150 offset:256
M v[100:103],a[212:215],a[236:239],v[100:103]
RD a[0:3],v144
RD a[4:7],v145
M v[104:107],a[208:211],a[240:243],v[104:107]
SA m0,s62,0xc400
TR a[196:197],v150 offset:8192
M v[104:107],a[212:215],a[244:247],v[104:107]
BL v129,s[48:51],s77 offen lds
M v[108:111],a[208:211],a[248:251],v[108:111]
TR a[198:199],v150 offset:8448
M v[108:111],a[212:215],a[252:255],v[108:111]
SA m0,s62,0xc800
RD a[8:11],v144 offset:2048
RD a[12:15],v145 offset:2048
M v[112:115],a[216:219],a[224:227],v[112:115]
BL v130,s[48:51],s77 offen lds
M v[112:115],a[220:223],a[228:231],v[112:115]
TR a[200:201],v151
M v[116:119],a[216:219],a[232:235],v[116:119]
RD a[16:19],v144 offset:4096
RD a[20:23],v145 offset:4096
M v[116:119],a[220:223],a[236:239],v[116:119]
TR a[202:203],v151 offset:256
M v[120:123],a[216:219],a[240:243],v[120:123]
RD a[24:27],v144 offset:6144
RD a[28:31],v145 offset:6144
M v[120:123],a[220:223],a[244:247],v[120:123]
TR a[204:205],v151 offset:8192
M v[124:127],a[216:219],a[248:251],v[124:127]
M v[124:127],a[220:223],a[252:255],v[124:127]
TR a[206:207],v151 offset:8448
W lgkmcnt(13)
M v[0:3],a[192:195],a[0:3],v[0:3]
SA m0,s62,0xcc00
TR a[208:209],v150 offset:512
W lgkmcnt(11)
M v[0:3],a[196:199],a[4:7],v[0:3]
BL v131,s[48:51],s77 offen lds
W lgkmcnt(10)
M v[4:7],a[192:195],a[8:11],v[4:7]
TR a[210:211],v150 offset:768
W lgkmcnt(10)
M v[4:7],a[196:199],a[12:15],v[4:7]
W lgkmcnt(8)
M v[8:11],a[192:195],a[16:19],v[8:11]
SA m0,s62,0xd000
TR a[212:213],v150 offset:8704
W lgkmcnt(8)
M v[8:11],a[196:199],a[20:23],v[8:11]
BL v132,s[48:51],s77 offen lds
W lgkmcnt(6)
M v[12:15],a[192:195],a[24:27],v[12:15]
TR a[214:215],v150 offset:8960
W lgkmcnt(6)
M v[12:15],a[196:199],a[28:31],v[12:15]
M v[16:19],a[200:203],a[0:3],v[16:19]
SA m0,s62,0xd400
TR a[216:217],v151 offset:512
W lgkmcnt(5)
M v[16:19],a[204:207],a[4:7],v[16:19]
BL v133,s[48:51],s77 offen lds
M v[20:23],a[200:203],a[8:11],v[20:23]
TR a[218:219],v151 offset:768
M v[20:23],a[204:207],a[12:15],v[20:23]
M v[24:27],a[200:203],a[16:19],v[24:27]
SA m0,s62,0xd800
TR a[220:221],v151 offset:8704
M v[24:27],a[204:207],a[20:23],v[24:27]
BL v134,s[48:51],s77 offen lds
M v[28:31],a[200:203],a[24:27],v[28:31]
TR a[222:223],v151 offset:8960
M v[28:31],a[204:207],a[28:31],v[28:31]
W lgkmcnt(6)
M v[32:35],a[208:211],a[0:3],v[32:35]
SA m0,s62,0xdc00
TR a[192:193],v150 offset:1024
W lgkmcnt(5)
M v[32:35],a[212:215],a[4:7],v[32:35]
BL v135,s[48:51],s77 offen lds
M v[36:39],a[208:211],a[8:11],v[36:39]
TR a[194:195],v150 offset:1280
M v[36:39],a[212:215],a[12:15],v[36:39]
M v[40:43],a[208:211],a[16:19],v[40:43]
SA s78,s61,0x380000
SA m0,s63,0xc000
TR a[196:197],v150 offset:9216
M v[40:43],a[212:215],a[20:23],v[40:43]
BL v136,s[52:55],s78 offen lds
M v[44:47],a[208:211],a[24:27],v[44:47]
TR a[198:199],v150 offset:9472
M v[44:47],a[212:215],a[28:31],v[44:47]
W lgkmcnt(6)
M v[48:51],a[216:219],a[0:3],v[48:51]
SA m0,s63,0xc400
TR a[200:201],v151 offset:1024
W lgkmcnt(5)
M v[48:51],a[220:223],a[4:7],v[48:51]
BL v137,s[52:55],s78 offen lds
M v[52:55],a[216:219],a[8:11],v[52:55]
TR a[202:203],v151 offset:1280
M v[52:55],a[220:223],a[12:15],v[52:55]
M v[56:59],a[216:219],a[16:19],v[56:59]
SA m0,s63,0xc800
TR a[204:205],v151 offset:9216
M v[56:59],a[220:223],a[20:23],v[56:59]
BL v138,s[52:55],s78 offen lds
M v[60:63],a[216:219],a[24:27],v[60:63]
TR a[206:207],v151 offset:9472
M v[60:63],a[220:223],a[28:31],v[60:63]
W lgkmcnt(6)
M v[64:67],a[192:195],a[0:3],v[64:67]
SA m0,s63,0xcc00
TR a[208:209],v150 offset:1536
W lgkmcnt(5)
M v[64:67],a[196:199],a[4:7],v[64:67]
BL v139,s[52:55],s78 offen lds
M v[68:71],a[192:195],a[8:11],v[68:71]
TR a[210:211],v150 offset:1792
M v[68:71],a[196:199],a[12:15],v[68:71]
M v[72:75],a[192:195],a[16:19],v[72:75]
TR a[212:213],v150 offset:9728
M v[72:75],a[196:199],a[20:23],v[72:75]
M v[76:79],a[192:195],a[24:27],v[76:79]
TR a[214:215],v150 offset:9984
M v[76:79],a[196:199],a[28:31],v[76:79]
W lgkmcnt(6)
M v[80:83],a[200:203],a[0:3],v[80:83]
TR a[216:217],v151 offset:1536
W lgkmcnt(5)
M v[80:83],a[204:207],a[4:7],v[80:83]
TR a[218:219],v151 offset:1792
M v[84:87],a[200:203],a[8:11],v[84:87]
TR a[220:221],v151 offset:9728
M v[84:87],a[204:207],a[12:15],v[84:87]
TR a[222:223],v151 offset:9984
M v[88:91],a[200:203],a[16:19],v[88:91]
M v[88:91],a[204:207],a[20:23],v[88:91]
M v[92:95],a[200:203],a[24:27],v[92:95]
M v[92:95],a[204:207],a[28:31],v[92:95]
W vmcnt(12) lgkmcnt(0)
s_barrier
v_and_b32 v232,7,v255
v_lshlrev_b32 v232,6,v232
s_lshl_b32 s84,s38,9
SA s84,s84,0x3501000
v_add_u32 v232,s84,v232
buffer_load_dwordx2 v[234:235],v232,s[44:47],0 offen sc1
M v[96:99],a[208:211],a[0:3],v[96:99]
TR a[192:193],v146
M v[96:99],a[212:215],a[4:7],v[96:99]
M v[100:103],a[208:211],a[8:11],v[100:103]
TR a[194:195],v146 offset:256
M v[100:103],a[212:215],a[12:15],v[100:103]
RD a[224:227],v140
RD a[228:231],v141
M v[104:107],a[208:211],a[16:19],v[104:107]
TR a[196:197],v146 offset:8192
M v[104:107],a[212:215],a[20:23],v[104:107]
M v[108:111],a[208:211],a[24:27],v[108:111]
TR a[198:199],v146 offset:8448
M v[108:111],a[212:215],a[28:31],v[108:111]
RD a[232:235],v140 offset:2048
RD a[236:239],v141 offset:2048
M v[112:115],a[216:219],a[0:3],v[112:115]
M v[112:115],a[220:223],a[4:7],v[112:115]
TR a[200:201],v147
M v[116:119],a[216:219],a[8:11],v[116:119]
RD a[240:243],v140 offset:4096
RD a[244:247],v141 offset:4096
M v[116:119],a[220:223],a[12:15],v[116:119]
TR a[202:203],v147 offset:256
M v[120:123],a[216:219],a[16:19],v[120:123]
RD a[248:251],v140 offset:6144
RD a[252:255],v141 offset:6144
M v[120:123],a[220:223],a[20:23],v[120:123]
TR a[204:205],v147 offset:8192
M v[124:127],a[216:219],a[24:27],v[124:127]
M v[124:127],a[220:223],a[28:31],v[124:127]
TR a[206:207],v147 offset:8448
W lgkmcnt(13)
M v[0:3],a[192:195],a[224:227],v[0:3]
TR a[208:209],v146 offset:512
W lgkmcnt(11)
M v[0:3],a[196:199],a[228:231],v[0:3]
W lgkmcnt(10)
M v[4:7],a[192:195],a[232:235],v[4:7]
TR a[210:211],v146 offset:768
W lgkmcnt(10)
M v[4:7],a[196:199],a[236:239],v[4:7]
W lgkmcnt(8)
M v[8:11],a[192:195],a[240:243],v[8:11]
TR a[212:213],v146 offset:8704
W lgkmcnt(8)
M v[8:11],a[196:199],a[244:247],v[8:11]
W lgkmcnt(6)
M v[12:15],a[192:195],a[248:251],v[12:15]
TR a[214:215],v146 offset:8960
W lgkmcnt(6)
M v[12:15],a[196:199],a[252:255],v[12:15]
M v[16:19],a[200:203],a[224:227],v[16:19]
TR a[216:217],v147 offset:512
W lgkmcnt(5)
M v[16:19],a[204:207],a[228:231],v[16:19]
M v[20:23],a[200:203],a[232:235],v[20:23]
TR a[218:219],v147 offset:768
M v[20:23],a[204:207],a[236:239],v[20:23]
M v[24:27],a[200:203],a[240:243],v[24:27]
TR a[220:221],v147 offset:8704
M v[24:27],a[204:207],a[244:247],v[24:27]
M v[28:31],a[200:203],a[248:251],v[28:31]
TR a[222:223],v147 offset:8960
M v[28:31],a[204:207],a[252:255],v[28:31]
W lgkmcnt(6)
M v[32:35],a[208:211],a[224:227],v[32:35]
TR a[192:193],v146 offset:1024
W lgkmcnt(5)
M v[32:35],a[212:215],a[228:231],v[32:35]
M v[36:39],a[208:211],a[232:235],v[36:39]
TR a[194:195],v146 offset:1280
M v[36:39],a[212:215],a[236:239],v[36:39]
M v[40:43],a[208:211],a[240:243],v[40:43]
TR a[196:197],v146 offset:9216
M v[40:43],a[212:215],a[244:247],v[40:43]
M v[44:47],a[208:211],a[248:251],v[44:47]
TR a[198:199],v146 offset:9472
M v[44:47],a[212:215],a[252:255],v[44:47]
W lgkmcnt(6)
M v[48:51],a[216:219],a[224:227],v[48:51]
TR a[200:201],v147 offset:1024
W lgkmcnt(5)
M v[48:51],a[220:223],a[228:231],v[48:51]
M v[52:55],a[216:219],a[232:235],v[52:55]
TR a[202:203],v147 offset:1280
M v[52:55],a[220:223],a[236:239],v[52:55]
M v[56:59],a[216:219],a[240:243],v[56:59]
TR a[204:205],v147 offset:9216
M v[56:59],a[220:223],a[244:247],v[56:59]
M v[60:63],a[216:219],a[248:251],v[60:63]
TR a[206:207],v147 offset:9472
M v[60:63],a[220:223],a[252:255],v[60:63]
W lgkmcnt(6)
M v[64:67],a[192:195],a[224:227],v[64:67]
TR a[208:209],v146 offset:1536
W lgkmcnt(5)
M v[64:67],a[196:199],a[228:231],v[64:67]
M v[68:71],a[192:195],a[232:235],v[68:71]
TR a[210:211],v146 offset:1792
M v[68:71],a[196:199],a[236:239],v[68:71]
M v[72:75],a[192:195],a[240:243],v[72:75]
TR a[212:213],v146 offset:9728
M v[72:75],a[196:199],a[244:247],v[72:75]
M v[76:79],a[192:195],a[248:251],v[76:79]
TR a[214:215],v146 offset:9984
M v[76:79],a[196:199],a[252:255],v[76:79]
W lgkmcnt(6)
M v[80:83],a[200:203],a[224:227],v[80:83]
TR a[216:217],v147 offset:1536
W lgkmcnt(5)
M v[80:83],a[204:207],a[228:231],v[80:83]
TR a[218:219],v147 offset:1792
M v[84:87],a[200:203],a[232:235],v[84:87]
TR a[220:221],v147 offset:9728
M v[84:87],a[204:207],a[236:239],v[84:87]
TR a[222:223],v147 offset:9984
M v[88:91],a[200:203],a[240:243],v[88:91]
M v[88:91],a[204:207],a[244:247],v[88:91]
M v[92:95],a[200:203],a[248:251],v[92:95]
M v[92:95],a[204:207],a[252:255],v[92:95]
W vmcnt(0) lgkmcnt(0)
v_cmp_ne_u32 s[96:97],s42,v234
v_cmp_ne_u32 vcc,s43,v235
s_nop 4
s_or_b64 vcc,vcc,s[96:97]
s_cbranch_vccnz fk_spin_kv
fk_spin_kv_back:
s_barrier
M v[96:99],a[208:211],a[224:227],v[96:99]
s_lshl_b32 s93,s36,13
s_lshl_b32 s92,s38,19
SA s92,s92,0
SA m0,s93,0
TR a[192:193],v148
M v[96:99],a[212:215],a[228:231],v[96:99]
BL v208,s[44:47],s92 offen sc1 lds
M v[100:103],a[208:211],a[232:235],v[100:103]
TR a[194:195],v148 offset:256
M v[100:103],a[212:215],a[236:239],v[100:103]
RD a[0:3],v142
RD a[4:7],v143
M v[104:107],a[208:211],a[240:243],v[104:107]
TR a[196:197],v148 offset:8192
M v[104:107],a[212:215],a[244:247],v[104:107]
BL v208,s[44:47],s92 offen offset:1024 sc1 lds
M v[108:111],a[208:211],a[248:251],v[108:111]
TR a[198:199],v148 offset:8448
M v[108:111],a[212:215],a[252:255],v[108:111]
RD a[8:11],v142 offset:2048
RD a[12:15],v143 offset:2048
M v[112:115],a[216:219],a[224:227],v[112:115]
BL v208,s[44:47],s92 offen offset:2048 sc1 lds
)~"
R"~(M v[112:115],a[220:223],a[228:231],v[112:115]
TR a[200:201],v149
M v[116:119],a[216:219],a[232:235],v[116:119]
RD a[16:19],v142 offset:4096
RD a[20:23],v143 offset:4096
M v[116:119],a[220:223],a[236:239],v[116:119]
TR a[202:203],v149 offset:256
M v[120:123],a[216:219],a[240:243],v[120:123]
RD a[24:27],v142 offset:6144
RD a[28:31],v143 offset:6144
M v[120:123],a[220:223],a[244:247],v[120:123]
TR a[204:205],v149 offset:8192
M v[124:127],a[216:219],a[248:251],v[124:127]
M v[124:127],a[220:223],a[252:255],v[124:127]
TR a[206:207],v149 offset:8448
W lgkmcnt(13)
M v[0:3],a[192:195],a[0:3],v[0:3]
TR a[208:209],v148 offset:512
W lgkmcnt(11)
M v[0:3],a[196:199],a[4:7],v[0:3]
BL v208,s[44:47],s92 offen offset:3072 sc1 lds
W lgkmcnt(10)
M v[4:7],a[192:195],a[8:11],v[4:7]
TR a[210:211],v148 offset:768
W lgkmcnt(10)
M v[4:7],a[196:199],a[12:15],v[4:7]
W lgkmcnt(8)
M v[8:11],a[192:195],a[16:19],v[8:11]
SA m0,s93,0x1000
TR a[212:213],v148 offset:8704
W lgkmcnt(8)
M v[8:11],a[196:199],a[20:23],v[8:11]
BL v209,s[44:47],s92 offen sc1 lds
W lgkmcnt(6)
M v[12:15],a[192:195],a[24:27],v[12:15]
TR a[214:215],v148 offset:8960
W lgkmcnt(6)
M v[12:15],a[196:199],a[28:31],v[12:15]
M v[16:19],a[200:203],a[0:3],v[16:19]
TR a[216:217],v149 offset:512
W lgkmcnt(5)
M v[16:19],a[204:207],a[4:7],v[16:19]
BL v209,s[44:47],s92 offen offset:1024 sc1 lds
M v[20:23],a[200:203],a[8:11],v[20:23]
TR a[218:219],v149 offset:768
M v[20:23],a[204:207],a[12:15],v[20:23]
M v[24:27],a[200:203],a[16:19],v[24:27]
TR a[220:221],v149 offset:8704
M v[24:27],a[204:207],a[20:23],v[24:27]
BL v209,s[44:47],s92 offen offset:2048 sc1 lds
M v[28:31],a[200:203],a[24:27],v[28:31]
TR a[222:223],v149 offset:8960
M v[28:31],a[204:207],a[28:31],v[28:31]
W lgkmcnt(6)
M v[32:35],a[208:211],a[0:3],v[32:35]
TR a[192:193],v148 offset:1024
W lgkmcnt(5)
M v[32:35],a[212:215],a[4:7],v[32:35]
BL v209,s[44:47],s92 offen offset:3072 sc1 lds
M v[36:39],a[208:211],a[8:11],v[36:39]
TR a[194:195],v148 offset:1280
M v[36:39],a[212:215],a[12:15],v[36:39]
M v[40:43],a[208:211],a[16:19],v[40:43]
TR a[196:197],v148 offset:9216
M v[40:43],a[212:215],a[20:23],v[40:43]
M v[44:47],a[208:211],a[24:27],v[44:47]
TR a[198:199],v148 offset:9472
M v[44:47],a[212:215],a[28:31],v[44:47]
W lgkmcnt(6)
M v[48:51],a[216:219],a[0:3],v[48:51]
TR a[200:201],v149 offset:1024
W lgkmcnt(5)
M v[48:51],a[220:223],a[4:7],v[48:51]
M v[52:55],a[216:219],a[8:11],v[52:55]
TR a[202:203],v149 offset:1280
M v[52:55],a[220:223],a[12:15],v[52:55]
M v[56:59],a[216:219],a[16:19],v[56:59]
TR a[204:205],v149 offset:9216
M v[56:59],a[220:223],a[20:23],v[56:59]
M v[60:63],a[216:219],a[24:27],v[60:63]
TR a[206:207],v149 offset:9472
M v[60:63],a[220:223],a[28:31],v[60:63]
W lgkmcnt(6)
M v[64:67],a[192:195],a[0:3],v[64:67]
TR a[208:209],v148 offset:1536
W lgkmcnt(5)
M v[64:67],a[196:199],a[4:7],v[64:67]
M v[68:71],a[192:195],a[8:11],v[68:71]
TR a[210:211],v148 offset:1792
M v[68:71],a[196:199],a[12:15],v[68:71]
M v[72:75],a[192:195],a[16:19],v[72:75]
TR a[212:213],v148 offset:9728
M v[72:75],a[196:199],a[20:23],v[72:75]
M v[76:79],a[192:195],a[24:27],v[76:79]
TR a[214:215],v148 offset:9984
M v[76:79],a[196:199],a[28:31],v[76:79]
W lgkmcnt(6)
M v[80:83],a[200:203],a[0:3],v[80:83]
TR a[216:217],v149 offset:1536
W lgkmcnt(5)
M v[80:83],a[204:207],a[4:7],v[80:83]
TR a[218:219],v149 offset:1792
M v[84:87],a[200:203],a[8:11],v[84:87]
TR a[220:221],v149 offset:9728
M v[84:87],a[204:207],a[12:15],v[84:87]
TR a[222:223],v149 offset:9984
M v[88:91],a[200:203],a[16:19],v[88:91]
M v[88:91],a[204:207],a[20:23],v[88:91]
M v[92:95],a[200:203],a[24:27],v[92:95]
M v[92:95],a[204:207],a[28:31],v[92:95]
W lgkmcnt(0)
s_barrier
M v[96:99],a[208:211],a[0:3],v[96:99]
s_lshl_b32 s93,s36,13
s_lshl_b32 s92,s38,19
SA s92,s92,0x8000
SA m0,s93,0x8000
M v[96:99],a[212:215],a[4:7],v[96:99]
BL v208,s[44:47],s92 offen sc1 lds
M v[100:103],a[208:211],a[8:11],v[100:103]
M v[100:103],a[212:215],a[12:15],v[100:103]
M v[104:107],a[208:211],a[16:19],v[104:107]
M v[104:107],a[212:215],a[20:23],v[104:107]
BL v208,s[44:47],s92 offen offset:1024 sc1 lds
M v[108:111],a[208:211],a[24:27],v[108:111]
M v[108:111],a[212:215],a[28:31],v[108:111]
M v[112:115],a[216:219],a[0:3],v[112:115]
BL v208,s[44:47],s92 offen offset:2048 sc1 lds
M v[112:115],a[220:223],a[4:7],v[112:115]
M v[116:119],a[216:219],a[8:11],v[116:119]
M v[116:119],a[220:223],a[12:15],v[116:119]
M v[120:123],a[216:219],a[16:19],v[120:123]
M v[120:123],a[220:223],a[20:23],v[120:123]
M v[124:127],a[216:219],a[24:27],v[124:127]
M v[124:127],a[220:223],a[28:31],v[124:127]
s_nop 0
BL v208,s[44:47],s92 offen offset:3072 sc1 lds
SA m0,s93,0x9000
s_nop 0
BL v209,s[44:47],s92 offen sc1 lds
s_nop 0
BL v209,s[44:47],s92 offen offset:1024 sc1 lds
s_nop 0
BL v209,s[44:47],s92 offen offset:2048 sc1 lds
s_nop 0
BL v209,s[44:47],s92 offen offset:3072 sc1 lds
s_mov_b32 s80,0x3e0293ee
s_mov_b32 s81,0x3e0293ee
s_lshl_b32 s84,s40,14
s_lshl_b32 s85,s41,15
SA s85,s85,0x18000
SA s85,s85,s84
v_add_u32 v224,s85,v254
s_lshl_b32 s86,s36,13
SA s86,s86,0x10000
s_lshl_b32 s87,s38,19
SA s87,s87,0x1000000
v_pk_mul_f32 v[0:1],v[0:1],s[80:81]
v_pk_mul_f32 v[2:3],v[2:3],s[80:81]
v_pk_mul_f32 v[16:17],v[16:17],s[80:81]
v_pk_mul_f32 v[18:19],v[18:19],s[80:81]
CV v128,v0,v1
CV v129,v2,v3
CV v130,v16,v17
CV v131,v18,v19
WR v224,v[128:131]
SA m0,s86,0
s_nop 0
BL v208,s[44:47],s87 offen sc1 lds
s_lshl_b32 s84,s40,12
SA s85,s84,0x18000
v_add_u32 v225,s85,v254
v_pk_mul_f32 v[4:5],v[4:5],s[80:81]
v_pk_mul_f32 v[6:7],v[6:7],s[80:81]
v_pk_mul_f32 v[20:21],v[20:21],s[80:81]
v_pk_mul_f32 v[22:23],v[22:23],s[80:81]
CV v132,v4,v5
CV v133,v6,v7
CV v134,v20,v21
CV v135,v22,v23
WR v224,v[132:135] offset:4096
BL v208,s[44:47],s87 offen offset:1024 sc1 lds
SA s85,s84,0x20000
v_add_u32 v212,s85,v254
s_sub_u32 s60,16,s39
v_pk_mul_f32 v[8:9],v[8:9],s[80:81]
v_pk_mul_f32 v[10:11],v[10:11],s[80:81]
v_pk_mul_f32 v[24:25],v[24:25],s[80:81]
v_pk_mul_f32 v[26:27],v[26:27],s[80:81]
CV v136,v8,v9
CV v137,v10,v11
CV v138,v24,v25
CV v139,v26,v27
WR v224,v[136:139] offset:8192
BL v208,s[44:47],s87 offen offset:2048 sc1 lds
SA s61,s39,1
s_lshl_b32 s84,s36,13
SA s65,s84,0
v_pk_mul_f32 v[12:13],v[12:13],s[80:81]
v_pk_mul_f32 v[14:15],v[14:15],s[80:81]
v_pk_mul_f32 v[28:29],v[28:29],s[80:81]
v_pk_mul_f32 v[30:31],v[30:31],s[80:81]
CV v140,v12,v13
CV v141,v14,v15
CV v142,v28,v29
CV v143,v30,v31
WR v224,v[140:143] offset:12288
BL v208,s[44:47],s87 offen offset:3072 sc1 lds
SA s66,s84,0x18000
s_lshl_b32 s84,s38,19
SA s69,s84,0
v_pk_mul_f32 v[32:33],v[32:33],s[80:81]
v_pk_mul_f32 v[34:35],v[34:35],s[80:81]
v_pk_mul_f32 v[48:49],v[48:49],s[80:81]
v_pk_mul_f32 v[50:51],v[50:51],s[80:81]
CV v128,v32,v33
CV v129,v34,v35
CV v130,v48,v49
CV v131,v50,v51
WR v224,v[128:131] offset:1024
SA m0,s86,0x1000
s_nop 0
BL v209,s[44:47],s87 offen sc1 lds
SA s77,s84,0x1000000
SA s64,s77,0x8000
SA s63,s69,0x10000
v_pk_mul_f32 v[36:37],v[36:37],s[80:81]
v_pk_mul_f32 v[38:39],v[38:39],s[80:81]
v_pk_mul_f32 v[52:53],v[52:53],s[80:81]
v_pk_mul_f32 v[54:55],v[54:55],s[80:81]
CV v132,v36,v37
CV v133,v38,v39
CV v134,v52,v53
CV v135,v54,v55
WR v224,v[132:135] offset:5120
BL v209,s[44:47],s87 offen offset:1024 sc1 lds
s_lshl_b32 s67,s60,15
SA s67,s67,s69
s_lshl_b32 s68,s61,15
v_pk_mul_f32 v[40:41],v[40:41],s[80:81]
v_pk_mul_f32 v[42:43],v[42:43],s[80:81]
v_pk_mul_f32 v[56:57],v[56:57],s[80:81]
v_pk_mul_f32 v[58:59],v[58:59],s[80:81]
CV v136,v40,v41
CV v137,v42,v43
CV v138,v56,v57
CV v139,v58,v59
WR v224,v[136:139] offset:9216
BL v209,s[44:47],s87 offen offset:2048 sc1 lds
SA s68,s68,s69
s_mov_b32 s70,0x40000000
s_lshl_b32 s84,s41,10
v_pk_mul_f32 v[44:45],v[44:45],s[80:81]
v_pk_mul_f32 v[46:47],v[46:47],s[80:81]
v_pk_mul_f32 v[60:61],v[60:61],s[80:81]
v_pk_mul_f32 v[62:63],v[62:63],s[80:81]
CV v140,v44,v45
CV v141,v46,v47
CV v142,v60,v61
CV v143,v62,v63
WR v224,v[140:143] offset:13312
BL v209,s[44:47],s87 offen offset:3072 sc1 lds
v_add_u32 v210,s84,v254
SA s84,s84,0x18000
v_add_u32 v211,s84,v254
v_pk_mul_f32 v[64:65],v[64:65],s[80:81]
v_pk_mul_f32 v[66:67],v[66:67],s[80:81]
v_pk_mul_f32 v[80:81],v[80:81],s[80:81]
v_pk_mul_f32 v[82:83],v[82:83],s[80:81]
CV v128,v64,v65
CV v129,v66,v67
CV v130,v80,v81
CV v131,v82,v83
WR v224,v[128:131] offset:2048
s_sub_u32 s84,15,s39
s_lshl_b32 s84,s84,3
s_lshl_b32 s85,s41,2
v_pk_mul_f32 v[68:69],v[68:69],s[80:81]
v_pk_mul_f32 v[70:71],v[70:71],s[80:81]
v_pk_mul_f32 v[84:85],v[84:85],s[80:81]
v_pk_mul_f32 v[86:87],v[86:87],s[80:81]
CV v132,v68,v69
CV v133,v70,v71
CV v134,v84,v85
CV v135,v86,v87
WR v224,v[132:135] offset:6144
SA s84,s84,s85
SA s84,s84,s40
s_mul_i32 s84,s84,0x24000
v_pk_mul_f32 v[72:73],v[72:73],s[80:81]
v_pk_mul_f32 v[74:75],v[74:75],s[80:81]
v_pk_mul_f32 v[88:89],v[88:89],s[80:81]
v_pk_mul_f32 v[90:91],v[90:91],s[80:81]
CV v136,v72,v73
CV v137,v74,v75
CV v138,v88,v89
CV v139,v90,v91
WR v224,v[136:139] offset:10240
s_lshl_b32 s85,s38,12
SA s84,s84,s85
SA s71,s84,0x2000000
v_pk_mul_f32 v[76:77],v[76:77],s[80:81]
v_pk_mul_f32 v[78:79],v[78:79],s[80:81]
v_pk_mul_f32 v[92:93],v[92:93],s[80:81]
v_pk_mul_f32 v[94:95],v[94:95],s[80:81]
CV v140,v76,v77
CV v141,v78,v79
CV v142,v92,v93
CV v143,v94,v95
WR v224,v[140:143] offset:14336
s_mov_b32 s84,s39
s_lshl_b32 s84,s84,3
s_lshl_b32 s85,s41,2
v_pk_mul_f32 v[96:97],v[96:97],s[80:81]
v_pk_mul_f32 v[98:99],v[98:99],s[80:81]
v_pk_mul_f32 v[112:113],v[112:113],s[80:81]
v_pk_mul_f32 v[114:115],v[114:115],s[80:81]
CV v128,v96,v97
CV v129,v98,v99
CV v130,v112,v113
CV v131,v114,v115
WR v224,v[128:131] offset:3072
SA s84,s84,s85
SA s84,s84,s40
s_mul_i32 s84,s84,0x24000
v_pk_mul_f32 v[100:101],v[100:101],s[80:81]
v_pk_mul_f32 v[102:103],v[102:103],s[80:81]
v_pk_mul_f32 v[116:117],v[116:117],s[80:81]
v_pk_mul_f32 v[118:119],v[118:119],s[80:81]
CV v132,v100,v101
CV v133,v102,v103
CV v134,v116,v117
CV v135,v118,v119
WR v224,v[132:135] offset:7168
s_lshl_b32 s85,s38,12
SA s84,s84,s85
SA s72,s84,0x2000000
v_pk_mul_f32 v[104:105],v[104:105],s[80:81]
v_pk_mul_f32 v[106:107],v[106:107],s[80:81]
v_pk_mul_f32 v[120:121],v[120:121],s[80:81]
v_pk_mul_f32 v[122:123],v[122:123],s[80:81]
CV v136,v104,v105
CV v137,v106,v107
CV v138,v120,v121
CV v139,v122,v123
WR v224,v[136:139] offset:11264
v_pk_mul_f32 v[108:109],v[108:109],s[80:81]
v_pk_mul_f32 v[110:111],v[110:111],s[80:81]
v_pk_mul_f32 v[124:125],v[124:125],s[80:81]
v_pk_mul_f32 v[126:127],v[126:127],s[80:81]
CV v140,v108,v109
CV v141,v110,v111
CV v142,v124,v125
CV v143,v126,v127
WR v224,v[140:143] offset:15360
W vmcnt(16) lgkmcnt(0)
s_barrier
RD a[192:195],v210
RD a[196:199],v210 offset:4096
RD a[200:203],v210 offset:8192
RD a[204:207],v210 offset:12288
RD a[208:211],v210 offset:2048
RD a[212:215],v210 offset:6144
RD a[216:219],v210 offset:10240
RD a[220:223],v210 offset:14336
RD a[224:227],v210 offset:16384
RD a[228:231],v210 offset:20480
RD a[232:235],v210 offset:24576
RD a[236:239],v210 offset:28672
RD a[240:243],v210 offset:18432
RD a[244:247],v210 offset:22528
RD a[248:251],v210 offset:26624
RD a[252:255],v210 offset:30720
RD a[128:131],v225
RD a[132:135],v225 offset:1024
RD a[136:139],v225 offset:2048
RD a[140:143],v225 offset:3072
RD a[144:147],v225 offset:8192
RD a[148:151],v225 offset:9216
RD a[152:155],v225 offset:10240
RD a[156:159],v225 offset:11264
RD a[160:163],v225 offset:16384
RD a[164:167],v225 offset:17408
RD a[168:171],v225 offset:18432
RD a[172:175],v225 offset:19456
RD a[176:179],v225 offset:24576
RD a[180:183],v225 offset:25600
RD a[184:187],v225 offset:26624
RD a[188:191],v225 offset:27648
v_xor_b32 v210,0x8000,v210
W lgkmcnt(0)
s_cmp_lg_u32 s57,0
s_cbranch_scc1 fk_safe1
W vmcnt(0) lgkmcnt(0)
s_barrier
M v[0:3],a[192:195],a[128:131],0
M v[0:3],a[196:199],a[132:135],v[0:3]
M v[0:3],a[200:203],a[136:139],v[0:3]
s_mov_b32 m0,s65
M v[0:3],a[204:207],a[140:143],v[0:3]
BL v208,s[44:47],s63 offen sc1 lds
M v[4:7],a[208:211],a[128:131],0
M v[4:7],a[212:215],a[132:135],v[4:7]
M v[4:7],a[216:219],a[136:139],v[4:7]
M v[4:7],a[220:223],a[140:143],v[4:7]
BL v208,s[44:47],s63 offen offset:1024 sc1 lds
M v[8:11],a[224:227],a[128:131],0
s_nop 0
EX v0,v0
M v[8:11],a[228:231],a[132:135],v[8:11]
M v[8:11],a[232:235],a[136:139],v[8:11]
EX v1,v1
M v[8:11],a[236:239],a[140:143],v[8:11]
BL v208,s[44:47],s63 offen offset:2048 sc1 lds
M v[12:15],a[240:243],a[128:131],0
EX v2,v2
M v[12:15],a[244:247],a[132:135],v[12:15]
M v[12:15],a[248:251],a[136:139],v[12:15]
EX v3,v3
M v[12:15],a[252:255],a[140:143],v[12:15]
BL v208,s[44:47],s63 offen offset:3072 sc1 lds
M v[16:19],a[192:195],a[144:147],0
EX v4,v4
M v[16:19],a[196:199],a[148:151],v[16:19]
M v[16:19],a[200:203],a[152:155],v[16:19]
SA m0,s65,0x1000
EX v5,v5
M v[16:19],a[204:207],a[156:159],v[16:19]
BL v209,s[44:47],s63 offen sc1 lds
M v[20:23],a[208:211],a[144:147],0
EX v6,v6
M v[20:23],a[212:215],a[148:151],v[20:23]
M v[20:23],a[216:219],a[152:155],v[20:23]
EX v7,v7
M v[20:23],a[220:223],a[156:159],v[20:23]
BL v209,s[44:47],s63 offen offset:1024 sc1 lds
M v[24:27],a[224:227],a[144:147],0
EX v8,v8
M v[24:27],a[228:231],a[148:151],v[24:27]
M v[24:27],a[232:235],a[152:155],v[24:27]
EX v9,v9
M v[24:27],a[236:239],a[156:159],v[24:27]
BL v209,s[44:47],s63 offen offset:2048 sc1 lds
M v[28:31],a[240:243],a[144:147],0
EX v10,v10
M v[28:31],a[244:247],a[148:151],v[28:31]
M v[28:31],a[248:251],a[152:155],v[28:31]
EX v11,v11
M v[28:31],a[252:255],a[156:159],v[28:31]
BL v209,s[44:47],s63 offen offset:3072 sc1 lds
M v[32:35],a[192:195],a[160:163],0
s_xor_b32 s65,s65,0x8000
EX v12,v12
M v[32:35],a[196:199],a[164:167],v[32:35]
EX v13,v13
M v[32:35],a[200:203],a[168:171],v[32:35]
SA s63,s63,0x8000
EX v14,v14
M v[32:35],a[204:207],a[172:175],v[32:35]
M v[36:39],a[208:211],a[160:163],0
s_cmp_eq_u32 s63,s67
EX v15,v15
M v[36:39],a[212:215],a[164:167],v[36:39]
EX v16,v16
M v[36:39],a[216:219],a[168:171],v[36:39]
s_cselect_b32 s63,s69,s63
EX v17,v17
M v[36:39],a[220:223],a[172:175],v[36:39]
CV v64,v0,v1
CV v65,v2,v3
M v[40:43],a[224:227],a[160:163],0
s_cselect_b32 s67,s68,s67
EX v18,v18
M v[40:43],a[228:231],a[164:167],v[40:43]
EX v19,v19
M v[40:43],a[232:235],a[168:171],v[40:43]
s_cselect_b32 s69,s70,s69
)~"
R"~(EX v20,v20
M v[40:43],a[236:239],a[172:175],v[40:43]
CV v66,v4,v5
CV v67,v6,v7
M v[44:47],a[240:243],a[160:163],0
EX v21,v21
M v[44:47],a[244:247],a[164:167],v[44:47]
EX v22,v22
M v[44:47],a[248:251],a[168:171],v[44:47]
EX v23,v23
M v[44:47],a[252:255],a[172:175],v[44:47]
CV v68,v8,v9
CV v69,v10,v11
M v[48:51],a[192:195],a[176:179],0
EX v24,v24
M v[48:51],a[196:199],a[180:183],v[48:51]
EX v25,v25
M v[48:51],a[200:203],a[184:187],v[48:51]
EX v26,v26
M v[48:51],a[204:207],a[188:191],v[48:51]
CV v70,v12,v13
CV v71,v14,v15
M v[52:55],a[208:211],a[176:179],0
EX v27,v27
M v[52:55],a[212:215],a[180:183],v[52:55]
EX v28,v28
M v[52:55],a[216:219],a[184:187],v[52:55]
EX v29,v29
M v[52:55],a[220:223],a[188:191],v[52:55]
M v[56:59],a[224:227],a[176:179],0
EX v30,v30
M v[56:59],a[228:231],a[180:183],v[56:59]
M v[56:59],a[232:235],a[184:187],v[56:59]
EX v31,v31
M v[56:59],a[236:239],a[188:191],v[56:59]
CV v72,v16,v17
CV v73,v18,v19
M v[60:63],a[240:243],a[176:179],0
EX v32,v32
M v[60:63],a[244:247],a[180:183],v[60:63]
CV v74,v20,v21
CV v75,v22,v23
M v[60:63],a[248:251],a[184:187],v[60:63]
EX v33,v33
M v[60:63],a[252:255],a[188:191],v[60:63]
CV v76,v24,v25
CV v77,v26,v27
EX v34,v34
RD a[192:195],v210
RD a[196:199],v210 offset:4096
EX v35,v35
CV v78,v28,v29
CV v79,v30,v31
EX v36,v36
RD a[200:203],v210 offset:8192
RD a[204:207],v210 offset:12288
EX v37,v37
EX v38,v38
RD a[208:211],v210 offset:2048
RD a[212:215],v210 offset:6144
EX v39,v39
EX v40,v40
RD a[216:219],v210 offset:10240
RD a[220:223],v210 offset:14336
EX v41,v41
EX v42,v42
RD a[224:227],v210 offset:16384
RD a[228:231],v210 offset:20480
EX v43,v43
EX v44,v44
RD a[232:235],v210 offset:24576
RD a[236:239],v210 offset:28672
EX v45,v45
EX v46,v46
RD a[240:243],v210 offset:18432
RD a[244:247],v210 offset:22528
EX v47,v47
CV v80,v32,v33
CV v81,v34,v35
EX v48,v48
RD a[248:251],v210 offset:26624
RD a[252:255],v210 offset:30720
EX v49,v49
v_xor_b32 v210,0x8000,v210
EX v50,v50
CV v82,v36,v37
CV v83,v38,v39
EX v51,v51
CV v84,v40,v41
CV v85,v42,v43
EX v52,v52
CV v86,v44,v45
CV v87,v46,v47
EX v53,v53
EX v54,v54
EX v55,v55
EX v56,v56
EX v57,v57
EX v58,v58
EX v59,v59
EX v60,v60
EX v61,v61
EX v62,v62
EX v63,v63
s_mov_b32 s75,0
s_mov_b32 s73,0
s_mov_b32 s74,0
s_mov_b32 s76,0
s_sub_u32 s62,s60,3
fk_F:
W vmcnt(0)
fk_F2:
W lgkmcnt(0)
s_barrier
M v[0:3],a[192:195],a[128:131],0
v_xor_b32 v211,0x8000,v211
CV v88,v48,v49
CV v89,v50,v51
M v[0:3],a[196:199],a[132:135],v[0:3]
RD v[96:99],v211
RD v[100:103],v211 offset:16384
M v[0:3],a[200:203],a[136:139],v[0:3]
s_mov_b32 m0,s66
CV v90,v52,v53
CV v91,v54,v55
M v[0:3],a[204:207],a[140:143],v[0:3]
BL v208,s[44:47],s64 offen sc1 lds
M v[4:7],a[208:211],a[128:131],0
CV v92,v56,v57
CV v93,v58,v59
M v[4:7],a[212:215],a[132:135],v[4:7]
RD v[104:107],v211 offset:2048
RD v[108:111],v211 offset:18432
M v[4:7],a[216:219],a[136:139],v[4:7]
CV v94,v60,v61
CV v95,v62,v63
M v[4:7],a[220:223],a[140:143],v[4:7]
BL v208,s[44:47],s64 offen offset:1024 sc1 lds
M v[8:11],a[224:227],a[128:131],0
EX v0,v0
M v[8:11],a[228:231],a[132:135],v[8:11]
RD v[112:115],v211 offset:4096
RD v[116:119],v211 offset:20480
M v[8:11],a[232:235],a[136:139],v[8:11]
EX v1,v1
M v[8:11],a[236:239],a[140:143],v[8:11]
BL v208,s[44:47],s64 offen offset:2048 sc1 lds
M v[12:15],a[240:243],a[128:131],0
EX v2,v2
M v[12:15],a[244:247],a[132:135],v[12:15]
RD v[120:123],v211 offset:6144
RD v[124:127],v211 offset:22528
M v[12:15],a[248:251],a[136:139],v[12:15]
EX v3,v3
M v[12:15],a[252:255],a[140:143],v[12:15]
BL v208,s[44:47],s64 offen offset:3072 sc1 lds
M v[16:19],a[192:195],a[144:147],0
EX v4,v4
M v[16:19],a[196:199],a[148:151],v[16:19]
RD v[128:131],v211 offset:8192
RD v[132:135],v211 offset:24576
M v[16:19],a[200:203],a[152:155],v[16:19]
SA m0,s66,0x1000
EX v5,v5
M v[16:19],a[204:207],a[156:159],v[16:19]
BL v209,s[44:47],s64 offen sc1 lds
M v[20:23],a[208:211],a[144:147],0
EX v6,v6
M v[20:23],a[212:215],a[148:151],v[20:23]
RD v[136:139],v211 offset:10240
RD v[140:143],v211 offset:26624
M v[20:23],a[216:219],a[152:155],v[20:23]
EX v7,v7
M v[20:23],a[220:223],a[156:159],v[20:23]
BL v209,s[44:47],s64 offen offset:1024 sc1 lds
M v[24:27],a[224:227],a[144:147],0
EX v8,v8
M v[24:27],a[228:231],a[148:151],v[24:27]
RD v[144:147],v211 offset:12288
RD v[148:151],v211 offset:28672
M v[24:27],a[232:235],a[152:155],v[24:27]
EX v9,v9
M v[24:27],a[236:239],a[156:159],v[24:27]
BL v209,s[44:47],s64 offen offset:2048 sc1 lds
M v[28:31],a[240:243],a[144:147],0
EX v10,v10
M v[28:31],a[244:247],a[148:151],v[28:31]
RD v[152:155],v211 offset:14336
RD v[156:159],v211 offset:30720
M v[28:31],a[248:251],a[152:155],v[28:31]
EX v11,v11
M v[28:31],a[252:255],a[156:159],v[28:31]
BL v209,s[44:47],s64 offen offset:3072 sc1 lds
M v[32:35],a[192:195],a[160:163],0
SA s64,s64,0x8000
EX v12,v12
M v[32:35],a[196:199],a[164:167],v[32:35]
W lgkmcnt(0)
M v[32:35],a[200:203],a[168:171],v[32:35]
s_mov_b32 m0,s65
EX v13,v13
M v[32:35],a[204:207],a[172:175],v[32:35]
BL v208,s[44:47],s63 offen sc1 lds
M v[36:39],a[208:211],a[160:163],0
s_xor_b32 s66,s66,0x8000
EX v14,v14
M v[36:39],a[212:215],a[164:167],v[36:39]
M v[36:39],a[216:219],a[168:171],v[36:39]
EX v15,v15
M v[36:39],a[220:223],a[172:175],v[36:39]
BL v208,s[44:47],s63 offen offset:1024 sc1 lds
M v[40:43],a[224:227],a[160:163],0
EX v16,v16
M v[40:43],a[228:231],a[164:167],v[40:43]
M v[40:43],a[232:235],a[168:171],v[40:43]
EX v17,v17
M v[40:43],a[236:239],a[172:175],v[40:43]
BL v208,s[44:47],s63 offen offset:2048 sc1 lds
M v[44:47],a[240:243],a[160:163],0
EX v18,v18
M v[44:47],a[244:247],a[164:167],v[44:47]
M v[44:47],a[248:251],a[168:171],v[44:47]
EX v19,v19
M v[44:47],a[252:255],a[172:175],v[44:47]
BL v208,s[44:47],s63 offen offset:3072 sc1 lds
M v[48:51],a[192:195],a[176:179],0
EX v20,v20
M v[48:51],a[196:199],a[180:183],v[48:51]
M v[48:51],a[200:203],a[184:187],v[48:51]
SA m0,s65,0x1000
EX v21,v21
M v[48:51],a[204:207],a[188:191],v[48:51]
BL v209,s[44:47],s63 offen sc1 lds
M v[52:55],a[208:211],a[176:179],0
EX v22,v22
M v[52:55],a[212:215],a[180:183],v[52:55]
M v[52:55],a[216:219],a[184:187],v[52:55]
EX v23,v23
M v[52:55],a[220:223],a[188:191],v[52:55]
BL v209,s[44:47],s63 offen offset:1024 sc1 lds
M v[56:59],a[224:227],a[176:179],0
EX v24,v24
M v[56:59],a[228:231],a[180:183],v[56:59]
M v[56:59],a[232:235],a[184:187],v[56:59]
EX v25,v25
M v[56:59],a[236:239],a[188:191],v[56:59]
BL v209,s[44:47],s63 offen offset:2048 sc1 lds
M v[60:63],a[240:243],a[176:179],0
EX v26,v26
M v[60:63],a[244:247],a[180:183],v[60:63]
M v[60:63],a[248:251],a[184:187],v[60:63]
EX v27,v27
M v[60:63],a[252:255],a[188:191],v[60:63]
BL v209,s[44:47],s63 offen offset:3072 sc1 lds
M a[0:3],v[96:99],v[64:67],0
s_xor_b32 s65,s65,0x8000
EX v28,v28
M a[0:3],v[100:103],v[68:71],a[0:3]
RD a[192:195],v210
RD a[196:199],v210 offset:4096
M a[4:7],v[104:107],v[64:67],0
SA s63,s63,0x8000
EX v29,v29
M a[4:7],v[108:111],v[68:71],a[4:7]
M a[8:11],v[112:115],v[64:67],0
s_cmp_eq_u32 s63,s67
EX v30,v30
M a[8:11],v[116:119],v[68:71],a[8:11]
RD a[200:203],v210 offset:8192
RD a[204:207],v210 offset:12288
M a[12:15],v[120:123],v[64:67],0
s_cselect_b32 s63,s69,s63
EX v31,v31
M a[12:15],v[124:127],v[68:71],a[12:15]
M a[16:19],v[128:131],v[64:67],0
s_cselect_b32 s67,s68,s67
EX v32,v32
M a[16:19],v[132:135],v[68:71],a[16:19]
RD a[208:211],v210 offset:2048
RD a[212:215],v210 offset:6144
M a[20:23],v[136:139],v[64:67],0
s_cselect_b32 s69,s70,s69
EX v33,v33
M a[20:23],v[140:143],v[68:71],a[20:23]
M a[24:27],v[144:147],v[64:67],0
EX v34,v34
M a[24:27],v[148:151],v[68:71],a[24:27]
RD a[216:219],v210 offset:10240
RD a[220:223],v210 offset:14336
M a[28:31],v[152:155],v[64:67],0
EX v35,v35
M a[28:31],v[156:159],v[68:71],a[28:31]
M v[188:191],v[204:207],v[64:67],0
EX v36,v36
M v[188:191],v[204:207],v[68:71],v[188:191]
RD a[224:227],v210 offset:16384
RD a[228:231],v210 offset:20480
M a[32:35],v[96:99],v[72:75],0
EX v37,v37
M a[32:35],v[100:103],v[76:79],a[32:35]
CV v64,v0,v1
CV v65,v2,v3
M a[36:39],v[104:107],v[72:75],0
EX v38,v38
M a[36:39],v[108:111],v[76:79],a[36:39]
RD a[232:235],v210 offset:24576
RD a[236:239],v210 offset:28672
M a[40:43],v[112:115],v[72:75],0
EX v39,v39
M a[40:43],v[116:119],v[76:79],a[40:43]
CV v66,v4,v5
CV v67,v6,v7
M a[44:47],v[120:123],v[72:75],0
EX v40,v40
M a[44:47],v[124:127],v[76:79],a[44:47]
RD a[240:243],v210 offset:18432
RD a[244:247],v210 offset:22528
M a[48:51],v[128:131],v[72:75],0
EX v41,v41
M a[48:51],v[132:135],v[76:79],a[48:51]
CV v68,v8,v9
CV v69,v10,v11
M a[52:55],v[136:139],v[72:75],0
EX v42,v42
M a[52:55],v[140:143],v[76:79],a[52:55]
RD a[248:251],v210 offset:26624
RD a[252:255],v210 offset:30720
M a[56:59],v[144:147],v[72:75],0
EX v43,v43
M a[56:59],v[148:151],v[76:79],a[56:59]
v_xor_b32 v210,0x8000,v210
M a[60:63],v[152:155],v[72:75],0
EX v44,v44
M a[60:63],v[156:159],v[76:79],a[60:63]
CV v70,v12,v13
CV v71,v14,v15
M v[192:195],v[204:207],v[72:75],0
EX v45,v45
M v[192:195],v[204:207],v[76:79],v[192:195]
M a[64:67],v[96:99],v[80:83],0
EX v46,v46
M a[64:67],v[100:103],v[84:87],a[64:67]
CV v72,v16,v17
CV v73,v18,v19
M a[68:71],v[104:107],v[80:83],0
EX v47,v47
M a[68:71],v[108:111],v[84:87],a[68:71]
CV v74,v20,v21
CV v75,v22,v23
M a[72:75],v[112:115],v[80:83],0
EX v48,v48
M a[72:75],v[116:119],v[84:87],a[72:75]
CV v76,v24,v25
CV v77,v26,v27
M a[76:79],v[120:123],v[80:83],0
EX v49,v49
M a[76:79],v[124:127],v[84:87],a[76:79]
CV v78,v28,v29
CV v79,v30,v31
M a[80:83],v[128:131],v[80:83],0
EX v50,v50
M a[80:83],v[132:135],v[84:87],a[80:83]
M a[84:87],v[136:139],v[80:83],0
EX v51,v51
M a[84:87],v[140:143],v[84:87],a[84:87]
M a[88:91],v[144:147],v[80:83],0
EX v52,v52
M a[88:91],v[148:151],v[84:87],a[88:91]
M a[92:95],v[152:155],v[80:83],0
EX v53,v53
M a[92:95],v[156:159],v[84:87],a[92:95]
M v[196:199],v[204:207],v[80:83],0
EX v54,v54
M v[196:199],v[204:207],v[84:87],v[196:199]
M a[96:99],v[96:99],v[88:91],0
EX v55,v55
M a[96:99],v[100:103],v[92:95],a[96:99]
CV v80,v32,v33
CV v81,v34,v35
M a[100:103],v[104:107],v[88:91],0
EX v56,v56
M a[100:103],v[108:111],v[92:95],a[100:103]
CV v82,v36,v37
CV v83,v38,v39
M a[104:107],v[112:115],v[88:91],0
EX v57,v57
M a[104:107],v[116:119],v[92:95],a[104:107]
CV v84,v40,v41
CV v85,v42,v43
M a[108:111],v[120:123],v[88:91],0
EX v58,v58
M a[108:111],v[124:127],v[92:95],a[108:111]
CV v86,v44,v45
CV v87,v46,v47
M a[112:115],v[128:131],v[88:91],0
EX v59,v59
M a[112:115],v[132:135],v[92:95],a[112:115]
M a[116:119],v[136:139],v[88:91],0
EX v60,v60
M a[116:119],v[140:143],v[92:95],a[116:119]
M a[120:123],v[144:147],v[88:91],0
EX v61,v61
M a[120:123],v[148:151],v[92:95],a[120:123]
M a[124:127],v[152:155],v[88:91],0
EX v62,v62
M a[124:127],v[156:159],v[92:95],a[124:127]
M v[200:203],v[204:207],v[88:91],0
EX v63,v63
M v[200:203],v[204:207],v[92:95],v[200:203]
s_cmp_eq_u32 s62,0
s_cbranch_scc1 fk_L
fk_G:
W vmcnt(0) lgkmcnt(0)
s_barrier
M v[0:3],a[192:195],a[128:131],0
v_xor_b32 v211,0x8000,v211
CV v88,v48,v49
CV v89,v50,v51
M v[0:3],a[196:199],a[132:135],v[0:3]
RD v[96:99],v211
RD v[100:103],v211 offset:16384
M v[0:3],a[200:203],a[136:139],v[0:3]
s_mov_b32 m0,s66
CV v90,v52,v53
CV v91,v54,v55
M v[0:3],a[204:207],a[140:143],v[0:3]
BL v208,s[44:47],s64 offen sc1 lds
M v[4:7],a[208:211],a[128:131],0
CV v92,v56,v57
CV v93,v58,v59
M v[4:7],a[212:215],a[132:135],v[4:7]
RD v[104:107],v211 offset:2048
RD v[108:111],v211 offset:18432
M v[4:7],a[216:219],a[136:139],v[4:7]
CV v94,v60,v61
CV v95,v62,v63
M v[4:7],a[220:223],a[140:143],v[4:7]
BL v208,s[44:47],s64 offen offset:1024 sc1 lds
M v[8:11],a[224:227],a[128:131],0
EX v0,v0
M v[8:11],a[228:231],a[132:135],v[8:11]
RD v[112:115],v211 offset:4096
RD v[116:119],v211 offset:20480
M v[8:11],a[232:235],a[136:139],v[8:11]
EX v1,v1
M v[8:11],a[236:239],a[140:143],v[8:11]
BL v208,s[44:47],s64 offen offset:2048 sc1 lds
M v[12:15],a[240:243],a[128:131],0
EX v2,v2
M v[12:15],a[244:247],a[132:135],v[12:15]
RD v[120:123],v211 offset:6144
RD v[124:127],v211 offset:22528
M v[12:15],a[248:251],a[136:139],v[12:15]
EX v3,v3
M v[12:15],a[252:255],a[140:143],v[12:15]
BL v208,s[44:47],s64 offen offset:3072 sc1 lds
M v[16:19],a[192:195],a[144:147],0
EX v4,v4
M v[16:19],a[196:199],a[148:151],v[16:19]
RD v[128:131],v211 offset:8192
RD v[132:135],v211 offset:24576
M v[16:19],a[200:203],a[152:155],v[16:19]
SA m0,s66,0x1000
EX v5,v5
M v[16:19],a[204:207],a[156:159],v[16:19]
BL v209,s[44:47],s64 offen sc1 lds
M v[20:23],a[208:211],a[144:147],0
EX v6,v6
M v[20:23],a[212:215],a[148:151],v[20:23]
RD v[136:139],v211 offset:10240
RD v[140:143],v211 offset:26624
M v[20:23],a[216:219],a[152:155],v[20:23]
EX v7,v7
M v[20:23],a[220:223],a[156:159],v[20:23]
BL v209,s[44:47],s64 offen offset:1024 sc1 lds
M v[24:27],a[224:227],a[144:147],0
EX v8,v8
M v[24:27],a[228:231],a[148:151],v[24:27]
RD v[144:147],v211 offset:12288
RD v[148:151],v211 offset:28672
M v[24:27],a[232:235],a[152:155],v[24:27]
EX v9,v9
M v[24:27],a[236:239],a[156:159],v[24:27]
BL v209,s[44:47],s64 offen offset:2048 sc1 lds
M v[28:31],a[240:243],a[144:147],0
EX v10,v10
M v[28:31],a[244:247],a[148:151],v[28:31]
RD v[152:155],v211 offset:14336
RD v[156:159],v211 offset:30720
M v[28:31],a[248:251],a[152:155],v[28:31]
EX v11,v11
M v[28:31],a[252:255],a[156:159],v[28:31]
BL v209,s[44:47],s64 offen offset:3072 sc1 lds
M v[32:35],a[192:195],a[160:163],0
SA s64,s64,0x8000
EX v12,v12
M v[32:35],a[196:199],a[164:167],v[32:35]
W lgkmcnt(0)
M v[32:35],a[200:203],a[168:171],v[32:35]
s_mov_b32 m0,s65
EX v13,v13
M v[32:35],a[204:207],a[172:175],v[32:35]
BL v208,s[44:47],s63 offen sc1 lds
M v[36:39],a[208:211],a[160:163],0
s_xor_b32 s66,s66,0x8000
EX v14,v14
M v[36:39],a[212:215],a[164:167],v[36:39]
M v[36:39],a[216:219],a[168:171],v[36:39]
EX v15,v15
M v[36:39],a[220:223],a[172:175],v[36:39]
BL v208,s[44:47],s63 offen offset:1024 sc1 lds
M v[40:43],a[224:227],a[160:163],0
EX v16,v16
M v[40:43],a[228:231],a[164:167],v[40:43]
M v[40:43],a[232:235],a[168:171],v[40:43]
EX v17,v17
M v[40:43],a[236:239],a[172:175],v[40:43]
BL v208,s[44:47],s63 offen offset:2048 sc1 lds
M v[44:47],a[240:243],a[160:163],0
EX v18,v18
M v[44:47],a[244:247],a[164:167],v[44:47]
M v[44:47],a[248:251],a[168:171],v[44:47]
)~"
R"~(EX v19,v19
M v[44:47],a[252:255],a[172:175],v[44:47]
BL v208,s[44:47],s63 offen offset:3072 sc1 lds
M v[48:51],a[192:195],a[176:179],0
EX v20,v20
M v[48:51],a[196:199],a[180:183],v[48:51]
M v[48:51],a[200:203],a[184:187],v[48:51]
SA m0,s65,0x1000
EX v21,v21
M v[48:51],a[204:207],a[188:191],v[48:51]
BL v209,s[44:47],s63 offen sc1 lds
M v[52:55],a[208:211],a[176:179],0
EX v22,v22
M v[52:55],a[212:215],a[180:183],v[52:55]
M v[52:55],a[216:219],a[184:187],v[52:55]
EX v23,v23
M v[52:55],a[220:223],a[188:191],v[52:55]
BL v209,s[44:47],s63 offen offset:1024 sc1 lds
M v[56:59],a[224:227],a[176:179],0
EX v24,v24
M v[56:59],a[228:231],a[180:183],v[56:59]
M v[56:59],a[232:235],a[184:187],v[56:59]
EX v25,v25
M v[56:59],a[236:239],a[188:191],v[56:59]
BL v209,s[44:47],s63 offen offset:2048 sc1 lds
M v[60:63],a[240:243],a[176:179],0
EX v26,v26
M v[60:63],a[244:247],a[180:183],v[60:63]
M v[60:63],a[248:251],a[184:187],v[60:63]
EX v27,v27
M v[60:63],a[252:255],a[188:191],v[60:63]
BL v209,s[44:47],s63 offen offset:3072 sc1 lds
M a[0:3],v[96:99],v[64:67],a[0:3]
s_xor_b32 s65,s65,0x8000
EX v28,v28
M a[0:3],v[100:103],v[68:71],a[0:3]
RD a[192:195],v210
RD a[196:199],v210 offset:4096
M a[4:7],v[104:107],v[64:67],a[4:7]
SA s63,s63,0x8000
EX v29,v29
M a[4:7],v[108:111],v[68:71],a[4:7]
M a[8:11],v[112:115],v[64:67],a[8:11]
s_cmp_eq_u32 s63,s67
EX v30,v30
M a[8:11],v[116:119],v[68:71],a[8:11]
RD a[200:203],v210 offset:8192
RD a[204:207],v210 offset:12288
M a[12:15],v[120:123],v[64:67],a[12:15]
s_cselect_b32 s63,s69,s63
EX v31,v31
M a[12:15],v[124:127],v[68:71],a[12:15]
M a[16:19],v[128:131],v[64:67],a[16:19]
s_cselect_b32 s67,s68,s67
EX v32,v32
M a[16:19],v[132:135],v[68:71],a[16:19]
RD a[208:211],v210 offset:2048
RD a[212:215],v210 offset:6144
M a[20:23],v[136:139],v[64:67],a[20:23]
s_cselect_b32 s69,s70,s69
EX v33,v33
M a[20:23],v[140:143],v[68:71],a[20:23]
M a[24:27],v[144:147],v[64:67],a[24:27]
EX v34,v34
M a[24:27],v[148:151],v[68:71],a[24:27]
RD a[216:219],v210 offset:10240
RD a[220:223],v210 offset:14336
M a[28:31],v[152:155],v[64:67],a[28:31]
EX v35,v35
M a[28:31],v[156:159],v[68:71],a[28:31]
M v[188:191],v[204:207],v[64:67],v[188:191]
EX v36,v36
M v[188:191],v[204:207],v[68:71],v[188:191]
RD a[224:227],v210 offset:16384
RD a[228:231],v210 offset:20480
M a[32:35],v[96:99],v[72:75],a[32:35]
EX v37,v37
M a[32:35],v[100:103],v[76:79],a[32:35]
CV v64,v0,v1
CV v65,v2,v3
M a[36:39],v[104:107],v[72:75],a[36:39]
EX v38,v38
M a[36:39],v[108:111],v[76:79],a[36:39]
RD a[232:235],v210 offset:24576
RD a[236:239],v210 offset:28672
M a[40:43],v[112:115],v[72:75],a[40:43]
EX v39,v39
M a[40:43],v[116:119],v[76:79],a[40:43]
CV v66,v4,v5
CV v67,v6,v7
M a[44:47],v[120:123],v[72:75],a[44:47]
EX v40,v40
M a[44:47],v[124:127],v[76:79],a[44:47]
RD a[240:243],v210 offset:18432
RD a[244:247],v210 offset:22528
M a[48:51],v[128:131],v[72:75],a[48:51]
EX v41,v41
M a[48:51],v[132:135],v[76:79],a[48:51]
CV v68,v8,v9
CV v69,v10,v11
M a[52:55],v[136:139],v[72:75],a[52:55]
EX v42,v42
M a[52:55],v[140:143],v[76:79],a[52:55]
RD a[248:251],v210 offset:26624
RD a[252:255],v210 offset:30720
M a[56:59],v[144:147],v[72:75],a[56:59]
EX v43,v43
M a[56:59],v[148:151],v[76:79],a[56:59]
v_xor_b32 v210,0x8000,v210
M a[60:63],v[152:155],v[72:75],a[60:63]
EX v44,v44
M a[60:63],v[156:159],v[76:79],a[60:63]
CV v70,v12,v13
CV v71,v14,v15
M v[192:195],v[204:207],v[72:75],v[192:195]
EX v45,v45
M v[192:195],v[204:207],v[76:79],v[192:195]
M a[64:67],v[96:99],v[80:83],a[64:67]
EX v46,v46
M a[64:67],v[100:103],v[84:87],a[64:67]
CV v72,v16,v17
CV v73,v18,v19
M a[68:71],v[104:107],v[80:83],a[68:71]
EX v47,v47
M a[68:71],v[108:111],v[84:87],a[68:71]
CV v74,v20,v21
CV v75,v22,v23
M a[72:75],v[112:115],v[80:83],a[72:75]
EX v48,v48
M a[72:75],v[116:119],v[84:87],a[72:75]
CV v76,v24,v25
CV v77,v26,v27
M a[76:79],v[120:123],v[80:83],a[76:79]
EX v49,v49
M a[76:79],v[124:127],v[84:87],a[76:79]
CV v78,v28,v29
CV v79,v30,v31
M a[80:83],v[128:131],v[80:83],a[80:83]
EX v50,v50
M a[80:83],v[132:135],v[84:87],a[80:83]
M a[84:87],v[136:139],v[80:83],a[84:87]
EX v51,v51
M a[84:87],v[140:143],v[84:87],a[84:87]
M a[88:91],v[144:147],v[80:83],a[88:91]
EX v52,v52
M a[88:91],v[148:151],v[84:87],a[88:91]
M a[92:95],v[152:155],v[80:83],a[92:95]
EX v53,v53
M a[92:95],v[156:159],v[84:87],a[92:95]
M v[196:199],v[204:207],v[80:83],v[196:199]
EX v54,v54
M v[196:199],v[204:207],v[84:87],v[196:199]
M a[96:99],v[96:99],v[88:91],a[96:99]
EX v55,v55
M a[96:99],v[100:103],v[92:95],a[96:99]
CV v80,v32,v33
CV v81,v34,v35
M a[100:103],v[104:107],v[88:91],a[100:103]
EX v56,v56
M a[100:103],v[108:111],v[92:95],a[100:103]
CV v82,v36,v37
CV v83,v38,v39
M a[104:107],v[112:115],v[88:91],a[104:107]
EX v57,v57
M a[104:107],v[116:119],v[92:95],a[104:107]
CV v84,v40,v41
CV v85,v42,v43
M a[108:111],v[120:123],v[88:91],a[108:111]
EX v58,v58
M a[108:111],v[124:127],v[92:95],a[108:111]
CV v86,v44,v45
CV v87,v46,v47
M a[112:115],v[128:131],v[88:91],a[112:115]
EX v59,v59
M a[112:115],v[132:135],v[92:95],a[112:115]
M a[116:119],v[136:139],v[88:91],a[116:119]
EX v60,v60
M a[116:119],v[140:143],v[92:95],a[116:119]
M a[120:123],v[144:147],v[88:91],a[120:123]
EX v61,v61
M a[120:123],v[148:151],v[92:95],a[120:123]
M a[124:127],v[152:155],v[88:91],a[124:127]
EX v62,v62
M a[124:127],v[156:159],v[92:95],a[124:127]
M v[200:203],v[204:207],v[88:91],v[200:203]
EX v63,v63
M v[200:203],v[204:207],v[92:95],v[200:203]
s_sub_u32 s62,s62,1
s_cmp_lg_u32 s62,0
s_cbranch_scc1 fk_G
fk_L:
W vmcnt(0) lgkmcnt(0)
s_barrier
M v[0:3],a[192:195],a[128:131],v[160:163]
v_xor_b32 v211,0x8000,v211
CV v88,v48,v49
CV v89,v50,v51
M v[0:3],a[196:199],a[132:135],v[0:3]
RD v[96:99],v211
RD v[100:103],v211 offset:16384
M v[0:3],a[200:203],a[136:139],v[0:3]
s_mov_b32 m0,s66
CV v90,v52,v53
CV v91,v54,v55
M v[0:3],a[204:207],a[140:143],v[0:3]
BL v208,s[44:47],s64 offen sc1 lds
M v[16:19],a[192:195],a[144:147],0
CV v92,v56,v57
CV v93,v58,v59
M v[16:19],a[196:199],a[148:151],v[16:19]
RD v[104:107],v211 offset:2048
RD v[108:111],v211 offset:18432
M v[16:19],a[200:203],a[152:155],v[16:19]
CV v94,v60,v61
CV v95,v62,v63
M v[16:19],a[204:207],a[156:159],v[16:19]
BL v208,s[44:47],s64 offen offset:1024 sc1 lds
M v[20:23],a[208:211],a[144:147],v[160:163]
EX v0,v0
M v[20:23],a[212:215],a[148:151],v[20:23]
RD v[112:115],v211 offset:4096
RD v[116:119],v211 offset:20480
M v[20:23],a[216:219],a[152:155],v[20:23]
EX v1,v1
M v[20:23],a[220:223],a[156:159],v[20:23]
BL v208,s[44:47],s64 offen offset:2048 sc1 lds
M v[32:35],a[192:195],a[160:163],0
EX v2,v2
M v[32:35],a[196:199],a[164:167],v[32:35]
RD v[120:123],v211 offset:6144
RD v[124:127],v211 offset:22528
M v[32:35],a[200:203],a[168:171],v[32:35]
EX v3,v3
M v[32:35],a[204:207],a[172:175],v[32:35]
BL v208,s[44:47],s64 offen offset:3072 sc1 lds
M v[36:39],a[208:211],a[160:163],0
EX v16,v16
M v[36:39],a[212:215],a[164:167],v[36:39]
RD v[128:131],v211 offset:8192
RD v[132:135],v211 offset:24576
M v[36:39],a[216:219],a[168:171],v[36:39]
SA m0,s66,0x1000
EX v17,v17
M v[36:39],a[220:223],a[172:175],v[36:39]
BL v209,s[44:47],s64 offen sc1 lds
M v[40:43],a[224:227],a[160:163],v[160:163]
EX v18,v18
M v[40:43],a[228:231],a[164:167],v[40:43]
RD v[136:139],v211 offset:10240
RD v[140:143],v211 offset:26624
M v[40:43],a[232:235],a[168:171],v[40:43]
EX v19,v19
M v[40:43],a[236:239],a[172:175],v[40:43]
BL v209,s[44:47],s64 offen offset:1024 sc1 lds
M v[48:51],a[192:195],a[176:179],0
EX v20,v20
M v[48:51],a[196:199],a[180:183],v[48:51]
RD v[144:147],v211 offset:12288
RD v[148:151],v211 offset:28672
M v[48:51],a[200:203],a[184:187],v[48:51]
EX v21,v21
M v[48:51],a[204:207],a[188:191],v[48:51]
BL v209,s[44:47],s64 offen offset:2048 sc1 lds
M v[52:55],a[208:211],a[176:179],0
EX v22,v22
M v[52:55],a[212:215],a[180:183],v[52:55]
RD v[152:155],v211 offset:14336
RD v[156:159],v211 offset:30720
M v[52:55],a[216:219],a[184:187],v[52:55]
EX v23,v23
M v[52:55],a[220:223],a[188:191],v[52:55]
BL v209,s[44:47],s64 offen offset:3072 sc1 lds
M v[56:59],a[224:227],a[176:179],0
s_mov_b32 s64,s77
EX v32,v32
M v[56:59],a[228:231],a[180:183],v[56:59]
W lgkmcnt(0)
M v[56:59],a[232:235],a[184:187],v[56:59]
s_mov_b32 m0,s65
EX v33,v33
M v[56:59],a[236:239],a[188:191],v[56:59]
BL v208,s[44:47],s63 offen sc1 lds
M v[60:63],a[240:243],a[176:179],v[160:163]
s_xor_b32 s66,s66,0x8000
EX v34,v34
M v[60:63],a[244:247],a[180:183],v[60:63]
M v[60:63],a[248:251],a[184:187],v[60:63]
EX v35,v35
M v[60:63],a[252:255],a[188:191],v[60:63]
BL v208,s[44:47],s63 offen offset:1024 sc1 lds
M a[0:3],v[96:99],v[64:67],a[0:3]
EX v36,v36
M a[0:3],v[100:103],v[68:71],a[0:3]
RD a[192:195],v210
RD a[196:199],v210 offset:4096
M a[4:7],v[104:107],v[64:67],a[4:7]
EX v37,v37
M a[4:7],v[108:111],v[68:71],a[4:7]
BL v208,s[44:47],s63 offen offset:2048 sc1 lds
M a[8:11],v[112:115],v[64:67],a[8:11]
EX v38,v38
M a[8:11],v[116:119],v[68:71],a[8:11]
RD a[200:203],v210 offset:8192
RD a[204:207],v210 offset:12288
M a[12:15],v[120:123],v[64:67],a[12:15]
EX v39,v39
M a[12:15],v[124:127],v[68:71],a[12:15]
BL v208,s[44:47],s63 offen offset:3072 sc1 lds
M a[16:19],v[128:131],v[64:67],a[16:19]
EX v40,v40
M a[16:19],v[132:135],v[68:71],a[16:19]
RD a[208:211],v210 offset:2048
RD a[212:215],v210 offset:6144
M a[20:23],v[136:139],v[64:67],a[20:23]
SA m0,s65,0x1000
EX v41,v41
M a[20:23],v[140:143],v[68:71],a[20:23]
BL v209,s[44:47],s63 offen sc1 lds
M a[24:27],v[144:147],v[64:67],a[24:27]
EX v42,v42
M a[24:27],v[148:151],v[68:71],a[24:27]
RD a[216:219],v210 offset:10240
RD a[220:223],v210 offset:14336
M a[28:31],v[152:155],v[64:67],a[28:31]
EX v43,v43
M a[28:31],v[156:159],v[68:71],a[28:31]
BL v209,s[44:47],s63 offen offset:1024 sc1 lds
M v[188:191],v[204:207],v[64:67],v[188:191]
EX v48,v48
M v[188:191],v[204:207],v[68:71],v[188:191]
RD a[224:227],v210 offset:16384
RD a[228:231],v210 offset:20480
M a[32:35],v[96:99],v[72:75],a[32:35]
EX v49,v49
M a[32:35],v[100:103],v[76:79],a[32:35]
BL v209,s[44:47],s63 offen offset:2048 sc1 lds
M a[36:39],v[104:107],v[72:75],a[36:39]
EX v50,v50
M a[36:39],v[108:111],v[76:79],a[36:39]
RD a[232:235],v210 offset:24576
RD a[236:239],v210 offset:28672
M a[40:43],v[112:115],v[72:75],a[40:43]
EX v51,v51
M a[40:43],v[116:119],v[76:79],a[40:43]
BL v209,s[44:47],s63 offen offset:3072 sc1 lds
M a[44:47],v[120:123],v[72:75],a[44:47]
s_xor_b32 s65,s65,0x8000
EX v52,v52
M a[44:47],v[124:127],v[76:79],a[44:47]
RD a[240:243],v210 offset:18432
RD a[244:247],v210 offset:22528
M a[48:51],v[128:131],v[72:75],a[48:51]
SA s63,s63,0x8000
EX v53,v53
M a[48:51],v[132:135],v[76:79],a[48:51]
CV v64,v0,v1
CV v65,v2,v3
M a[52:55],v[136:139],v[72:75],a[52:55]
s_cmp_eq_u32 s63,s67
EX v54,v54
M a[52:55],v[140:143],v[76:79],a[52:55]
RD a[248:251],v210 offset:26624
RD a[252:255],v210 offset:30720
M a[56:59],v[144:147],v[72:75],a[56:59]
s_cselect_b32 s63,s69,s63
EX v55,v55
M a[56:59],v[148:151],v[76:79],a[56:59]
v_xor_b32 v210,0x8000,v210
M a[60:63],v[152:155],v[72:75],a[60:63]
s_cselect_b32 s67,s68,s67
EX v56,v56
M a[60:63],v[156:159],v[76:79],a[60:63]
v_mov_b32 v66,0
v_mov_b32 v67,0
M v[192:195],v[204:207],v[72:75],v[192:195]
s_cselect_b32 s69,s70,s69
EX v57,v57
M v[192:195],v[204:207],v[76:79],v[192:195]
RD a[128:131],v212
RD a[132:135],v212 offset:1024
M a[64:67],v[96:99],v[80:83],a[64:67]
EX v58,v58
M a[64:67],v[100:103],v[84:87],a[64:67]
CV v72,v16,v17
CV v73,v18,v19
M a[68:71],v[104:107],v[80:83],a[68:71]
EX v59,v59
M a[68:71],v[108:111],v[84:87],a[68:71]
CV v74,v20,v21
CV v75,v22,v23
M a[72:75],v[112:115],v[80:83],a[72:75]
EX v60,v60
M a[72:75],v[116:119],v[84:87],a[72:75]
RD a[136:139],v212 offset:2048
RD a[140:143],v212 offset:3072
M a[76:79],v[120:123],v[80:83],a[76:79]
EX v61,v61
M a[76:79],v[124:127],v[84:87],a[76:79]
RD a[144:147],v212 offset:8192
RD a[148:151],v212 offset:9216
M a[80:83],v[128:131],v[80:83],a[80:83]
EX v62,v62
M a[80:83],v[132:135],v[84:87],a[80:83]
RD a[152:155],v212 offset:10240
RD a[156:159],v212 offset:11264
M a[84:87],v[136:139],v[80:83],a[84:87]
EX v63,v63
M a[84:87],v[140:143],v[84:87],a[84:87]
RD a[160:163],v212 offset:16384
RD a[164:167],v212 offset:17408
M a[88:91],v[144:147],v[80:83],a[88:91]
M a[88:91],v[148:151],v[84:87],a[88:91]
RD a[168:171],v212 offset:18432
RD a[172:175],v212 offset:19456
M a[92:95],v[152:155],v[80:83],a[92:95]
M a[92:95],v[156:159],v[84:87],a[92:95]
RD a[176:179],v212 offset:24576
RD a[180:183],v212 offset:25600
M v[196:199],v[204:207],v[80:83],v[196:199]
M v[196:199],v[204:207],v[84:87],v[196:199]
RD a[184:187],v212 offset:26624
RD a[188:191],v212 offset:27648
M a[96:99],v[96:99],v[88:91],a[96:99]
M a[96:99],v[100:103],v[92:95],a[96:99]
CV v80,v32,v33
CV v81,v34,v35
M a[100:103],v[104:107],v[88:91],a[100:103]
M a[100:103],v[108:111],v[92:95],a[100:103]
CV v82,v36,v37
CV v83,v38,v39
M a[104:107],v[112:115],v[88:91],a[104:107]
M a[104:107],v[116:119],v[92:95],a[104:107]
CV v84,v40,v41
CV v85,v42,v43
M a[108:111],v[120:123],v[88:91],a[108:111]
M a[108:111],v[124:127],v[92:95],a[108:111]
v_mov_b32 v86,0
v_mov_b32 v87,0
M a[112:115],v[128:131],v[88:91],a[112:115]
M a[112:115],v[132:135],v[92:95],a[112:115]
M a[116:119],v[136:139],v[88:91],a[116:119]
M a[116:119],v[140:143],v[92:95],a[116:119]
M a[120:123],v[144:147],v[88:91],a[120:123]
M a[120:123],v[148:151],v[92:95],a[120:123]
M a[124:127],v[152:155],v[88:91],a[124:127]
M a[124:127],v[156:159],v[92:95],a[124:127]
M v[200:203],v[204:207],v[88:91],v[200:203]
M v[200:203],v[204:207],v[92:95],v[200:203]
s_cmp_eq_u32 s74,1
s_cbranch_scc1 fk_DR
s_mov_b32 s74,1
s_cmp_eq_u32 s61,1
s_cbranch_scc1 fk_Lp
W vmcnt(0) lgkmcnt(0)
s_barrier
M v[0:3],a[192:195],a[128:131],0
v_xor_b32 v211,0x8000,v211
CV v88,v48,v49
CV v89,v50,v51
M v[0:3],a[196:199],a[132:135],v[0:3]
RD v[96:99],v211
RD v[100:103],v211 offset:16384
M v[0:3],a[200:203],a[136:139],v[0:3]
s_mov_b32 m0,s66
CV v90,v52,v53
CV v91,v54,v55
M v[0:3],a[204:207],a[140:143],v[0:3]
BL v208,s[44:47],s64 offen sc1 lds
M v[4:7],a[208:211],a[128:131],0
CV v92,v56,v57
CV v93,v58,v59
M v[4:7],a[212:215],a[132:135],v[4:7]
RD v[104:107],v211 offset:2048
RD v[108:111],v211 offset:18432
M v[4:7],a[216:219],a[136:139],v[4:7]
)~"
R"~(CV v94,v60,v61
CV v95,v62,v63
M v[4:7],a[220:223],a[140:143],v[4:7]
BL v208,s[44:47],s64 offen offset:1024 sc1 lds
M v[8:11],a[224:227],a[128:131],0
EX v0,v0
M v[8:11],a[228:231],a[132:135],v[8:11]
RD v[112:115],v211 offset:4096
RD v[116:119],v211 offset:20480
M v[8:11],a[232:235],a[136:139],v[8:11]
EX v1,v1
M v[8:11],a[236:239],a[140:143],v[8:11]
BL v208,s[44:47],s64 offen offset:2048 sc1 lds
M v[12:15],a[240:243],a[128:131],0
EX v2,v2
M v[12:15],a[244:247],a[132:135],v[12:15]
RD v[120:123],v211 offset:6144
RD v[124:127],v211 offset:22528
M v[12:15],a[248:251],a[136:139],v[12:15]
EX v3,v3
M v[12:15],a[252:255],a[140:143],v[12:15]
BL v208,s[44:47],s64 offen offset:3072 sc1 lds
M v[16:19],a[192:195],a[144:147],0
EX v4,v4
M v[16:19],a[196:199],a[148:151],v[16:19]
RD v[128:131],v211 offset:8192
RD v[132:135],v211 offset:24576
M v[16:19],a[200:203],a[152:155],v[16:19]
SA m0,s66,0x1000
EX v5,v5
M v[16:19],a[204:207],a[156:159],v[16:19]
BL v209,s[44:47],s64 offen sc1 lds
M v[20:23],a[208:211],a[144:147],0
EX v6,v6
M v[20:23],a[212:215],a[148:151],v[20:23]
RD v[136:139],v211 offset:10240
RD v[140:143],v211 offset:26624
M v[20:23],a[216:219],a[152:155],v[20:23]
EX v7,v7
M v[20:23],a[220:223],a[156:159],v[20:23]
BL v209,s[44:47],s64 offen offset:1024 sc1 lds
M v[24:27],a[224:227],a[144:147],0
EX v8,v8
M v[24:27],a[228:231],a[148:151],v[24:27]
RD v[144:147],v211 offset:12288
RD v[148:151],v211 offset:28672
M v[24:27],a[232:235],a[152:155],v[24:27]
EX v9,v9
M v[24:27],a[236:239],a[156:159],v[24:27]
BL v209,s[44:47],s64 offen offset:2048 sc1 lds
M v[28:31],a[240:243],a[144:147],0
EX v10,v10
M v[28:31],a[244:247],a[148:151],v[28:31]
RD v[152:155],v211 offset:14336
RD v[156:159],v211 offset:30720
M v[28:31],a[248:251],a[152:155],v[28:31]
EX v11,v11
M v[28:31],a[252:255],a[156:159],v[28:31]
BL v209,s[44:47],s64 offen offset:3072 sc1 lds
M v[32:35],a[192:195],a[160:163],0
SA s64,s64,0x8000
EX v12,v12
M v[32:35],a[196:199],a[164:167],v[32:35]
W lgkmcnt(0)
EX v13,v13
M v[32:35],a[200:203],a[168:171],v[32:35]
s_mov_b32 m0,s65
EX v14,v14
M v[32:35],a[204:207],a[172:175],v[32:35]
BL v208,s[44:47],s63 offen sc1 lds
M v[36:39],a[208:211],a[160:163],0
s_xor_b32 s66,s66,0x8000
EX v15,v15
M v[36:39],a[212:215],a[164:167],v[36:39]
EX v16,v16
M v[36:39],a[216:219],a[168:171],v[36:39]
EX v17,v17
M v[36:39],a[220:223],a[172:175],v[36:39]
BL v208,s[44:47],s63 offen offset:1024 sc1 lds
M v[40:43],a[224:227],a[160:163],0
EX v18,v18
M v[40:43],a[228:231],a[164:167],v[40:43]
EX v19,v19
M v[40:43],a[232:235],a[168:171],v[40:43]
EX v20,v20
M v[40:43],a[236:239],a[172:175],v[40:43]
BL v208,s[44:47],s63 offen offset:2048 sc1 lds
M v[44:47],a[240:243],a[160:163],0
EX v21,v21
M v[44:47],a[244:247],a[164:167],v[44:47]
EX v22,v22
M v[44:47],a[248:251],a[168:171],v[44:47]
EX v23,v23
M v[44:47],a[252:255],a[172:175],v[44:47]
BL v208,s[44:47],s63 offen offset:3072 sc1 lds
M v[48:51],a[192:195],a[176:179],0
EX v24,v24
M v[48:51],a[196:199],a[180:183],v[48:51]
EX v25,v25
M v[48:51],a[200:203],a[184:187],v[48:51]
SA m0,s65,0x1000
EX v26,v26
M v[48:51],a[204:207],a[188:191],v[48:51]
BL v209,s[44:47],s63 offen sc1 lds
M v[52:55],a[208:211],a[176:179],0
EX v27,v27
M v[52:55],a[212:215],a[180:183],v[52:55]
EX v28,v28
M v[52:55],a[216:219],a[184:187],v[52:55]
EX v29,v29
M v[52:55],a[220:223],a[188:191],v[52:55]
BL v209,s[44:47],s63 offen offset:1024 sc1 lds
M v[56:59],a[224:227],a[176:179],0
EX v30,v30
M v[56:59],a[228:231],a[180:183],v[56:59]
EX v31,v31
M v[56:59],a[232:235],a[184:187],v[56:59]
EX v32,v32
M v[56:59],a[236:239],a[188:191],v[56:59]
BL v209,s[44:47],s63 offen offset:2048 sc1 lds
M v[60:63],a[240:243],a[176:179],0
EX v33,v33
M v[60:63],a[244:247],a[180:183],v[60:63]
EX v34,v34
M v[60:63],a[248:251],a[184:187],v[60:63]
EX v35,v35
M v[60:63],a[252:255],a[188:191],v[60:63]
BL v209,s[44:47],s63 offen offset:3072 sc1 lds
M a[0:3],v[96:99],v[64:67],a[0:3]
s_xor_b32 s65,s65,0x8000
EX v36,v36
M a[4:7],v[104:107],v[64:67],a[4:7]
RD a[192:195],v210
RD a[196:199],v210 offset:4096
M a[8:11],v[112:115],v[64:67],a[8:11]
SA s63,s63,0x8000
EX v37,v37
M a[12:15],v[120:123],v[64:67],a[12:15]
M a[16:19],v[128:131],v[64:67],a[16:19]
s_cmp_eq_u32 s63,s67
EX v38,v38
M a[20:23],v[136:139],v[64:67],a[20:23]
RD a[200:203],v210 offset:8192
RD a[204:207],v210 offset:12288
M a[24:27],v[144:147],v[64:67],a[24:27]
s_cselect_b32 s63,s69,s63
EX v39,v39
M a[28:31],v[152:155],v[64:67],a[28:31]
M v[188:191],v[204:207],v[64:67],v[188:191]
s_cselect_b32 s67,s68,s67
EX v40,v40
M a[32:35],v[96:99],v[72:75],a[32:35]
RD a[208:211],v210 offset:2048
RD a[212:215],v210 offset:6144
M a[36:39],v[104:107],v[72:75],a[36:39]
s_cselect_b32 s69,s70,s69
EX v41,v41
M a[40:43],v[112:115],v[72:75],a[40:43]
CV v64,v0,v1
CV v65,v2,v3
M a[44:47],v[120:123],v[72:75],a[44:47]
EX v42,v42
M a[48:51],v[128:131],v[72:75],a[48:51]
RD a[216:219],v210 offset:10240
RD a[220:223],v210 offset:14336
M a[52:55],v[136:139],v[72:75],a[52:55]
EX v43,v43
M a[56:59],v[144:147],v[72:75],a[56:59]
CV v66,v4,v5
CV v67,v6,v7
M a[60:63],v[152:155],v[72:75],a[60:63]
EX v44,v44
M v[192:195],v[204:207],v[72:75],v[192:195]
RD a[224:227],v210 offset:16384
RD a[228:231],v210 offset:20480
M a[64:67],v[96:99],v[80:83],a[64:67]
EX v45,v45
M a[64:67],v[100:103],v[84:87],a[64:67]
CV v68,v8,v9
CV v69,v10,v11
M a[68:71],v[104:107],v[80:83],a[68:71]
EX v46,v46
M a[68:71],v[108:111],v[84:87],a[68:71]
RD a[232:235],v210 offset:24576
RD a[236:239],v210 offset:28672
M a[72:75],v[112:115],v[80:83],a[72:75]
EX v47,v47
M a[72:75],v[116:119],v[84:87],a[72:75]
CV v70,v12,v13
CV v71,v14,v15
M a[76:79],v[120:123],v[80:83],a[76:79]
EX v48,v48
M a[76:79],v[124:127],v[84:87],a[76:79]
RD a[240:243],v210 offset:18432
RD a[244:247],v210 offset:22528
M a[80:83],v[128:131],v[80:83],a[80:83]
EX v49,v49
M a[80:83],v[132:135],v[84:87],a[80:83]
CV v72,v16,v17
CV v73,v18,v19
M a[84:87],v[136:139],v[80:83],a[84:87]
EX v50,v50
M a[84:87],v[140:143],v[84:87],a[84:87]
RD a[248:251],v210 offset:26624
RD a[252:255],v210 offset:30720
M a[88:91],v[144:147],v[80:83],a[88:91]
EX v51,v51
M a[88:91],v[148:151],v[84:87],a[88:91]
v_xor_b32 v210,0x8000,v210
M a[92:95],v[152:155],v[80:83],a[92:95]
EX v52,v52
M a[92:95],v[156:159],v[84:87],a[92:95]
CV v74,v20,v21
CV v75,v22,v23
M v[196:199],v[204:207],v[80:83],v[196:199]
EX v53,v53
M v[196:199],v[204:207],v[84:87],v[196:199]
CV v76,v24,v25
CV v77,v26,v27
M a[96:99],v[96:99],v[88:91],a[96:99]
EX v54,v54
M a[96:99],v[100:103],v[92:95],a[96:99]
CV v78,v28,v29
CV v79,v30,v31
M a[100:103],v[104:107],v[88:91],a[100:103]
EX v55,v55
M a[100:103],v[108:111],v[92:95],a[100:103]
CV v80,v32,v33
CV v81,v34,v35
M a[104:107],v[112:115],v[88:91],a[104:107]
EX v56,v56
M a[104:107],v[116:119],v[92:95],a[104:107]
CV v82,v36,v37
CV v83,v38,v39
M a[108:111],v[120:123],v[88:91],a[108:111]
EX v57,v57
M a[108:111],v[124:127],v[92:95],a[108:111]
CV v84,v40,v41
CV v85,v42,v43
M a[112:115],v[128:131],v[88:91],a[112:115]
EX v58,v58
M a[112:115],v[132:135],v[92:95],a[112:115]
CV v86,v44,v45
CV v87,v46,v47
M a[116:119],v[136:139],v[88:91],a[116:119]
EX v59,v59
M a[116:119],v[140:143],v[92:95],a[116:119]
W lgkmcnt(0)
s_barrier
M a[120:123],v[144:147],v[88:91],a[120:123]
EX v60,v60
M a[120:123],v[148:151],v[92:95],a[120:123]
M a[124:127],v[152:155],v[88:91],a[124:127]
EX v61,v61
M a[124:127],v[156:159],v[92:95],a[124:127]
M v[200:203],v[204:207],v[88:91],v[200:203]
EX v62,v62
M v[200:203],v[204:207],v[92:95],v[200:203]
EX v63,v63
s_branch fk_EPI
fk_Lp:
W vmcnt(0) lgkmcnt(0)
s_barrier
M v[0:3],a[192:195],a[128:131],v[160:163]
v_xor_b32 v211,0x8000,v211
CV v88,v48,v49
CV v89,v50,v51
M v[0:3],a[196:199],a[132:135],v[0:3]
RD v[96:99],v211
RD v[100:103],v211 offset:16384
M v[0:3],a[200:203],a[136:139],v[0:3]
s_mov_b32 m0,s66
CV v90,v52,v53
CV v91,v54,v55
M v[0:3],a[204:207],a[140:143],v[0:3]
BL v208,s[44:47],s64 offen sc1 lds
M v[16:19],a[192:195],a[144:147],0
CV v92,v56,v57
CV v93,v58,v59
M v[16:19],a[196:199],a[148:151],v[16:19]
RD v[104:107],v211 offset:2048
RD v[108:111],v211 offset:18432
M v[16:19],a[200:203],a[152:155],v[16:19]
CV v94,v60,v61
CV v95,v62,v63
M v[16:19],a[204:207],a[156:159],v[16:19]
BL v208,s[44:47],s64 offen offset:1024 sc1 lds
M v[20:23],a[208:211],a[144:147],v[160:163]
EX v0,v0
M v[20:23],a[212:215],a[148:151],v[20:23]
RD v[112:115],v211 offset:4096
RD v[116:119],v211 offset:20480
M v[20:23],a[216:219],a[152:155],v[20:23]
EX v1,v1
M v[20:23],a[220:223],a[156:159],v[20:23]
BL v208,s[44:47],s64 offen offset:2048 sc1 lds
M v[32:35],a[192:195],a[160:163],0
EX v2,v2
M v[32:35],a[196:199],a[164:167],v[32:35]
RD v[120:123],v211 offset:6144
RD v[124:127],v211 offset:22528
M v[32:35],a[200:203],a[168:171],v[32:35]
EX v3,v3
M v[32:35],a[204:207],a[172:175],v[32:35]
BL v208,s[44:47],s64 offen offset:3072 sc1 lds
M v[36:39],a[208:211],a[160:163],0
EX v16,v16
M v[36:39],a[212:215],a[164:167],v[36:39]
RD v[128:131],v211 offset:8192
RD v[132:135],v211 offset:24576
M v[36:39],a[216:219],a[168:171],v[36:39]
SA m0,s66,0x1000
EX v17,v17
M v[36:39],a[220:223],a[172:175],v[36:39]
BL v209,s[44:47],s64 offen sc1 lds
M v[40:43],a[224:227],a[160:163],v[160:163]
EX v18,v18
M v[40:43],a[228:231],a[164:167],v[40:43]
RD v[136:139],v211 offset:10240
RD v[140:143],v211 offset:26624
M v[40:43],a[232:235],a[168:171],v[40:43]
EX v19,v19
M v[40:43],a[236:239],a[172:175],v[40:43]
BL v209,s[44:47],s64 offen offset:1024 sc1 lds
M v[48:51],a[192:195],a[176:179],0
EX v20,v20
M v[48:51],a[196:199],a[180:183],v[48:51]
RD v[144:147],v211 offset:12288
RD v[148:151],v211 offset:28672
M v[48:51],a[200:203],a[184:187],v[48:51]
EX v21,v21
M v[48:51],a[204:207],a[188:191],v[48:51]
BL v209,s[44:47],s64 offen offset:2048 sc1 lds
M v[52:55],a[208:211],a[176:179],0
EX v22,v22
M v[52:55],a[212:215],a[180:183],v[52:55]
RD v[152:155],v211 offset:14336
RD v[156:159],v211 offset:30720
M v[52:55],a[216:219],a[184:187],v[52:55]
EX v23,v23
M v[52:55],a[220:223],a[188:191],v[52:55]
BL v209,s[44:47],s64 offen offset:3072 sc1 lds
M v[56:59],a[224:227],a[176:179],0
s_mov_b32 s64,s77
EX v32,v32
M v[56:59],a[228:231],a[180:183],v[56:59]
W lgkmcnt(0)
M v[56:59],a[232:235],a[184:187],v[56:59]
s_mov_b32 m0,s65
EX v33,v33
M v[56:59],a[236:239],a[188:191],v[56:59]
BL v208,s[44:47],s63 offen sc1 lds
M v[60:63],a[240:243],a[176:179],v[160:163]
s_xor_b32 s66,s66,0x8000
EX v34,v34
M v[60:63],a[244:247],a[180:183],v[60:63]
M v[60:63],a[248:251],a[184:187],v[60:63]
EX v35,v35
M v[60:63],a[252:255],a[188:191],v[60:63]
BL v208,s[44:47],s63 offen offset:1024 sc1 lds
M a[0:3],v[96:99],v[64:67],a[0:3]
EX v36,v36
M a[4:7],v[104:107],v[64:67],a[4:7]
RD a[192:195],v210
RD a[196:199],v210 offset:4096
M a[8:11],v[112:115],v[64:67],a[8:11]
EX v37,v37
M a[12:15],v[120:123],v[64:67],a[12:15]
BL v208,s[44:47],s63 offen offset:2048 sc1 lds
M a[16:19],v[128:131],v[64:67],a[16:19]
EX v38,v38
M a[20:23],v[136:139],v[64:67],a[20:23]
RD a[200:203],v210 offset:8192
RD a[204:207],v210 offset:12288
M a[24:27],v[144:147],v[64:67],a[24:27]
EX v39,v39
M a[28:31],v[152:155],v[64:67],a[28:31]
BL v208,s[44:47],s63 offen offset:3072 sc1 lds
M v[188:191],v[204:207],v[64:67],v[188:191]
EX v40,v40
M a[32:35],v[96:99],v[72:75],a[32:35]
RD a[208:211],v210 offset:2048
RD a[212:215],v210 offset:6144
M a[36:39],v[104:107],v[72:75],a[36:39]
SA m0,s65,0x1000
EX v41,v41
M a[40:43],v[112:115],v[72:75],a[40:43]
BL v209,s[44:47],s63 offen sc1 lds
M a[44:47],v[120:123],v[72:75],a[44:47]
EX v42,v42
M a[48:51],v[128:131],v[72:75],a[48:51]
RD a[216:219],v210 offset:10240
RD a[220:223],v210 offset:14336
M a[52:55],v[136:139],v[72:75],a[52:55]
EX v43,v43
M a[56:59],v[144:147],v[72:75],a[56:59]
BL v209,s[44:47],s63 offen offset:1024 sc1 lds
M a[60:63],v[152:155],v[72:75],a[60:63]
EX v48,v48
M v[192:195],v[204:207],v[72:75],v[192:195]
RD a[224:227],v210 offset:16384
RD a[228:231],v210 offset:20480
M a[64:67],v[96:99],v[80:83],a[64:67]
EX v49,v49
M a[64:67],v[100:103],v[84:87],a[64:67]
BL v209,s[44:47],s63 offen offset:2048 sc1 lds
M a[68:71],v[104:107],v[80:83],a[68:71]
EX v50,v50
M a[68:71],v[108:111],v[84:87],a[68:71]
RD a[232:235],v210 offset:24576
RD a[236:239],v210 offset:28672
M a[72:75],v[112:115],v[80:83],a[72:75]
EX v51,v51
M a[72:75],v[116:119],v[84:87],a[72:75]
BL v209,s[44:47],s63 offen offset:3072 sc1 lds
M a[76:79],v[120:123],v[80:83],a[76:79]
s_xor_b32 s65,s65,0x8000
EX v52,v52
M a[76:79],v[124:127],v[84:87],a[76:79]
RD a[240:243],v210 offset:18432
RD a[244:247],v210 offset:22528
M a[80:83],v[128:131],v[80:83],a[80:83]
SA s63,s63,0x8000
EX v53,v53
M a[80:83],v[132:135],v[84:87],a[80:83]
CV v64,v0,v1
CV v65,v2,v3
M a[84:87],v[136:139],v[80:83],a[84:87]
s_cmp_eq_u32 s63,s67
EX v54,v54
M a[84:87],v[140:143],v[84:87],a[84:87]
RD a[248:251],v210 offset:26624
RD a[252:255],v210 offset:30720
M a[88:91],v[144:147],v[80:83],a[88:91]
s_cselect_b32 s63,s69,s63
EX v55,v55
M a[88:91],v[148:151],v[84:87],a[88:91]
v_xor_b32 v210,0x8000,v210
M a[92:95],v[152:155],v[80:83],a[92:95]
s_cselect_b32 s67,s68,s67
EX v56,v56
M a[92:95],v[156:159],v[84:87],a[92:95]
v_mov_b32 v66,0
v_mov_b32 v67,0
M v[196:199],v[204:207],v[80:83],v[196:199]
s_cselect_b32 s69,s70,s69
EX v57,v57
M v[196:199],v[204:207],v[84:87],v[196:199]
CV v72,v16,v17
CV v73,v18,v19
M a[96:99],v[96:99],v[88:91],a[96:99]
EX v58,v58
M a[96:99],v[100:103],v[92:95],a[96:99]
CV v74,v20,v21
CV v75,v22,v23
M a[100:103],v[104:107],v[88:91],a[100:103]
EX v59,v59
M a[100:103],v[108:111],v[92:95],a[100:103]
CV v80,v32,v33
CV v81,v34,v35
M a[104:107],v[112:115],v[88:91],a[104:107]
EX v60,v60
M a[104:107],v[116:119],v[92:95],a[104:107]
CV v82,v36,v37
CV v83,v38,v39
M a[108:111],v[120:123],v[88:91],a[108:111]
EX v61,v61
M a[108:111],v[124:127],v[92:95],a[108:111]
CV v84,v40,v41
CV v85,v42,v43
M a[112:115],v[128:131],v[88:91],a[112:115]
EX v62,v62
M a[112:115],v[132:135],v[92:95],a[112:115]
v_mov_b32 v86,0
v_mov_b32 v87,0
M a[116:119],v[136:139],v[88:91],a[116:119]
EX v63,v63
M a[116:119],v[140:143],v[92:95],a[116:119]
W lgkmcnt(0)
s_barrier
M a[120:123],v[144:147],v[88:91],a[120:123]
M a[120:123],v[148:151],v[92:95],a[120:123]
M a[124:127],v[152:155],v[88:91],a[124:127]
)~"
R"~(M a[124:127],v[156:159],v[92:95],a[124:127]
M v[200:203],v[204:207],v[88:91],v[200:203]
M v[200:203],v[204:207],v[92:95],v[200:203]
fk_EPI:
s_cmp_eq_u32 s41,0
s_cbranch_scc0 fk_epi1
s_and_b32 s84,s66,0x8000
SA s84,s84,0x10000
s_xor_b32 s85,s36,0
s_and_b32 s86,s85,1
s_lshl_b32 s86,s86,14
s_cmp_lt_u32 s85,2
s_cselect_b32 s87,s84,0x20000
SA s87,s87,s86
v_add_u32 v224,s87,v254
v_mov_b32 v228,v188
v_mov_b32 v229,v192
v_mov_b32 v230,v196
v_mov_b32 v231,v200
WR v224,a[64:67]
s_and_b32 s88,s65,0x8000
s_lshl_b32 s89,s36,10
SA s89,s89,s88
v_add_u32 v226,s89,v254
s_lshl_b32 s89,s40,11
SA s90,s89,s88
v_add_u32 v227,s90,v254
v_xor_b32 v217,0x800,v227
WR v226,v[228:231]
s_xor_b32 s85,s36,1
s_and_b32 s86,s85,1
s_lshl_b32 s86,s86,14
s_cmp_lt_u32 s85,2
s_cselect_b32 s87,s84,0x20000
SA s87,s87,s86
v_add_u32 v225,s87,v254
s_mov_b32 s92,0x1c800000
s_mov_b32 s93,0x6c800000
AR v96,a0
AR v97,a1
AR v98,a2
AR v99,a3
WR v224,a[68:71] offset:1024
AR v100,a4
AR v101,a5
AR v102,a6
AR v103,a7
WR v224,a[72:75] offset:2048
AR v104,a8
AR v105,a9
AR v106,a10
AR v107,a11
WR v224,a[76:79] offset:3072
AR v108,a12
AR v109,a13
AR v110,a14
AR v111,a15
WR v224,a[80:83] offset:4096
AR v112,a16
AR v113,a17
AR v114,a18
AR v115,a19
WR v224,a[84:87] offset:5120
AR v116,a20
AR v117,a21
AR v118,a22
AR v119,a23
WR v224,a[88:91] offset:6144
AR v120,a24
AR v121,a25
AR v122,a26
AR v123,a27
WR v224,a[92:95] offset:7168
AR v124,a28
AR v125,a29
AR v126,a30
AR v127,a31
WR v224,a[96:99] offset:8192
AR v128,a32
AR v129,a33
AR v130,a34
AR v131,a35
WR v224,a[100:103] offset:9216
AR v132,a36
AR v133,a37
AR v134,a38
AR v135,a39
WR v224,a[104:107] offset:10240
AR v136,a40
AR v137,a41
AR v138,a42
AR v139,a43
WR v224,a[108:111] offset:11264
AR v140,a44
AR v141,a45
AR v142,a46
AR v143,a47
WR v224,a[112:115] offset:12288
AR v144,a48
AR v145,a49
AR v146,a50
AR v147,a51
WR v224,a[116:119] offset:13312
AR v148,a52
AR v149,a53
AR v150,a54
AR v151,a55
WR v224,a[120:123] offset:14336
AR v152,a56
AR v153,a57
AR v154,a58
AR v155,a59
WR v224,a[124:127] offset:15360
AR v156,a60
AR v157,a61
AR v158,a62
AR v159,a63
W vmcnt(0) lgkmcnt(0)
s_barrier
RD v[228:231],v227
RD v[232:235],v227 offset:1024
RD v[236:239],v217
RD v[240:243],v217 offset:1024
RD v[164:167],v225
RD v[168:171],v225 offset:1024
RD v[172:175],v225 offset:2048
RD v[176:179],v225 offset:3072
RD v[180:183],v225 offset:4096
RD v[184:187],v225 offset:5120
W lgkmcnt(6)
v_pk_add_f32 v[228:229],v[228:229],v[232:233]
v_pk_add_f32 v[230:231],v[230:231],v[234:235]
v_pk_add_f32 v[236:237],v[236:237],v[240:241]
v_pk_add_f32 v[238:239],v[238:239],v[242:243]
v_rcp_f32 v244,v228
v_rcp_f32 v246,v229
v_min3_f32 v240,v228,v229,v230
v_max3_f32 v241,v228,v229,v230
v_min3_f32 v240,v240,v231,v236
v_max3_f32 v241,v241,v231,v236
v_min3_f32 v240,v240,v237,v238
v_max3_f32 v241,v241,v237,v238
v_min_f32 v240,v240,v239
v_max_f32 v241,v241,v239
RD v[228:231],v225 offset:6144
RD v[232:235],v225 offset:7168
v_mov_b32 v245,v244
v_mov_b32 v247,v246
v_cmp_gt_f32 s[94:95],s92,v240
v_cmp_lt_f32 vcc,s93,v241
W lgkmcnt(7)
v_pk_add_f32 v[96:97],v[96:97],v[164:165]
v_pk_add_f32 v[98:99],v[98:99],v[166:167]
W lgkmcnt(6)
v_pk_add_f32 v[100:101],v[100:101],v[168:169]
v_pk_add_f32 v[102:103],v[102:103],v[170:171]
RD v[164:167],v225 offset:8192
RD v[168:171],v225 offset:9216
s_or_b64 s[94:95],s[94:95],vcc
s_cselect_b32 s94,1,0
s_or_b32 s56,s56,s94
v_pk_mul_f32 v[96:97],v[96:97],v[244:245]
v_pk_mul_f32 v[98:99],v[98:99],v[244:245]
v_pk_mul_f32 v[100:101],v[100:101],v[244:245]
v_pk_mul_f32 v[102:103],v[102:103],v[244:245]
CV v96,v96,v97
CV v97,v98,v99
CV v98,v100,v101
CV v99,v102,v103
BS v[96:99],v254,s[44:47],s71 offen sc1
W lgkmcnt(7)
v_pk_add_f32 v[104:105],v[104:105],v[172:173]
v_pk_add_f32 v[106:107],v[106:107],v[174:175]
W lgkmcnt(6)
v_pk_add_f32 v[108:109],v[108:109],v[176:177]
v_pk_add_f32 v[110:111],v[110:111],v[178:179]
RD v[172:175],v225 offset:10240
RD v[176:179],v225 offset:11264
v_pk_mul_f32 v[104:105],v[104:105],v[244:245]
v_pk_mul_f32 v[106:107],v[106:107],v[244:245]
v_pk_mul_f32 v[108:109],v[108:109],v[244:245]
v_pk_mul_f32 v[110:111],v[110:111],v[244:245]
CV v104,v104,v105
CV v105,v106,v107
CV v106,v108,v109
CV v107,v110,v111
BS v[104:107],v254,s[44:47],s71 offen offset:1024 sc1
W lgkmcnt(7)
v_pk_add_f32 v[112:113],v[112:113],v[180:181]
v_pk_add_f32 v[114:115],v[114:115],v[182:183]
W lgkmcnt(6)
v_pk_add_f32 v[116:117],v[116:117],v[184:185]
v_pk_add_f32 v[118:119],v[118:119],v[186:187]
RD v[180:183],v225 offset:12288
RD v[184:187],v225 offset:13312
v_pk_mul_f32 v[112:113],v[112:113],v[244:245]
v_pk_mul_f32 v[114:115],v[114:115],v[244:245]
v_pk_mul_f32 v[116:117],v[116:117],v[244:245]
v_pk_mul_f32 v[118:119],v[118:119],v[244:245]
CV v112,v112,v113
CV v113,v114,v115
CV v114,v116,v117
CV v115,v118,v119
BS v[112:115],v254,s[44:47],s71 offen offset:2048 sc1
W lgkmcnt(7)
v_pk_add_f32 v[120:121],v[120:121],v[228:229]
v_pk_add_f32 v[122:123],v[122:123],v[230:231]
W lgkmcnt(6)
v_pk_add_f32 v[124:125],v[124:125],v[232:233]
v_pk_add_f32 v[126:127],v[126:127],v[234:235]
RD v[228:231],v225 offset:14336
RD v[232:235],v225 offset:15360
v_pk_mul_f32 v[120:121],v[120:121],v[244:245]
v_pk_mul_f32 v[122:123],v[122:123],v[244:245]
v_pk_mul_f32 v[124:125],v[124:125],v[244:245]
v_pk_mul_f32 v[126:127],v[126:127],v[244:245]
CV v120,v120,v121
CV v121,v122,v123
CV v122,v124,v125
CV v123,v126,v127
BS v[120:123],v254,s[44:47],s71 offen offset:3072 sc1
W lgkmcnt(7)
v_pk_add_f32 v[128:129],v[128:129],v[164:165]
v_pk_add_f32 v[130:131],v[130:131],v[166:167]
W lgkmcnt(6)
v_pk_add_f32 v[132:133],v[132:133],v[168:169]
v_pk_add_f32 v[134:135],v[134:135],v[170:171]
v_pk_mul_f32 v[128:129],v[128:129],v[246:247]
v_pk_mul_f32 v[130:131],v[130:131],v[246:247]
v_pk_mul_f32 v[132:133],v[132:133],v[246:247]
v_pk_mul_f32 v[134:135],v[134:135],v[246:247]
CV v128,v128,v129
CV v129,v130,v131
CV v130,v132,v133
CV v131,v134,v135
SA s71,s71,0x48000
BS v[128:131],v254,s[44:47],s71 offen sc1
W lgkmcnt(5)
v_pk_add_f32 v[136:137],v[136:137],v[172:173]
v_pk_add_f32 v[138:139],v[138:139],v[174:175]
W lgkmcnt(4)
v_pk_add_f32 v[140:141],v[140:141],v[176:177]
v_pk_add_f32 v[142:143],v[142:143],v[178:179]
v_pk_mul_f32 v[136:137],v[136:137],v[246:247]
v_pk_mul_f32 v[138:139],v[138:139],v[246:247]
v_pk_mul_f32 v[140:141],v[140:141],v[246:247]
v_pk_mul_f32 v[142:143],v[142:143],v[246:247]
CV v136,v136,v137
CV v137,v138,v139
CV v138,v140,v141
CV v139,v142,v143
BS v[136:139],v254,s[44:47],s71 offen offset:1024 sc1
W lgkmcnt(3)
v_pk_add_f32 v[144:145],v[144:145],v[180:181]
v_pk_add_f32 v[146:147],v[146:147],v[182:183]
W lgkmcnt(2)
v_pk_add_f32 v[148:149],v[148:149],v[184:185]
v_pk_add_f32 v[150:151],v[150:151],v[186:187]
v_pk_mul_f32 v[144:145],v[144:145],v[246:247]
v_pk_mul_f32 v[146:147],v[146:147],v[246:247]
v_pk_mul_f32 v[148:149],v[148:149],v[246:247]
v_pk_mul_f32 v[150:151],v[150:151],v[246:247]
CV v144,v144,v145
CV v145,v146,v147
CV v146,v148,v149
CV v147,v150,v151
BS v[144:147],v254,s[44:47],s71 offen offset:2048 sc1
W lgkmcnt(1)
v_pk_add_f32 v[152:153],v[152:153],v[228:229]
v_pk_add_f32 v[154:155],v[154:155],v[230:231]
W lgkmcnt(0)
v_pk_add_f32 v[156:157],v[156:157],v[232:233]
v_pk_add_f32 v[158:159],v[158:159],v[234:235]
v_pk_mul_f32 v[152:153],v[152:153],v[246:247]
v_pk_mul_f32 v[154:155],v[154:155],v[246:247]
v_pk_mul_f32 v[156:157],v[156:157],v[246:247]
v_pk_mul_f32 v[158:159],v[158:159],v[246:247]
CV v152,v152,v153
CV v153,v154,v155
CV v154,v156,v157
CV v155,v158,v159
BS v[152:155],v254,s[44:47],s71 offen offset:3072 sc1
s_mov_b32 s71,s72
s_branch fk_epi_done
fk_epi1:
s_and_b32 s84,s66,0x8000
SA s84,s84,0x10000
s_xor_b32 s85,s36,0
s_and_b32 s86,s85,1
s_lshl_b32 s86,s86,14
s_cmp_lt_u32 s85,2
s_cselect_b32 s87,s84,0x20000
SA s87,s87,s86
v_add_u32 v224,s87,v254
v_mov_b32 v228,v188
v_mov_b32 v229,v192
v_mov_b32 v230,v196
v_mov_b32 v231,v200
WR v224,a[0:3]
s_and_b32 s88,s65,0x8000
s_lshl_b32 s89,s36,10
SA s89,s89,s88
v_add_u32 v226,s89,v254
s_lshl_b32 s89,s40,11
SA s90,s89,s88
v_add_u32 v227,s90,v254
v_xor_b32 v217,0x800,v227
WR v226,v[228:231]
s_xor_b32 s85,s36,1
s_and_b32 s86,s85,1
s_lshl_b32 s86,s86,14
s_cmp_lt_u32 s85,2
s_cselect_b32 s87,s84,0x20000
SA s87,s87,s86
v_add_u32 v225,s87,v254
s_mov_b32 s92,0x1c800000
s_mov_b32 s93,0x6c800000
AR v96,a64
AR v97,a65
AR v98,a66
AR v99,a67
WR v224,a[4:7] offset:1024
AR v100,a68
AR v101,a69
AR v102,a70
AR v103,a71
WR v224,a[8:11] offset:2048
AR v104,a72
AR v105,a73
AR v106,a74
AR v107,a75
WR v224,a[12:15] offset:3072
AR v108,a76
AR v109,a77
AR v110,a78
AR v111,a79
WR v224,a[16:19] offset:4096
AR v112,a80
AR v113,a81
AR v114,a82
AR v115,a83
WR v224,a[20:23] offset:5120
AR v116,a84
AR v117,a85
AR v118,a86
AR v119,a87
WR v224,a[24:27] offset:6144
AR v120,a88
AR v121,a89
AR v122,a90
AR v123,a91
WR v224,a[28:31] offset:7168
AR v124,a92
AR v125,a93
AR v126,a94
AR v127,a95
WR v224,a[32:35] offset:8192
AR v128,a96
AR v129,a97
AR v130,a98
AR v131,a99
WR v224,a[36:39] offset:9216
AR v132,a100
AR v133,a101
AR v134,a102
AR v135,a103
WR v224,a[40:43] offset:10240
AR v136,a104
AR v137,a105
AR v138,a106
AR v139,a107
WR v224,a[44:47] offset:11264
AR v140,a108
AR v141,a109
AR v142,a110
AR v143,a111
WR v224,a[48:51] offset:12288
AR v144,a112
AR v145,a113
AR v146,a114
AR v147,a115
WR v224,a[52:55] offset:13312
AR v148,a116
AR v149,a117
AR v150,a118
AR v151,a119
WR v224,a[56:59] offset:14336
AR v152,a120
AR v153,a121
AR v154,a122
AR v155,a123
WR v224,a[60:63] offset:15360
AR v156,a124
AR v157,a125
AR v158,a126
AR v159,a127
W vmcnt(0) lgkmcnt(0)
s_barrier
RD v[228:231],v227
RD v[232:235],v227 offset:1024
RD v[236:239],v217
RD v[240:243],v217 offset:1024
RD v[164:167],v225
RD v[168:171],v225 offset:1024
RD v[172:175],v225 offset:2048
RD v[176:179],v225 offset:3072
RD v[180:183],v225 offset:4096
RD v[184:187],v225 offset:5120
W lgkmcnt(6)
v_pk_add_f32 v[228:229],v[228:229],v[232:233]
v_pk_add_f32 v[230:231],v[230:231],v[234:235]
v_pk_add_f32 v[236:237],v[236:237],v[240:241]
v_pk_add_f32 v[238:239],v[238:239],v[242:243]
v_rcp_f32 v244,v230
v_rcp_f32 v246,v231
v_min3_f32 v240,v228,v229,v230
v_max3_f32 v241,v228,v229,v230
v_min3_f32 v240,v240,v231,v236
v_max3_f32 v241,v241,v231,v236
v_min3_f32 v240,v240,v237,v238
v_max3_f32 v241,v241,v237,v238
v_min_f32 v240,v240,v239
v_max_f32 v241,v241,v239
RD v[228:231],v225 offset:6144
RD v[232:235],v225 offset:7168
v_mov_b32 v245,v244
v_mov_b32 v247,v246
v_cmp_gt_f32 s[94:95],s92,v240
v_cmp_lt_f32 vcc,s93,v241
W lgkmcnt(7)
v_pk_add_f32 v[96:97],v[96:97],v[164:165]
v_pk_add_f32 v[98:99],v[98:99],v[166:167]
W lgkmcnt(6)
v_pk_add_f32 v[100:101],v[100:101],v[168:169]
v_pk_add_f32 v[102:103],v[102:103],v[170:171]
RD v[164:167],v225 offset:8192
RD v[168:171],v225 offset:9216
s_or_b64 s[94:95],s[94:95],vcc
s_cselect_b32 s94,1,0
s_or_b32 s56,s56,s94
v_pk_mul_f32 v[96:97],v[96:97],v[244:245]
v_pk_mul_f32 v[98:99],v[98:99],v[244:245]
v_pk_mul_f32 v[100:101],v[100:101],v[244:245]
v_pk_mul_f32 v[102:103],v[102:103],v[244:245]
CV v96,v96,v97
CV v97,v98,v99
CV v98,v100,v101
CV v99,v102,v103
BS v[96:99],v254,s[44:47],s71 offen sc1
W lgkmcnt(7)
v_pk_add_f32 v[104:105],v[104:105],v[172:173]
v_pk_add_f32 v[106:107],v[106:107],v[174:175]
W lgkmcnt(6)
v_pk_add_f32 v[108:109],v[108:109],v[176:177]
v_pk_add_f32 v[110:111],v[110:111],v[178:179]
RD v[172:175],v225 offset:10240
RD v[176:179],v225 offset:11264
v_pk_mul_f32 v[104:105],v[104:105],v[244:245]
v_pk_mul_f32 v[106:107],v[106:107],v[244:245]
v_pk_mul_f32 v[108:109],v[108:109],v[244:245]
v_pk_mul_f32 v[110:111],v[110:111],v[244:245]
CV v104,v104,v105
CV v105,v106,v107
CV v106,v108,v109
CV v107,v110,v111
BS v[104:107],v254,s[44:47],s71 offen offset:1024 sc1
W lgkmcnt(7)
v_pk_add_f32 v[112:113],v[112:113],v[180:181]
v_pk_add_f32 v[114:115],v[114:115],v[182:183]
W lgkmcnt(6)
v_pk_add_f32 v[116:117],v[116:117],v[184:185]
v_pk_add_f32 v[118:119],v[118:119],v[186:187]
RD v[180:183],v225 offset:12288
RD v[184:187],v225 offset:13312
v_pk_mul_f32 v[112:113],v[112:113],v[244:245]
v_pk_mul_f32 v[114:115],v[114:115],v[244:245]
v_pk_mul_f32 v[116:117],v[116:117],v[244:245]
v_pk_mul_f32 v[118:119],v[118:119],v[244:245]
CV v112,v112,v113
CV v113,v114,v115
CV v114,v116,v117
CV v115,v118,v119
BS v[112:115],v254,s[44:47],s71 offen offset:2048 sc1
W lgkmcnt(7)
v_pk_add_f32 v[120:121],v[120:121],v[228:229]
v_pk_add_f32 v[122:123],v[122:123],v[230:231]
W lgkmcnt(6)
v_pk_add_f32 v[124:125],v[124:125],v[232:233]
v_pk_add_f32 v[126:127],v[126:127],v[234:235]
RD v[228:231],v225 offset:14336
RD v[232:235],v225 offset:15360
v_pk_mul_f32 v[120:121],v[120:121],v[244:245]
v_pk_mul_f32 v[122:123],v[122:123],v[244:245]
v_pk_mul_f32 v[124:125],v[124:125],v[244:245]
v_pk_mul_f32 v[126:127],v[126:127],v[244:245]
CV v120,v120,v121
CV v121,v122,v123
CV v122,v124,v125
CV v123,v126,v127
BS v[120:123],v254,s[44:47],s71 offen offset:3072 sc1
W lgkmcnt(7)
v_pk_add_f32 v[128:129],v[128:129],v[164:165]
v_pk_add_f32 v[130:131],v[130:131],v[166:167]
W lgkmcnt(6)
v_pk_add_f32 v[132:133],v[132:133],v[168:169]
v_pk_add_f32 v[134:135],v[134:135],v[170:171]
v_pk_mul_f32 v[128:129],v[128:129],v[246:247]
v_pk_mul_f32 v[130:131],v[130:131],v[246:247]
v_pk_mul_f32 v[132:133],v[132:133],v[246:247]
v_pk_mul_f32 v[134:135],v[134:135],v[246:247]
CV v128,v128,v129
CV v129,v130,v131
CV v130,v132,v133
CV v131,v134,v135
SA s71,s71,0x48000
BS v[128:131],v254,s[44:47],s71 offen sc1
W lgkmcnt(5)
v_pk_add_f32 v[136:137],v[136:137],v[172:173]
v_pk_add_f32 v[138:139],v[138:139],v[174:175]
W lgkmcnt(4)
v_pk_add_f32 v[140:141],v[140:141],v[176:177]
v_pk_add_f32 v[142:143],v[142:143],v[178:179]
v_pk_mul_f32 v[136:137],v[136:137],v[246:247]
v_pk_mul_f32 v[138:139],v[138:139],v[246:247]
v_pk_mul_f32 v[140:141],v[140:141],v[246:247]
v_pk_mul_f32 v[142:143],v[142:143],v[246:247]
CV v136,v136,v137
CV v137,v138,v139
CV v138,v140,v141
CV v139,v142,v143
BS v[136:139],v254,s[44:47],s71 offen offset:1024 sc1
W lgkmcnt(3)
v_pk_add_f32 v[144:145],v[144:145],v[180:181]
v_pk_add_f32 v[146:147],v[146:147],v[182:183]
W lgkmcnt(2)
v_pk_add_f32 v[148:149],v[148:149],v[184:185]
v_pk_add_f32 v[150:151],v[150:151],v[186:187]
v_pk_mul_f32 v[144:145],v[144:145],v[246:247]
)~"
R"~(v_pk_mul_f32 v[146:147],v[146:147],v[246:247]
v_pk_mul_f32 v[148:149],v[148:149],v[246:247]
v_pk_mul_f32 v[150:151],v[150:151],v[246:247]
CV v144,v144,v145
CV v145,v146,v147
CV v146,v148,v149
CV v147,v150,v151
BS v[144:147],v254,s[44:47],s71 offen offset:2048 sc1
W lgkmcnt(1)
v_pk_add_f32 v[152:153],v[152:153],v[228:229]
v_pk_add_f32 v[154:155],v[154:155],v[230:231]
W lgkmcnt(0)
v_pk_add_f32 v[156:157],v[156:157],v[232:233]
v_pk_add_f32 v[158:159],v[158:159],v[234:235]
v_pk_mul_f32 v[152:153],v[152:153],v[246:247]
v_pk_mul_f32 v[154:155],v[154:155],v[246:247]
v_pk_mul_f32 v[156:157],v[156:157],v[246:247]
v_pk_mul_f32 v[158:159],v[158:159],v[246:247]
CV v152,v152,v153
CV v153,v154,v155
CV v154,v156,v157
CV v155,v158,v159
BS v[152:155],v254,s[44:47],s71 offen offset:3072 sc1
s_mov_b32 s71,s72
fk_epi_done:
s_cmp_eq_u32 s76,1
s_cbranch_scc1 fk_attn_end
s_cmp_eq_u32 s76,2
s_cbranch_scc1 fk_safe2
s_cmp_eq_u32 s76,3
s_cbranch_scc1 fk_attn_done
s_mov_b32 s76,1
s_cmp_eq_u32 s61,1
s_cbranch_scc1 fk_DRf
s_cmp_eq_u32 s61,2
s_cbranch_scc1 fk_Lf
s_sub_u32 s62,s61,3
s_branch fk_F2
fk_Lf:
W lgkmcnt(0)
s_barrier
M v[0:3],a[192:195],a[128:131],v[160:163]
v_xor_b32 v211,0x8000,v211
CV v88,v48,v49
CV v89,v50,v51
M v[0:3],a[196:199],a[132:135],v[0:3]
RD v[96:99],v211
RD v[100:103],v211 offset:16384
M v[0:3],a[200:203],a[136:139],v[0:3]
s_mov_b32 m0,s66
CV v90,v52,v53
CV v91,v54,v55
M v[0:3],a[204:207],a[140:143],v[0:3]
BL v208,s[44:47],s64 offen sc1 lds
M v[16:19],a[192:195],a[144:147],0
CV v92,v56,v57
CV v93,v58,v59
M v[16:19],a[196:199],a[148:151],v[16:19]
RD v[104:107],v211 offset:2048
RD v[108:111],v211 offset:18432
M v[16:19],a[200:203],a[152:155],v[16:19]
CV v94,v60,v61
CV v95,v62,v63
M v[16:19],a[204:207],a[156:159],v[16:19]
BL v208,s[44:47],s64 offen offset:1024 sc1 lds
M v[20:23],a[208:211],a[144:147],v[160:163]
EX v0,v0
M v[20:23],a[212:215],a[148:151],v[20:23]
RD v[112:115],v211 offset:4096
RD v[116:119],v211 offset:20480
M v[20:23],a[216:219],a[152:155],v[20:23]
EX v1,v1
M v[20:23],a[220:223],a[156:159],v[20:23]
BL v208,s[44:47],s64 offen offset:2048 sc1 lds
M v[32:35],a[192:195],a[160:163],0
EX v2,v2
M v[32:35],a[196:199],a[164:167],v[32:35]
RD v[120:123],v211 offset:6144
RD v[124:127],v211 offset:22528
M v[32:35],a[200:203],a[168:171],v[32:35]
EX v3,v3
M v[32:35],a[204:207],a[172:175],v[32:35]
BL v208,s[44:47],s64 offen offset:3072 sc1 lds
M v[36:39],a[208:211],a[160:163],0
EX v16,v16
M v[36:39],a[212:215],a[164:167],v[36:39]
RD v[128:131],v211 offset:8192
RD v[132:135],v211 offset:24576
M v[36:39],a[216:219],a[168:171],v[36:39]
SA m0,s66,0x1000
EX v17,v17
M v[36:39],a[220:223],a[172:175],v[36:39]
BL v209,s[44:47],s64 offen sc1 lds
M v[40:43],a[224:227],a[160:163],v[160:163]
EX v18,v18
M v[40:43],a[228:231],a[164:167],v[40:43]
RD v[136:139],v211 offset:10240
RD v[140:143],v211 offset:26624
M v[40:43],a[232:235],a[168:171],v[40:43]
EX v19,v19
M v[40:43],a[236:239],a[172:175],v[40:43]
BL v209,s[44:47],s64 offen offset:1024 sc1 lds
M v[48:51],a[192:195],a[176:179],0
EX v20,v20
M v[48:51],a[196:199],a[180:183],v[48:51]
RD v[144:147],v211 offset:12288
RD v[148:151],v211 offset:28672
M v[48:51],a[200:203],a[184:187],v[48:51]
EX v21,v21
M v[48:51],a[204:207],a[188:191],v[48:51]
BL v209,s[44:47],s64 offen offset:2048 sc1 lds
M v[52:55],a[208:211],a[176:179],0
EX v22,v22
M v[52:55],a[212:215],a[180:183],v[52:55]
RD v[152:155],v211 offset:14336
RD v[156:159],v211 offset:30720
M v[52:55],a[216:219],a[184:187],v[52:55]
EX v23,v23
M v[52:55],a[220:223],a[188:191],v[52:55]
BL v209,s[44:47],s64 offen offset:3072 sc1 lds
M v[56:59],a[224:227],a[176:179],0
s_mov_b32 s64,s77
EX v32,v32
M v[56:59],a[228:231],a[180:183],v[56:59]
W lgkmcnt(0)
M v[56:59],a[232:235],a[184:187],v[56:59]
s_mov_b32 m0,s65
EX v33,v33
M v[56:59],a[236:239],a[188:191],v[56:59]
BL v208,s[44:47],s63 offen sc1 lds
M v[60:63],a[240:243],a[176:179],v[160:163]
s_xor_b32 s66,s66,0x8000
EX v34,v34
M v[60:63],a[244:247],a[180:183],v[60:63]
M v[60:63],a[248:251],a[184:187],v[60:63]
EX v35,v35
M v[60:63],a[252:255],a[188:191],v[60:63]
BL v208,s[44:47],s63 offen offset:1024 sc1 lds
M a[0:3],v[96:99],v[64:67],0
EX v36,v36
M a[0:3],v[100:103],v[68:71],a[0:3]
RD a[192:195],v210
RD a[196:199],v210 offset:4096
M a[4:7],v[104:107],v[64:67],0
EX v37,v37
M a[4:7],v[108:111],v[68:71],a[4:7]
BL v208,s[44:47],s63 offen offset:2048 sc1 lds
M a[8:11],v[112:115],v[64:67],0
EX v38,v38
M a[8:11],v[116:119],v[68:71],a[8:11]
RD a[200:203],v210 offset:8192
RD a[204:207],v210 offset:12288
M a[12:15],v[120:123],v[64:67],0
EX v39,v39
M a[12:15],v[124:127],v[68:71],a[12:15]
BL v208,s[44:47],s63 offen offset:3072 sc1 lds
M a[16:19],v[128:131],v[64:67],0
EX v40,v40
M a[16:19],v[132:135],v[68:71],a[16:19]
RD a[208:211],v210 offset:2048
RD a[212:215],v210 offset:6144
M a[20:23],v[136:139],v[64:67],0
SA m0,s65,0x1000
EX v41,v41
M a[20:23],v[140:143],v[68:71],a[20:23]
BL v209,s[44:47],s63 offen sc1 lds
M a[24:27],v[144:147],v[64:67],0
EX v42,v42
M a[24:27],v[148:151],v[68:71],a[24:27]
RD a[216:219],v210 offset:10240
RD a[220:223],v210 offset:14336
M a[28:31],v[152:155],v[64:67],0
EX v43,v43
M a[28:31],v[156:159],v[68:71],a[28:31]
BL v209,s[44:47],s63 offen offset:1024 sc1 lds
M v[188:191],v[204:207],v[64:67],0
EX v48,v48
M v[188:191],v[204:207],v[68:71],v[188:191]
RD a[224:227],v210 offset:16384
RD a[228:231],v210 offset:20480
M a[32:35],v[96:99],v[72:75],0
EX v49,v49
M a[32:35],v[100:103],v[76:79],a[32:35]
BL v209,s[44:47],s63 offen offset:2048 sc1 lds
M a[36:39],v[104:107],v[72:75],0
EX v50,v50
M a[36:39],v[108:111],v[76:79],a[36:39]
RD a[232:235],v210 offset:24576
RD a[236:239],v210 offset:28672
M a[40:43],v[112:115],v[72:75],0
EX v51,v51
M a[40:43],v[116:119],v[76:79],a[40:43]
BL v209,s[44:47],s63 offen offset:3072 sc1 lds
M a[44:47],v[120:123],v[72:75],0
s_xor_b32 s65,s65,0x8000
EX v52,v52
M a[44:47],v[124:127],v[76:79],a[44:47]
RD a[240:243],v210 offset:18432
RD a[244:247],v210 offset:22528
M a[48:51],v[128:131],v[72:75],0
SA s63,s63,0x8000
EX v53,v53
M a[48:51],v[132:135],v[76:79],a[48:51]
CV v64,v0,v1
CV v65,v2,v3
M a[52:55],v[136:139],v[72:75],0
s_cmp_eq_u32 s63,s67
EX v54,v54
M a[52:55],v[140:143],v[76:79],a[52:55]
RD a[248:251],v210 offset:26624
RD a[252:255],v210 offset:30720
M a[56:59],v[144:147],v[72:75],0
s_cselect_b32 s63,s69,s63
EX v55,v55
M a[56:59],v[148:151],v[76:79],a[56:59]
v_xor_b32 v210,0x8000,v210
M a[60:63],v[152:155],v[72:75],0
s_cselect_b32 s67,s68,s67
EX v56,v56
M a[60:63],v[156:159],v[76:79],a[60:63]
v_mov_b32 v66,0
v_mov_b32 v67,0
M v[192:195],v[204:207],v[72:75],0
s_cselect_b32 s69,s70,s69
EX v57,v57
M v[192:195],v[204:207],v[76:79],v[192:195]
M a[64:67],v[96:99],v[80:83],0
EX v58,v58
M a[64:67],v[100:103],v[84:87],a[64:67]
CV v72,v16,v17
CV v73,v18,v19
M a[68:71],v[104:107],v[80:83],0
EX v59,v59
M a[68:71],v[108:111],v[84:87],a[68:71]
CV v74,v20,v21
CV v75,v22,v23
M a[72:75],v[112:115],v[80:83],0
EX v60,v60
M a[72:75],v[116:119],v[84:87],a[72:75]
M a[76:79],v[120:123],v[80:83],0
EX v61,v61
M a[76:79],v[124:127],v[84:87],a[76:79]
M a[80:83],v[128:131],v[80:83],0
EX v62,v62
M a[80:83],v[132:135],v[84:87],a[80:83]
M a[84:87],v[136:139],v[80:83],0
EX v63,v63
M a[84:87],v[140:143],v[84:87],a[84:87]
M a[88:91],v[144:147],v[80:83],0
M a[88:91],v[148:151],v[84:87],a[88:91]
M a[92:95],v[152:155],v[80:83],0
M a[92:95],v[156:159],v[84:87],a[92:95]
M v[196:199],v[204:207],v[80:83],0
M v[196:199],v[204:207],v[84:87],v[196:199]
M a[96:99],v[96:99],v[88:91],0
M a[96:99],v[100:103],v[92:95],a[96:99]
CV v80,v32,v33
CV v81,v34,v35
M a[100:103],v[104:107],v[88:91],0
M a[100:103],v[108:111],v[92:95],a[100:103]
CV v82,v36,v37
CV v83,v38,v39
M a[104:107],v[112:115],v[88:91],0
M a[104:107],v[116:119],v[92:95],a[104:107]
CV v84,v40,v41
CV v85,v42,v43
M a[108:111],v[120:123],v[88:91],0
M a[108:111],v[124:127],v[92:95],a[108:111]
v_mov_b32 v86,0
v_mov_b32 v87,0
M a[112:115],v[128:131],v[88:91],0
M a[112:115],v[132:135],v[92:95],a[112:115]
M a[116:119],v[136:139],v[88:91],0
M a[116:119],v[140:143],v[92:95],a[116:119]
M a[120:123],v[144:147],v[88:91],0
M a[120:123],v[148:151],v[92:95],a[120:123]
M a[124:127],v[152:155],v[88:91],0
M a[124:127],v[156:159],v[92:95],a[124:127]
M v[200:203],v[204:207],v[88:91],0
M v[200:203],v[204:207],v[92:95],v[200:203]
fk_DR:
W vmcnt(0) lgkmcnt(0)
s_barrier
v_xor_b32 v211,0x8000,v211
RD v[96:99],v211
RD v[100:103],v211 offset:16384
RD v[104:107],v211 offset:2048
RD v[108:111],v211 offset:18432
RD v[112:115],v211 offset:4096
RD v[116:119],v211 offset:20480
RD v[120:123],v211 offset:6144
RD v[124:127],v211 offset:22528
RD v[128:131],v211 offset:8192
RD v[132:135],v211 offset:24576
RD v[136:139],v211 offset:10240
RD v[140:143],v211 offset:26624
RD v[144:147],v211 offset:12288
RD v[148:151],v211 offset:28672
RD v[152:155],v211 offset:14336
RD v[156:159],v211 offset:30720
CV v88,v48,v49
CV v89,v50,v51
CV v90,v52,v53
CV v91,v54,v55
W lgkmcnt(0)
CV v92,v56,v57
CV v93,v58,v59
M a[0:3],v[96:99],v[64:67],a[0:3]
CV v94,v60,v61
CV v95,v62,v63
M a[4:7],v[104:107],v[64:67],a[4:7]
M a[8:11],v[112:115],v[64:67],a[8:11]
M a[12:15],v[120:123],v[64:67],a[12:15]
M a[16:19],v[128:131],v[64:67],a[16:19]
M a[20:23],v[136:139],v[64:67],a[20:23]
M a[24:27],v[144:147],v[64:67],a[24:27]
M a[28:31],v[152:155],v[64:67],a[28:31]
M v[188:191],v[204:207],v[64:67],v[188:191]
s_and_b32 s84,s37,3
s_mul_i32 s84,s84,0x480
s_lshr_b32 s85,s37,3
s_lshl_b32 s85,s85,2
SA s85,s85,s36
s_mul_i32 s85,s85,9
SA s84,s84,s85
s_mov_b64 s[48:49],s[22:23]
s_and_b32 s49,s49,0xffff
s_mov_b32 s50,0x400000
s_mov_b32 s51,0x20000
s_lshl_b32 s85,s84,10
v_lshlrev_b32 v0,7,v255
M a[32:35],v[96:99],v[72:75],a[32:35]
M a[36:39],v[104:107],v[72:75],a[36:39]
buffer_load_dword v1,v0,s[48:51],s85 offen
M a[40:43],v[112:115],v[72:75],a[40:43]
M a[44:47],v[120:123],v[72:75],a[44:47]
M a[48:51],v[128:131],v[72:75],a[48:51]
M a[52:55],v[136:139],v[72:75],a[52:55]
M a[56:59],v[144:147],v[72:75],a[56:59]
M a[60:63],v[152:155],v[72:75],a[60:63]
SA s86,s85,0x2000
v_and_b32 v2,7,v255
v_lshlrev_b32 v2,7,v2
M v[192:195],v[204:207],v[72:75],v[192:195]
M a[64:67],v[96:99],v[80:83],a[64:67]
buffer_load_dword v3,v2,s[48:51],s86 offen
M a[64:67],v[100:103],v[84:87],a[64:67]
M a[68:71],v[104:107],v[80:83],a[68:71]
M a[68:71],v[108:111],v[84:87],a[68:71]
M a[72:75],v[112:115],v[80:83],a[72:75]
M a[72:75],v[116:119],v[84:87],a[72:75]
M a[76:79],v[120:123],v[80:83],a[76:79]
s_mov_b64 s[48:49],s[12:13]
s_and_b32 s49,s49,0xffff
s_mov_b32 s50,0x80000
s_mov_b32 s51,0x20000
s_max_u32 s86,s84,0x1000
s_sub_u32 s86,s86,0x1000
s_lshl_b32 s86,s86,10
s_cmp_ge_u32 s84,0xff8
s_cselect_b32 s86,s86,0x40000000
M a[76:79],v[124:127],v[84:87],a[76:79]
M a[80:83],v[128:131],v[80:83],a[80:83]
buffer_load_dword v4,v0,s[48:51],s86 offen
M a[80:83],v[132:135],v[84:87],a[80:83]
M a[84:87],v[136:139],v[80:83],a[84:87]
M a[84:87],v[140:143],v[84:87],a[84:87]
M a[88:91],v[144:147],v[80:83],a[88:91]
M a[88:91],v[148:151],v[84:87],a[88:91]
M a[92:95],v[152:155],v[80:83],a[92:95]
M a[92:95],v[156:159],v[84:87],a[92:95]
M v[196:199],v[204:207],v[80:83],v[196:199]
M v[196:199],v[204:207],v[84:87],v[196:199]
M a[96:99],v[96:99],v[88:91],a[96:99]
M a[96:99],v[100:103],v[92:95],a[96:99]
M a[100:103],v[104:107],v[88:91],a[100:103]
M a[100:103],v[108:111],v[92:95],a[100:103]
M a[104:107],v[112:115],v[88:91],a[104:107]
M a[104:107],v[116:119],v[92:95],a[104:107]
M a[108:111],v[120:123],v[88:91],a[108:111]
M a[108:111],v[124:127],v[92:95],a[108:111]
M a[112:115],v[128:131],v[88:91],a[112:115]
M a[112:115],v[132:135],v[92:95],a[112:115]
M a[116:119],v[136:139],v[88:91],a[116:119]
M a[116:119],v[140:143],v[92:95],a[116:119]
W lgkmcnt(0)
s_barrier
M a[120:123],v[144:147],v[88:91],a[120:123]
M a[120:123],v[148:151],v[92:95],a[120:123]
M a[124:127],v[152:155],v[88:91],a[124:127]
M a[124:127],v[156:159],v[92:95],a[124:127]
M v[200:203],v[204:207],v[88:91],v[200:203]
M v[200:203],v[204:207],v[92:95],v[200:203]
s_branch fk_EPI
fk_DRf:
W vmcnt(0) lgkmcnt(0)
s_barrier
v_xor_b32 v211,0x8000,v211
RD v[96:99],v211
RD v[100:103],v211 offset:16384
RD v[104:107],v211 offset:2048
RD v[108:111],v211 offset:18432
RD v[112:115],v211 offset:4096
RD v[116:119],v211 offset:20480
RD v[120:123],v211 offset:6144
RD v[124:127],v211 offset:22528
RD v[128:131],v211 offset:8192
RD v[132:135],v211 offset:24576
RD v[136:139],v211 offset:10240
RD v[140:143],v211 offset:26624
RD v[144:147],v211 offset:12288
RD v[148:151],v211 offset:28672
RD v[152:155],v211 offset:14336
RD v[156:159],v211 offset:30720
CV v88,v48,v49
CV v89,v50,v51
CV v90,v52,v53
CV v91,v54,v55
W lgkmcnt(0)
CV v92,v56,v57
CV v93,v58,v59
M a[0:3],v[96:99],v[64:67],0
CV v94,v60,v61
CV v95,v62,v63
M a[4:7],v[104:107],v[64:67],0
M a[8:11],v[112:115],v[64:67],0
M a[12:15],v[120:123],v[64:67],0
M a[16:19],v[128:131],v[64:67],0
M a[20:23],v[136:139],v[64:67],0
M a[24:27],v[144:147],v[64:67],0
M a[28:31],v[152:155],v[64:67],0
M v[188:191],v[204:207],v[64:67],0
s_and_b32 s84,s37,3
s_mul_i32 s84,s84,0x480
s_lshr_b32 s85,s37,3
s_lshl_b32 s85,s85,2
SA s85,s85,s36
s_mul_i32 s85,s85,9
SA s84,s84,s85
s_mov_b64 s[48:49],s[22:23]
s_and_b32 s49,s49,0xffff
s_mov_b32 s50,0x400000
s_mov_b32 s51,0x20000
s_lshl_b32 s85,s84,10
v_lshlrev_b32 v0,7,v255
M a[32:35],v[96:99],v[72:75],0
M a[36:39],v[104:107],v[72:75],0
buffer_load_dword v1,v0,s[48:51],s85 offen
M a[40:43],v[112:115],v[72:75],0
M a[44:47],v[120:123],v[72:75],0
M a[48:51],v[128:131],v[72:75],0
M a[52:55],v[136:139],v[72:75],0
M a[56:59],v[144:147],v[72:75],0
M a[60:63],v[152:155],v[72:75],0
SA s86,s85,0x2000
v_and_b32 v2,7,v255
v_lshlrev_b32 v2,7,v2
M v[192:195],v[204:207],v[72:75],0
M a[64:67],v[96:99],v[80:83],0
buffer_load_dword v3,v2,s[48:51],s86 offen
M a[64:67],v[100:103],v[84:87],a[64:67]
M a[68:71],v[104:107],v[80:83],0
M a[68:71],v[108:111],v[84:87],a[68:71]
M a[72:75],v[112:115],v[80:83],0
M a[72:75],v[116:119],v[84:87],a[72:75]
M a[76:79],v[120:123],v[80:83],0
s_mov_b64 s[48:49],s[12:13]
s_and_b32 s49,s49,0xffff
s_mov_b32 s50,0x80000
s_mov_b32 s51,0x20000
s_max_u32 s86,s84,0x1000
s_sub_u32 s86,s86,0x1000
s_lshl_b32 s86,s86,10
s_cmp_ge_u32 s84,0xff8
)~"
R"~(s_cselect_b32 s86,s86,0x40000000
M a[76:79],v[124:127],v[84:87],a[76:79]
M a[80:83],v[128:131],v[80:83],0
buffer_load_dword v4,v0,s[48:51],s86 offen
M a[80:83],v[132:135],v[84:87],a[80:83]
M a[84:87],v[136:139],v[80:83],0
M a[84:87],v[140:143],v[84:87],a[84:87]
M a[88:91],v[144:147],v[80:83],0
M a[88:91],v[148:151],v[84:87],a[88:91]
M a[92:95],v[152:155],v[80:83],0
M a[92:95],v[156:159],v[84:87],a[92:95]
M v[196:199],v[204:207],v[80:83],0
M v[196:199],v[204:207],v[84:87],v[196:199]
M a[96:99],v[96:99],v[88:91],0
M a[96:99],v[100:103],v[92:95],a[96:99]
M a[100:103],v[104:107],v[88:91],0
M a[100:103],v[108:111],v[92:95],a[100:103]
M a[104:107],v[112:115],v[88:91],0
M a[104:107],v[116:119],v[92:95],a[104:107]
M a[108:111],v[120:123],v[88:91],0
M a[108:111],v[124:127],v[92:95],a[108:111]
M a[112:115],v[128:131],v[88:91],0
M a[112:115],v[132:135],v[92:95],a[112:115]
M a[116:119],v[136:139],v[88:91],0
M a[116:119],v[140:143],v[92:95],a[116:119]
W lgkmcnt(0)
s_barrier
M a[120:123],v[144:147],v[88:91],0
M a[120:123],v[148:151],v[92:95],a[120:123]
M a[124:127],v[152:155],v[88:91],0
M a[124:127],v[156:159],v[92:95],a[124:127]
M v[200:203],v[204:207],v[88:91],0
M v[200:203],v[204:207],v[92:95],v[200:203]
s_branch fk_EPI
fk_redo:
W vmcnt(0) lgkmcnt(0)
s_barrier
s_mov_b32 s57,1
s_mov_b32 s64,0x40000000
s_and_b32 s67,s37,7
s_lshr_b32 s66,s37,3
s_mov_b64 s[48:49],s[8:9]
s_and_b32 s49,s49,0xffff
s_mov_b32 s50,0x200000
s_mov_b32 s51,0x20000
s_lshl_b32 s84,s41,1
SA s84,s84,s40
s_lshl_b32 s62,s84,13
s_sub_u32 s85,15,s39
s_cmp_eq_u32 s41,0
s_cselect_b32 s85,s85,s39
s_lshl_b32 s85,s85,7
s_lshl_b32 s86,s40,6
SA s85,s85,s86
s_lshl_b32 s60,s85,10
s_lshl_b32 s63,s36,12
SA s63,s63,0x8000
s_lshl_b32 s61,s36,17
s_lshl_b32 s86,s38,8
SA s61,s61,s86
s_mov_b64 s[52:53],s[16:17]
s_and_b32 s53,s53,0xffff
s_mov_b32 s54,0x400000
s_mov_b32 s55,0x20000
v_lshrrev_b32 v140,3,v255
v_and_b32 v141,7,v255
v_lshrrev_b32 v142,1,v140
v_xor_b32 v141,v141,v142
v_mul_u32_u24 v140,0x400,v140
v_xor_b32 v143,0,v141
v_lshl_add_u32 v143,v143,4,v140
v_add_u32 v128,0,v143
v_xor_b32 v143,4,v141
v_lshl_add_u32 v143,v143,4,v140
v_add_u32 v129,0x2000,v143
v_xor_b32 v143,0,v141
v_lshl_add_u32 v143,v143,4,v140
v_add_u32 v130,0x4000,v143
v_xor_b32 v143,4,v141
v_lshl_add_u32 v143,v143,4,v140
v_add_u32 v131,0x6000,v143
v_xor_b32 v143,0,v141
v_lshl_add_u32 v143,v143,4,v140
v_add_u32 v132,0x8000,v143
v_xor_b32 v143,4,v141
v_lshl_add_u32 v143,v143,4,v140
v_add_u32 v133,0xa000,v143
v_xor_b32 v143,0,v141
v_lshl_add_u32 v143,v143,4,v140
v_add_u32 v134,0xc000,v143
v_xor_b32 v143,4,v141
v_lshl_add_u32 v143,v143,4,v140
v_add_u32 v135,0xe000,v143
v_bfe_u32 v140,v255,2,3
v_mul_u32_u24 v140,0x2000,v140
v_lshrrev_b32 v141,5,v255
v_lshl_add_u32 v140,v141,6,v140
v_and_b32 v141,3,v255
v_xor_b32 v142,0,v141
v_lshl_add_u32 v142,v142,4,v140
v_add_u32 v136,0,v142
v_xor_b32 v142,0,v141
v_lshl_add_u32 v142,v142,4,v140
v_add_u32 v137,0x80,v142
v_xor_b32 v142,2,v141
v_lshl_add_u32 v142,v142,4,v140
v_add_u32 v138,0x10000,v142
v_xor_b32 v142,2,v141
v_lshl_add_u32 v142,v142,4,v140
v_add_u32 v139,0x10080,v142
SA s77,s60,0
SA m0,s62,0
s_nop 0
BL v128,s[48:51],s77 offen lds
SA m0,s62,0x400
s_nop 0
BL v129,s[48:51],s77 offen lds
SA m0,s62,0x800
s_nop 0
BL v130,s[48:51],s77 offen lds
SA m0,s62,0xc00
s_nop 0
BL v131,s[48:51],s77 offen lds
SA m0,s62,0x1000
s_nop 0
BL v132,s[48:51],s77 offen lds
SA m0,s62,0x1400
s_nop 0
BL v133,s[48:51],s77 offen lds
SA m0,s62,0x1800
s_nop 0
BL v134,s[48:51],s77 offen lds
SA m0,s62,0x1c00
s_nop 0
BL v135,s[48:51],s77 offen lds
SA s78,s61,0
SA m0,s63,0
s_nop 0
BL v136,s[52:55],s78 offen lds
SA m0,s63,0x400
s_nop 0
BL v137,s[52:55],s78 offen lds
SA m0,s63,0x800
s_nop 0
BL v138,s[52:55],s78 offen lds
SA m0,s63,0xc00
s_nop 0
BL v139,s[52:55],s78 offen lds
SA s77,s60,0x80
SA m0,s62,0xc000
s_nop 0
BL v128,s[48:51],s77 offen lds
SA m0,s62,0xc400
s_nop 0
BL v129,s[48:51],s77 offen lds
SA m0,s62,0xc800
s_nop 0
BL v130,s[48:51],s77 offen lds
SA m0,s62,0xcc00
s_nop 0
BL v131,s[48:51],s77 offen lds
SA m0,s62,0xd000
s_nop 0
BL v132,s[48:51],s77 offen lds
SA m0,s62,0xd400
s_nop 0
BL v133,s[48:51],s77 offen lds
SA m0,s62,0xd800
s_nop 0
BL v134,s[48:51],s77 offen lds
SA m0,s62,0xdc00
s_nop 0
BL v135,s[48:51],s77 offen lds
SA s78,s61,0x80000
SA m0,s63,0xc000
s_nop 0
BL v136,s[52:55],s78 offen lds
SA m0,s63,0xc400
s_nop 0
BL v137,s[52:55],s78 offen lds
SA m0,s63,0xc800
s_nop 0
BL v138,s[52:55],s78 offen lds
SA m0,s63,0xcc00
s_nop 0
BL v139,s[52:55],s78 offen lds
SA s77,s60,0x100
SA m0,s62,0x18000
s_nop 0
BL v128,s[48:51],s77 offen lds
SA m0,s62,0x18400
s_nop 0
BL v129,s[48:51],s77 offen lds
SA m0,s62,0x18800
s_nop 0
BL v130,s[48:51],s77 offen lds
SA m0,s62,0x18c00
s_nop 0
BL v131,s[48:51],s77 offen lds
SA m0,s62,0x19000
s_nop 0
BL v132,s[48:51],s77 offen lds
SA m0,s62,0x19400
s_nop 0
BL v133,s[48:51],s77 offen lds
SA m0,s62,0x19800
s_nop 0
BL v134,s[48:51],s77 offen lds
SA m0,s62,0x19c00
s_nop 0
BL v135,s[48:51],s77 offen lds
SA s78,s61,0x100000
SA m0,s63,0x18000
s_nop 0
BL v136,s[52:55],s78 offen lds
SA m0,s63,0x18400
s_nop 0
BL v137,s[52:55],s78 offen lds
SA m0,s63,0x18800
s_nop 0
BL v138,s[52:55],s78 offen lds
SA m0,s63,0x18c00
s_nop 0
BL v139,s[52:55],s78 offen lds
W vmcnt(0)
v_and_b32 v224,15,v255
v_lshrrev_b32 v225,1,v224
v_lshrrev_b32 v226,4,v255
v_xor_b32 v225,v225,v226
v_lshlrev_b32 v224,7,v224
v_xor_b32 v226,0,v225
v_lshl_add_u32 v140,v226,4,v224
v_xor_b32 v226,4,v225
v_lshl_add_u32 v141,v226,4,v224
v_add_u32 v140,s62,v140
v_add_u32 v141,s62,v141
v_add_u32 v142,0xc000,v140
v_add_u32 v143,0xc000,v141
v_add_u32 v144,0x18000,v140
v_add_u32 v145,0x18000,v141
v_lshrrev_b32 v224,4,v255
v_lshlrev_b32 v224,11,v224
v_bfe_u32 v225,v255,2,2
v_lshl_add_u32 v224,v225,6,v224
v_and_b32 v225,3,v255
v_lshl_add_u32 v224,v225,3,v224
v_bfe_u32 v225,v255,4,1
v_lshl_add_u32 v146,v225,5,v224
v_xor_b32 v225,1,v225
v_lshl_add_u32 v147,v225,5,v224
v_add_u32 v150,0x20000,v146
v_add_u32 v151,0x20000,v147
v_add_u32 v148,0x14000,v146
v_add_u32 v149,0x14000,v147
v_add_u32 v146,0x8000,v146
v_add_u32 v147,0x8000,v147
s_lshl_b32 s84,s36,13
v_add_u32 v208,s84,v254
v_add_u32 v209,0x1000,v208
s_sub_u32 s79,s40,s41
s_lshl_b32 s79,s79,4
s_branch fk_qg
fk_safe1:
AW a0,0
AW a1,0
AW a2,0
AW a3,0
AW a4,0
AW a5,0
AW a6,0
AW a7,0
AW a8,0
AW a9,0
AW a10,0
AW a11,0
AW a12,0
AW a13,0
AW a14,0
AW a15,0
AW a16,0
AW a17,0
AW a18,0
AW a19,0
AW a20,0
AW a21,0
AW a22,0
AW a23,0
AW a24,0
AW a25,0
AW a26,0
AW a27,0
AW a28,0
AW a29,0
AW a30,0
AW a31,0
AW a32,0
AW a33,0
AW a34,0
AW a35,0
AW a36,0
AW a37,0
AW a38,0
AW a39,0
AW a40,0
AW a41,0
AW a42,0
AW a43,0
AW a44,0
AW a45,0
AW a46,0
AW a47,0
AW a48,0
AW a49,0
AW a50,0
AW a51,0
AW a52,0
AW a53,0
AW a54,0
AW a55,0
AW a56,0
AW a57,0
AW a58,0
AW a59,0
AW a60,0
AW a61,0
AW a62,0
AW a63,0
AW a64,0
AW a65,0
AW a66,0
AW a67,0
AW a68,0
AW a69,0
AW a70,0
AW a71,0
AW a72,0
AW a73,0
AW a74,0
AW a75,0
AW a76,0
AW a77,0
AW a78,0
AW a79,0
AW a80,0
AW a81,0
AW a82,0
AW a83,0
AW a84,0
AW a85,0
AW a86,0
AW a87,0
AW a88,0
AW a89,0
AW a90,0
AW a91,0
AW a92,0
AW a93,0
AW a94,0
AW a95,0
AW a96,0
AW a97,0
AW a98,0
AW a99,0
AW a100,0
AW a101,0
AW a102,0
AW a103,0
AW a104,0
AW a105,0
AW a106,0
AW a107,0
AW a108,0
AW a109,0
AW a110,0
AW a111,0
AW a112,0
AW a113,0
AW a114,0
AW a115,0
AW a116,0
AW a117,0
AW a118,0
AW a119,0
AW a120,0
AW a121,0
AW a122,0
AW a123,0
AW a124,0
AW a125,0
AW a126,0
AW a127,0
v_mov_b32 v188,0
v_mov_b32 v189,0
v_mov_b32 v190,0
v_mov_b32 v191,0
v_mov_b32 v192,0
v_mov_b32 v193,0
v_mov_b32 v194,0
v_mov_b32 v195,0
v_mov_b32 v196,0
v_mov_b32 v197,0
v_mov_b32 v198,0
v_mov_b32 v199,0
v_mov_b32 v200,0
v_mov_b32 v201,0
v_mov_b32 v202,0
v_mov_b32 v203,0
v_mov_b32 v218,0xf149f2ca
v_mov_b32 v219,0xf149f2ca
v_mov_b32 v220,0xf149f2ca
v_mov_b32 v221,0xf149f2ca
s_mov_b32 s62,s60
s_lshl_b32 s84,s38,19
SA s63,s84,0
SA s64,s84,0x1000000
s_lshl_b32 s84,s36,13
SA s65,s84,0
SA s66,s84,0x10000
s_lshl_b32 s84,s41,10
v_add_u32 v210,s84,v254
SA s84,s84,0x10000
v_add_u32 v211,s84,v254
fk_sf0_loop:
W vmcnt(0) lgkmcnt(0)
s_barrier
SA m0,s65,0
s_nop 0
BL v208,s[44:47],s63 offen sc1 lds
BL v208,s[44:47],s63 offen offset:1024 sc1 lds
BL v208,s[44:47],s63 offen offset:2048 sc1 lds
BL v208,s[44:47],s63 offen offset:3072 sc1 lds
SA m0,s65,0x1000
s_nop 0
BL v209,s[44:47],s63 offen sc1 lds
BL v209,s[44:47],s63 offen offset:1024 sc1 lds
BL v209,s[44:47],s63 offen offset:2048 sc1 lds
BL v209,s[44:47],s63 offen offset:3072 sc1 lds
SA m0,s66,0
s_nop 0
BL v208,s[44:47],s64 offen sc1 lds
BL v208,s[44:47],s64 offen offset:1024 sc1 lds
BL v208,s[44:47],s64 offen offset:2048 sc1 lds
BL v208,s[44:47],s64 offen offset:3072 sc1 lds
SA m0,s66,0x1000
s_nop 0
BL v209,s[44:47],s64 offen sc1 lds
BL v209,s[44:47],s64 offen offset:1024 sc1 lds
BL v209,s[44:47],s64 offen offset:2048 sc1 lds
BL v209,s[44:47],s64 offen offset:3072 sc1 lds
SA s63,s63,0x8000
SA s64,s64,0x8000
W vmcnt(0)
s_barrier
RD a[192:195],v210
RD a[196:199],v210 offset:4096
RD a[200:203],v210 offset:8192
RD a[204:207],v210 offset:12288
RD a[208:211],v210 offset:2048
RD a[212:215],v210 offset:6144
RD a[216:219],v210 offset:10240
RD a[220:223],v210 offset:14336
RD a[224:227],v210 offset:16384
RD a[228:231],v210 offset:20480
RD a[232:235],v210 offset:24576
RD a[236:239],v210 offset:28672
RD a[240:243],v210 offset:18432
RD a[244:247],v210 offset:22528
RD a[248:251],v210 offset:26624
RD a[252:255],v210 offset:30720
RD v[96:99],v211
RD v[100:103],v211 offset:16384
RD v[104:107],v211 offset:2048
RD v[108:111],v211 offset:18432
RD v[112:115],v211 offset:4096
RD v[116:119],v211 offset:20480
RD v[120:123],v211 offset:6144
RD v[124:127],v211 offset:22528
RD v[128:131],v211 offset:8192
RD v[132:135],v211 offset:24576
RD v[136:139],v211 offset:10240
RD v[140:143],v211 offset:26624
RD v[144:147],v211 offset:12288
RD v[148:151],v211 offset:28672
RD v[152:155],v211 offset:14336
RD v[156:159],v211 offset:30720
W lgkmcnt(0)
s_cmp_eq_u32 s62,1
s_cbranch_scc1 fk_sf0_mask
M v[0:3],a[192:195],a[128:131],0
M v[0:3],a[196:199],a[132:135],v[0:3]
M v[0:3],a[200:203],a[136:139],v[0:3]
M v[0:3],a[204:207],a[140:143],v[0:3]
M v[4:7],a[208:211],a[128:131],0
M v[4:7],a[212:215],a[132:135],v[4:7]
M v[4:7],a[216:219],a[136:139],v[4:7]
M v[4:7],a[220:223],a[140:143],v[4:7]
M v[8:11],a[224:227],a[128:131],0
M v[8:11],a[228:231],a[132:135],v[8:11]
M v[8:11],a[232:235],a[136:139],v[8:11]
M v[8:11],a[236:239],a[140:143],v[8:11]
M v[12:15],a[240:243],a[128:131],0
M v[12:15],a[244:247],a[132:135],v[12:15]
M v[12:15],a[248:251],a[136:139],v[12:15]
M v[12:15],a[252:255],a[140:143],v[12:15]
M v[16:19],a[192:195],a[144:147],0
M v[16:19],a[196:199],a[148:151],v[16:19]
M v[16:19],a[200:203],a[152:155],v[16:19]
M v[16:19],a[204:207],a[156:159],v[16:19]
M v[20:23],a[208:211],a[144:147],0
M v[20:23],a[212:215],a[148:151],v[20:23]
M v[20:23],a[216:219],a[152:155],v[20:23]
M v[20:23],a[220:223],a[156:159],v[20:23]
M v[24:27],a[224:227],a[144:147],0
M v[24:27],a[228:231],a[148:151],v[24:27]
M v[24:27],a[232:235],a[152:155],v[24:27]
M v[24:27],a[236:239],a[156:159],v[24:27]
M v[28:31],a[240:243],a[144:147],0
M v[28:31],a[244:247],a[148:151],v[28:31]
M v[28:31],a[248:251],a[152:155],v[28:31]
M v[28:31],a[252:255],a[156:159],v[28:31]
M v[32:35],a[192:195],a[160:163],0
M v[32:35],a[196:199],a[164:167],v[32:35]
M v[32:35],a[200:203],a[168:171],v[32:35]
M v[32:35],a[204:207],a[172:175],v[32:35]
M v[36:39],a[208:211],a[160:163],0
M v[36:39],a[212:215],a[164:167],v[36:39]
M v[36:39],a[216:219],a[168:171],v[36:39]
M v[36:39],a[220:223],a[172:175],v[36:39]
M v[40:43],a[224:227],a[160:163],0
M v[40:43],a[228:231],a[164:167],v[40:43]
M v[40:43],a[232:235],a[168:171],v[40:43]
M v[40:43],a[236:239],a[172:175],v[40:43]
M v[44:47],a[240:243],a[160:163],0
M v[44:47],a[244:247],a[164:167],v[44:47]
M v[44:47],a[248:251],a[168:171],v[44:47]
M v[44:47],a[252:255],a[172:175],v[44:47]
M v[48:51],a[192:195],a[176:179],0
M v[48:51],a[196:199],a[180:183],v[48:51]
M v[48:51],a[200:203],a[184:187],v[48:51]
M v[48:51],a[204:207],a[188:191],v[48:51]
M v[52:55],a[208:211],a[176:179],0
M v[52:55],a[212:215],a[180:183],v[52:55]
M v[52:55],a[216:219],a[184:187],v[52:55]
M v[52:55],a[220:223],a[188:191],v[52:55]
M v[56:59],a[224:227],a[176:179],0
M v[56:59],a[228:231],a[180:183],v[56:59]
M v[56:59],a[232:235],a[184:187],v[56:59]
M v[56:59],a[236:239],a[188:191],v[56:59]
M v[60:63],a[240:243],a[176:179],0
M v[60:63],a[244:247],a[180:183],v[60:63]
M v[60:63],a[248:251],a[184:187],v[60:63]
M v[60:63],a[252:255],a[188:191],v[60:63]
s_branch fk_sf0_qk
fk_sf0_mask:
v_mov_b32 v4,0xff800000
v_mov_b32 v5,0xff800000
v_mov_b32 v6,0xff800000
v_mov_b32 v7,0xff800000
v_mov_b32 v8,0xff800000
v_mov_b32 v9,0xff800000
v_mov_b32 v10,0xff800000
v_mov_b32 v11,0xff800000
v_mov_b32 v12,0xff800000
v_mov_b32 v13,0xff800000
v_mov_b32 v14,0xff800000
v_mov_b32 v15,0xff800000
v_mov_b32 v24,0xff800000
v_mov_b32 v25,0xff800000
v_mov_b32 v26,0xff800000
v_mov_b32 v27,0xff800000
v_mov_b32 v28,0xff800000
v_mov_b32 v29,0xff800000
v_mov_b32 v30,0xff800000
v_mov_b32 v31,0xff800000
v_mov_b32 v44,0xff800000
v_mov_b32 v45,0xff800000
v_mov_b32 v46,0xff800000
v_mov_b32 v47,0xff800000
M v[0:3],a[192:195],a[128:131],v[160:163]
M v[0:3],a[196:199],a[132:135],v[0:3]
M v[0:3],a[200:203],a[136:139],v[0:3]
M v[0:3],a[204:207],a[140:143],v[0:3]
M v[16:19],a[192:195],a[144:147],0
M v[16:19],a[196:199],a[148:151],v[16:19]
M v[16:19],a[200:203],a[152:155],v[16:19]
M v[16:19],a[204:207],a[156:159],v[16:19]
M v[20:23],a[208:211],a[144:147],v[160:163]
M v[20:23],a[212:215],a[148:151],v[20:23]
M v[20:23],a[216:219],a[152:155],v[20:23]
M v[20:23],a[220:223],a[156:159],v[20:23]
M v[32:35],a[192:195],a[160:163],0
M v[32:35],a[196:199],a[164:167],v[32:35]
M v[32:35],a[200:203],a[168:171],v[32:35]
M v[32:35],a[204:207],a[172:175],v[32:35]
M v[36:39],a[208:211],a[160:163],0
M v[36:39],a[212:215],a[164:167],v[36:39]
M v[36:39],a[216:219],a[168:171],v[36:39]
M v[36:39],a[220:223],a[172:175],v[36:39]
M v[40:43],a[224:227],a[160:163],v[160:163]
M v[40:43],a[228:231],a[164:167],v[40:43]
M v[40:43],a[232:235],a[168:171],v[40:43]
M v[40:43],a[236:239],a[172:175],v[40:43]
)~"
R"~(M v[48:51],a[192:195],a[176:179],0
M v[48:51],a[196:199],a[180:183],v[48:51]
M v[48:51],a[200:203],a[184:187],v[48:51]
M v[48:51],a[204:207],a[188:191],v[48:51]
M v[52:55],a[208:211],a[176:179],0
M v[52:55],a[212:215],a[180:183],v[52:55]
M v[52:55],a[216:219],a[184:187],v[52:55]
M v[52:55],a[220:223],a[188:191],v[52:55]
M v[56:59],a[224:227],a[176:179],0
M v[56:59],a[228:231],a[180:183],v[56:59]
M v[56:59],a[232:235],a[184:187],v[56:59]
M v[56:59],a[236:239],a[188:191],v[56:59]
M v[60:63],a[240:243],a[176:179],v[160:163]
M v[60:63],a[244:247],a[180:183],v[60:63]
M v[60:63],a[248:251],a[184:187],v[60:63]
M v[60:63],a[252:255],a[188:191],v[60:63]
fk_sf0_qk:
s_nop 7
v_max3_f32 v224,v0,v1,v2
v_max3_f32 v224,v224,v3,v4
v_max3_f32 v224,v224,v5,v6
v_max3_f32 v224,v224,v7,v8
v_max3_f32 v224,v224,v9,v10
v_max3_f32 v224,v224,v11,v12
v_max3_f32 v224,v224,v13,v14
v_max_f32 v224,v224,v15
v_max3_f32 v225,v16,v17,v18
v_max3_f32 v225,v225,v19,v20
v_max3_f32 v225,v225,v21,v22
v_max3_f32 v225,v225,v23,v24
v_max3_f32 v225,v225,v25,v26
v_max3_f32 v225,v225,v27,v28
v_max3_f32 v225,v225,v29,v30
v_max_f32 v225,v225,v31
v_max3_f32 v226,v32,v33,v34
v_max3_f32 v226,v226,v35,v36
v_max3_f32 v226,v226,v37,v38
v_max3_f32 v226,v226,v39,v40
v_max3_f32 v226,v226,v41,v42
v_max3_f32 v226,v226,v43,v44
v_max3_f32 v226,v226,v45,v46
v_max_f32 v226,v226,v47
v_max3_f32 v227,v48,v49,v50
v_max3_f32 v227,v227,v51,v52
v_max3_f32 v227,v227,v53,v54
v_max3_f32 v227,v227,v55,v56
v_max3_f32 v227,v227,v57,v58
v_max3_f32 v227,v227,v59,v60
v_max3_f32 v227,v227,v61,v62
v_max_f32 v227,v227,v63
v_mov_b32 v228,v224
v_mov_b32 v229,v225
v_mov_b32 v230,v226
v_mov_b32 v231,v227
s_nop 1
v_permlane16_swap_b32 v224,v228
v_permlane16_swap_b32 v225,v229
v_permlane16_swap_b32 v226,v230
v_permlane16_swap_b32 v227,v231
v_max_f32 v224,v224,v228
v_max_f32 v225,v225,v229
v_max_f32 v226,v226,v230
v_max_f32 v227,v227,v231
v_mov_b32 v228,v224
v_mov_b32 v229,v225
v_mov_b32 v230,v226
v_mov_b32 v231,v227
s_nop 1
v_permlane32_swap_b32 v224,v228
v_permlane32_swap_b32 v225,v229
v_permlane32_swap_b32 v226,v230
v_permlane32_swap_b32 v227,v231
v_max_f32 v224,v224,v228
v_max_f32 v225,v225,v229
v_max_f32 v226,v226,v230
v_max_f32 v227,v227,v231
v_max_f32 v224,v224,v218
v_sub_f32 v228,v218,v224
v_mov_b32 v218,v224
v_max_f32 v225,v225,v219
v_sub_f32 v229,v219,v225
v_mov_b32 v219,v225
v_max_f32 v226,v226,v220
v_sub_f32 v230,v220,v226
v_mov_b32 v220,v226
v_max_f32 v227,v227,v221
v_sub_f32 v231,v221,v227
v_mov_b32 v221,v227
EX v228,v228
EX v229,v229
EX v230,v230
EX v231,v231
v_sub_f32 v0,v0,v218
v_sub_f32 v1,v1,v218
v_sub_f32 v2,v2,v218
v_sub_f32 v3,v3,v218
v_sub_f32 v4,v4,v218
v_sub_f32 v5,v5,v218
v_sub_f32 v6,v6,v218
v_sub_f32 v7,v7,v218
v_sub_f32 v8,v8,v218
v_sub_f32 v9,v9,v218
v_sub_f32 v10,v10,v218
v_sub_f32 v11,v11,v218
v_sub_f32 v12,v12,v218
v_sub_f32 v13,v13,v218
v_sub_f32 v14,v14,v218
v_sub_f32 v15,v15,v218
v_sub_f32 v16,v16,v219
v_sub_f32 v17,v17,v219
v_sub_f32 v18,v18,v219
v_sub_f32 v19,v19,v219
v_sub_f32 v20,v20,v219
v_sub_f32 v21,v21,v219
v_sub_f32 v22,v22,v219
v_sub_f32 v23,v23,v219
v_sub_f32 v24,v24,v219
v_sub_f32 v25,v25,v219
v_sub_f32 v26,v26,v219
v_sub_f32 v27,v27,v219
v_sub_f32 v28,v28,v219
v_sub_f32 v29,v29,v219
v_sub_f32 v30,v30,v219
v_sub_f32 v31,v31,v219
v_sub_f32 v32,v32,v220
v_sub_f32 v33,v33,v220
v_sub_f32 v34,v34,v220
v_sub_f32 v35,v35,v220
v_sub_f32 v36,v36,v220
v_sub_f32 v37,v37,v220
v_sub_f32 v38,v38,v220
v_sub_f32 v39,v39,v220
v_sub_f32 v40,v40,v220
v_sub_f32 v41,v41,v220
v_sub_f32 v42,v42,v220
v_sub_f32 v43,v43,v220
v_sub_f32 v44,v44,v220
v_sub_f32 v45,v45,v220
v_sub_f32 v46,v46,v220
v_sub_f32 v47,v47,v220
v_sub_f32 v48,v48,v221
v_sub_f32 v49,v49,v221
v_sub_f32 v50,v50,v221
v_sub_f32 v51,v51,v221
v_sub_f32 v52,v52,v221
v_sub_f32 v53,v53,v221
v_sub_f32 v54,v54,v221
v_sub_f32 v55,v55,v221
v_sub_f32 v56,v56,v221
v_sub_f32 v57,v57,v221
v_sub_f32 v58,v58,v221
v_sub_f32 v59,v59,v221
v_sub_f32 v60,v60,v221
v_sub_f32 v61,v61,v221
v_sub_f32 v62,v62,v221
v_sub_f32 v63,v63,v221
EX v0,v0
EX v1,v1
EX v2,v2
EX v3,v3
EX v4,v4
EX v5,v5
EX v6,v6
EX v7,v7
EX v8,v8
EX v9,v9
EX v10,v10
EX v11,v11
EX v12,v12
EX v13,v13
EX v14,v14
EX v15,v15
EX v16,v16
EX v17,v17
EX v18,v18
EX v19,v19
EX v20,v20
EX v21,v21
EX v22,v22
EX v23,v23
EX v24,v24
EX v25,v25
EX v26,v26
EX v27,v27
EX v28,v28
EX v29,v29
EX v30,v30
EX v31,v31
EX v32,v32
EX v33,v33
EX v34,v34
EX v35,v35
EX v36,v36
EX v37,v37
EX v38,v38
EX v39,v39
EX v40,v40
EX v41,v41
EX v42,v42
EX v43,v43
EX v44,v44
EX v45,v45
EX v46,v46
EX v47,v47
EX v48,v48
EX v49,v49
EX v50,v50
EX v51,v51
EX v52,v52
EX v53,v53
EX v54,v54
EX v55,v55
EX v56,v56
EX v57,v57
EX v58,v58
EX v59,v59
EX v60,v60
EX v61,v61
EX v62,v62
EX v63,v63
s_nop 0
CV v64,v0,v1
CV v65,v2,v3
CV v66,v4,v5
CV v67,v6,v7
CV v68,v8,v9
CV v69,v10,v11
CV v70,v12,v13
CV v71,v14,v15
CV v72,v16,v17
CV v73,v18,v19
CV v74,v20,v21
CV v75,v22,v23
CV v76,v24,v25
CV v77,v26,v27
CV v78,v28,v29
CV v79,v30,v31
CV v80,v32,v33
CV v81,v34,v35
CV v82,v36,v37
CV v83,v38,v39
CV v84,v40,v41
CV v85,v42,v43
CV v86,v44,v45
CV v87,v46,v47
CV v88,v48,v49
CV v89,v50,v51
CV v90,v52,v53
CV v91,v54,v55
CV v92,v56,v57
CV v93,v58,v59
CV v94,v60,v61
CV v95,v62,v63
AR v232,a0
AR v233,a1
AR v234,a2
AR v235,a3
AR v236,a4
AR v237,a5
AR v238,a6
AR v239,a7
v_mul_f32 v232,v232,v228
v_mul_f32 v233,v233,v228
v_mul_f32 v234,v234,v228
v_mul_f32 v235,v235,v228
v_mul_f32 v236,v236,v228
v_mul_f32 v237,v237,v228
v_mul_f32 v238,v238,v228
v_mul_f32 v239,v239,v228
AW a0,v232
AW a1,v233
AW a2,v234
AW a3,v235
AW a4,v236
AW a5,v237
AW a6,v238
AW a7,v239
AR v232,a8
AR v233,a9
AR v234,a10
AR v235,a11
AR v236,a12
AR v237,a13
AR v238,a14
AR v239,a15
v_mul_f32 v232,v232,v228
v_mul_f32 v233,v233,v228
v_mul_f32 v234,v234,v228
v_mul_f32 v235,v235,v228
v_mul_f32 v236,v236,v228
v_mul_f32 v237,v237,v228
v_mul_f32 v238,v238,v228
v_mul_f32 v239,v239,v228
AW a8,v232
AW a9,v233
AW a10,v234
AW a11,v235
AW a12,v236
AW a13,v237
AW a14,v238
AW a15,v239
AR v232,a16
AR v233,a17
AR v234,a18
AR v235,a19
AR v236,a20
AR v237,a21
AR v238,a22
AR v239,a23
v_mul_f32 v232,v232,v228
v_mul_f32 v233,v233,v228
v_mul_f32 v234,v234,v228
v_mul_f32 v235,v235,v228
v_mul_f32 v236,v236,v228
v_mul_f32 v237,v237,v228
v_mul_f32 v238,v238,v228
v_mul_f32 v239,v239,v228
AW a16,v232
AW a17,v233
AW a18,v234
AW a19,v235
AW a20,v236
AW a21,v237
AW a22,v238
AW a23,v239
AR v232,a24
AR v233,a25
AR v234,a26
AR v235,a27
AR v236,a28
AR v237,a29
AR v238,a30
AR v239,a31
v_mul_f32 v232,v232,v228
v_mul_f32 v233,v233,v228
v_mul_f32 v234,v234,v228
v_mul_f32 v235,v235,v228
v_mul_f32 v236,v236,v228
v_mul_f32 v237,v237,v228
v_mul_f32 v238,v238,v228
v_mul_f32 v239,v239,v228
AW a24,v232
AW a25,v233
AW a26,v234
AW a27,v235
AW a28,v236
AW a29,v237
AW a30,v238
AW a31,v239
v_mul_f32 v188,v188,v228
v_mul_f32 v189,v189,v228
v_mul_f32 v190,v190,v228
v_mul_f32 v191,v191,v228
AR v232,a32
AR v233,a33
AR v234,a34
AR v235,a35
AR v236,a36
AR v237,a37
AR v238,a38
AR v239,a39
v_mul_f32 v232,v232,v229
v_mul_f32 v233,v233,v229
v_mul_f32 v234,v234,v229
v_mul_f32 v235,v235,v229
v_mul_f32 v236,v236,v229
v_mul_f32 v237,v237,v229
v_mul_f32 v238,v238,v229
v_mul_f32 v239,v239,v229
AW a32,v232
AW a33,v233
AW a34,v234
AW a35,v235
AW a36,v236
AW a37,v237
AW a38,v238
AW a39,v239
AR v232,a40
AR v233,a41
AR v234,a42
AR v235,a43
AR v236,a44
AR v237,a45
AR v238,a46
AR v239,a47
v_mul_f32 v232,v232,v229
v_mul_f32 v233,v233,v229
v_mul_f32 v234,v234,v229
v_mul_f32 v235,v235,v229
v_mul_f32 v236,v236,v229
v_mul_f32 v237,v237,v229
v_mul_f32 v238,v238,v229
v_mul_f32 v239,v239,v229
AW a40,v232
AW a41,v233
AW a42,v234
AW a43,v235
AW a44,v236
AW a45,v237
AW a46,v238
AW a47,v239
AR v232,a48
AR v233,a49
AR v234,a50
AR v235,a51
AR v236,a52
AR v237,a53
AR v238,a54
AR v239,a55
v_mul_f32 v232,v232,v229
v_mul_f32 v233,v233,v229
v_mul_f32 v234,v234,v229
v_mul_f32 v235,v235,v229
v_mul_f32 v236,v236,v229
v_mul_f32 v237,v237,v229
v_mul_f32 v238,v238,v229
v_mul_f32 v239,v239,v229
AW a48,v232
AW a49,v233
AW a50,v234
AW a51,v235
AW a52,v236
AW a53,v237
AW a54,v238
AW a55,v239
AR v232,a56
AR v233,a57
AR v234,a58
AR v235,a59
AR v236,a60
AR v237,a61
AR v238,a62
AR v239,a63
v_mul_f32 v232,v232,v229
v_mul_f32 v233,v233,v229
v_mul_f32 v234,v234,v229
v_mul_f32 v235,v235,v229
v_mul_f32 v236,v236,v229
v_mul_f32 v237,v237,v229
v_mul_f32 v238,v238,v229
v_mul_f32 v239,v239,v229
AW a56,v232
AW a57,v233
AW a58,v234
AW a59,v235
AW a60,v236
AW a61,v237
AW a62,v238
AW a63,v239
v_mul_f32 v192,v192,v229
v_mul_f32 v193,v193,v229
v_mul_f32 v194,v194,v229
v_mul_f32 v195,v195,v229
AR v232,a64
AR v233,a65
AR v234,a66
AR v235,a67
AR v236,a68
AR v237,a69
AR v238,a70
AR v239,a71
v_mul_f32 v232,v232,v230
v_mul_f32 v233,v233,v230
v_mul_f32 v234,v234,v230
v_mul_f32 v235,v235,v230
v_mul_f32 v236,v236,v230
v_mul_f32 v237,v237,v230
v_mul_f32 v238,v238,v230
v_mul_f32 v239,v239,v230
AW a64,v232
AW a65,v233
AW a66,v234
AW a67,v235
AW a68,v236
AW a69,v237
AW a70,v238
AW a71,v239
AR v232,a72
AR v233,a73
AR v234,a74
AR v235,a75
AR v236,a76
AR v237,a77
AR v238,a78
AR v239,a79
v_mul_f32 v232,v232,v230
v_mul_f32 v233,v233,v230
v_mul_f32 v234,v234,v230
v_mul_f32 v235,v235,v230
v_mul_f32 v236,v236,v230
v_mul_f32 v237,v237,v230
v_mul_f32 v238,v238,v230
v_mul_f32 v239,v239,v230
AW a72,v232
AW a73,v233
AW a74,v234
AW a75,v235
AW a76,v236
AW a77,v237
AW a78,v238
AW a79,v239
AR v232,a80
AR v233,a81
AR v234,a82
AR v235,a83
AR v236,a84
AR v237,a85
AR v238,a86
AR v239,a87
v_mul_f32 v232,v232,v230
v_mul_f32 v233,v233,v230
v_mul_f32 v234,v234,v230
v_mul_f32 v235,v235,v230
v_mul_f32 v236,v236,v230
v_mul_f32 v237,v237,v230
v_mul_f32 v238,v238,v230
v_mul_f32 v239,v239,v230
AW a80,v232
AW a81,v233
AW a82,v234
AW a83,v235
AW a84,v236
AW a85,v237
AW a86,v238
AW a87,v239
AR v232,a88
AR v233,a89
AR v234,a90
AR v235,a91
AR v236,a92
AR v237,a93
AR v238,a94
AR v239,a95
v_mul_f32 v232,v232,v230
v_mul_f32 v233,v233,v230
v_mul_f32 v234,v234,v230
v_mul_f32 v235,v235,v230
v_mul_f32 v236,v236,v230
v_mul_f32 v237,v237,v230
v_mul_f32 v238,v238,v230
v_mul_f32 v239,v239,v230
AW a88,v232
AW a89,v233
AW a90,v234
AW a91,v235
AW a92,v236
AW a93,v237
AW a94,v238
AW a95,v239
v_mul_f32 v196,v196,v230
v_mul_f32 v197,v197,v230
v_mul_f32 v198,v198,v230
v_mul_f32 v199,v199,v230
AR v232,a96
AR v233,a97
AR v234,a98
AR v235,a99
AR v236,a100
AR v237,a101
AR v238,a102
AR v239,a103
v_mul_f32 v232,v232,v231
v_mul_f32 v233,v233,v231
v_mul_f32 v234,v234,v231
v_mul_f32 v235,v235,v231
v_mul_f32 v236,v236,v231
v_mul_f32 v237,v237,v231
v_mul_f32 v238,v238,v231
v_mul_f32 v239,v239,v231
AW a96,v232
AW a97,v233
AW a98,v234
AW a99,v235
AW a100,v236
AW a101,v237
AW a102,v238
AW a103,v239
AR v232,a104
AR v233,a105
AR v234,a106
AR v235,a107
AR v236,a108
AR v237,a109
AR v238,a110
AR v239,a111
v_mul_f32 v232,v232,v231
v_mul_f32 v233,v233,v231
v_mul_f32 v234,v234,v231
v_mul_f32 v235,v235,v231
v_mul_f32 v236,v236,v231
v_mul_f32 v237,v237,v231
v_mul_f32 v238,v238,v231
v_mul_f32 v239,v239,v231
AW a104,v232
AW a105,v233
AW a106,v234
AW a107,v235
AW a108,v236
AW a109,v237
AW a110,v238
AW a111,v239
AR v232,a112
AR v233,a113
AR v234,a114
AR v235,a115
AR v236,a116
AR v237,a117
AR v238,a118
AR v239,a119
v_mul_f32 v232,v232,v231
v_mul_f32 v233,v233,v231
v_mul_f32 v234,v234,v231
v_mul_f32 v235,v235,v231
v_mul_f32 v236,v236,v231
v_mul_f32 v237,v237,v231
v_mul_f32 v238,v238,v231
v_mul_f32 v239,v239,v231
AW a112,v232
AW a113,v233
AW a114,v234
AW a115,v235
AW a116,v236
AW a117,v237
AW a118,v238
AW a119,v239
AR v232,a120
AR v233,a121
AR v234,a122
AR v235,a123
AR v236,a124
AR v237,a125
AR v238,a126
AR v239,a127
v_mul_f32 v232,v232,v231
v_mul_f32 v233,v233,v231
v_mul_f32 v234,v234,v231
v_mul_f32 v235,v235,v231
v_mul_f32 v236,v236,v231
v_mul_f32 v237,v237,v231
v_mul_f32 v238,v238,v231
v_mul_f32 v239,v239,v231
AW a120,v232
AW a121,v233
AW a122,v234
AW a123,v235
AW a124,v236
AW a125,v237
AW a126,v238
AW a127,v239
v_mul_f32 v200,v200,v231
v_mul_f32 v201,v201,v231
v_mul_f32 v202,v202,v231
v_mul_f32 v203,v203,v231
s_nop 1
M a[0:3],v[96:99],v[64:67],a[0:3]
M a[0:3],v[100:103],v[68:71],a[0:3]
M a[4:7],v[104:107],v[64:67],a[4:7]
M a[4:7],v[108:111],v[68:71],a[4:7]
M a[8:11],v[112:115],v[64:67],a[8:11]
M a[8:11],v[116:119],v[68:71],a[8:11]
M a[12:15],v[120:123],v[64:67],a[12:15]
M a[12:15],v[124:127],v[68:71],a[12:15]
M a[16:19],v[128:131],v[64:67],a[16:19]
M a[16:19],v[132:135],v[68:71],a[16:19]
M a[20:23],v[136:139],v[64:67],a[20:23]
M a[20:23],v[140:143],v[68:71],a[20:23]
M a[24:27],v[144:147],v[64:67],a[24:27]
M a[24:27],v[148:151],v[68:71],a[24:27]
M a[28:31],v[152:155],v[64:67],a[28:31]
M a[28:31],v[156:159],v[68:71],a[28:31]
M v[188:191],v[204:207],v[64:67],v[188:191]
M v[188:191],v[204:207],v[68:71],v[188:191]
M a[32:35],v[96:99],v[72:75],a[32:35]
M a[32:35],v[100:103],v[76:79],a[32:35]
M a[36:39],v[104:107],v[72:75],a[36:39]
M a[36:39],v[108:111],v[76:79],a[36:39]
M a[40:43],v[112:115],v[72:75],a[40:43]
M a[40:43],v[116:119],v[76:79],a[40:43]
M a[44:47],v[120:123],v[72:75],a[44:47]
M a[44:47],v[124:127],v[76:79],a[44:47]
M a[48:51],v[128:131],v[72:75],a[48:51]
M a[48:51],v[132:135],v[76:79],a[48:51]
M a[52:55],v[136:139],v[72:75],a[52:55]
M a[52:55],v[140:143],v[76:79],a[52:55]
M a[56:59],v[144:147],v[72:75],a[56:59]
M a[56:59],v[148:151],v[76:79],a[56:59]
M a[60:63],v[152:155],v[72:75],a[60:63]
M a[60:63],v[156:159],v[76:79],a[60:63]
M v[192:195],v[204:207],v[72:75],v[192:195]
M v[192:195],v[204:207],v[76:79],v[192:195]
M a[64:67],v[96:99],v[80:83],a[64:67]
M a[64:67],v[100:103],v[84:87],a[64:67]
M a[68:71],v[104:107],v[80:83],a[68:71]
M a[68:71],v[108:111],v[84:87],a[68:71]
M a[72:75],v[112:115],v[80:83],a[72:75]
M a[72:75],v[116:119],v[84:87],a[72:75]
M a[76:79],v[120:123],v[80:83],a[76:79]
M a[76:79],v[124:127],v[84:87],a[76:79]
M a[80:83],v[128:131],v[80:83],a[80:83]
M a[80:83],v[132:135],v[84:87],a[80:83]
M a[84:87],v[136:139],v[80:83],a[84:87]
M a[84:87],v[140:143],v[84:87],a[84:87]
M a[88:91],v[144:147],v[80:83],a[88:91]
M a[88:91],v[148:151],v[84:87],a[88:91]
M a[92:95],v[152:155],v[80:83],a[92:95]
M a[92:95],v[156:159],v[84:87],a[92:95]
M v[196:199],v[204:207],v[80:83],v[196:199]
M v[196:199],v[204:207],v[84:87],v[196:199]
)~"
R"~(M a[96:99],v[96:99],v[88:91],a[96:99]
M a[96:99],v[100:103],v[92:95],a[96:99]
M a[100:103],v[104:107],v[88:91],a[100:103]
M a[100:103],v[108:111],v[92:95],a[100:103]
M a[104:107],v[112:115],v[88:91],a[104:107]
M a[104:107],v[116:119],v[92:95],a[104:107]
M a[108:111],v[120:123],v[88:91],a[108:111]
M a[108:111],v[124:127],v[92:95],a[108:111]
M a[112:115],v[128:131],v[88:91],a[112:115]
M a[112:115],v[132:135],v[92:95],a[112:115]
M a[116:119],v[136:139],v[88:91],a[116:119]
M a[116:119],v[140:143],v[92:95],a[116:119]
M a[120:123],v[144:147],v[88:91],a[120:123]
M a[120:123],v[148:151],v[92:95],a[120:123]
M a[124:127],v[152:155],v[88:91],a[124:127]
M a[124:127],v[156:159],v[92:95],a[124:127]
M v[200:203],v[204:207],v[88:91],v[200:203]
M v[200:203],v[204:207],v[92:95],v[200:203]
s_nop 7
s_sub_u32 s62,s62,1
s_cmp_lg_u32 s62,0
s_cbranch_scc1 fk_sf0_loop
s_and_b32 s88,s65,0x8000
s_lshl_b32 s89,s36,10
SA s89,s89,s88
v_add_u32 v226,s89,v254
s_lshl_b32 s89,s40,11
SA s90,s89,s88
v_add_u32 v227,s90,v254
v_xor_b32 v217,0x800,v227
W lgkmcnt(0)
s_barrier
v_mov_b32 v228,v218
v_mov_b32 v229,v219
v_mov_b32 v230,v220
v_mov_b32 v231,v221
WR v226,v[228:231]
v_xor_b32 v227,0x400,v226
W lgkmcnt(0)
s_barrier
RD v[228:231],v227
W lgkmcnt(0)
v_max_f32 v228,v228,v218
v_max_f32 v229,v229,v219
v_max_f32 v230,v230,v220
v_max_f32 v231,v231,v221
v_sub_f32 v228,v218,v228
v_sub_f32 v229,v219,v229
v_sub_f32 v230,v220,v230
v_sub_f32 v231,v221,v231
EX v228,v228
EX v229,v229
EX v230,v230
EX v231,v231
s_nop 0
AR v232,a0
AR v233,a1
AR v234,a2
AR v235,a3
AR v236,a4
AR v237,a5
AR v238,a6
AR v239,a7
v_mul_f32 v232,v232,v228
v_mul_f32 v233,v233,v228
v_mul_f32 v234,v234,v228
v_mul_f32 v235,v235,v228
v_mul_f32 v236,v236,v228
v_mul_f32 v237,v237,v228
v_mul_f32 v238,v238,v228
v_mul_f32 v239,v239,v228
AW a0,v232
AW a1,v233
AW a2,v234
AW a3,v235
AW a4,v236
AW a5,v237
AW a6,v238
AW a7,v239
AR v232,a8
AR v233,a9
AR v234,a10
AR v235,a11
AR v236,a12
AR v237,a13
AR v238,a14
AR v239,a15
v_mul_f32 v232,v232,v228
v_mul_f32 v233,v233,v228
v_mul_f32 v234,v234,v228
v_mul_f32 v235,v235,v228
v_mul_f32 v236,v236,v228
v_mul_f32 v237,v237,v228
v_mul_f32 v238,v238,v228
v_mul_f32 v239,v239,v228
AW a8,v232
AW a9,v233
AW a10,v234
AW a11,v235
AW a12,v236
AW a13,v237
AW a14,v238
AW a15,v239
AR v232,a16
AR v233,a17
AR v234,a18
AR v235,a19
AR v236,a20
AR v237,a21
AR v238,a22
AR v239,a23
v_mul_f32 v232,v232,v228
v_mul_f32 v233,v233,v228
v_mul_f32 v234,v234,v228
v_mul_f32 v235,v235,v228
v_mul_f32 v236,v236,v228
v_mul_f32 v237,v237,v228
v_mul_f32 v238,v238,v228
v_mul_f32 v239,v239,v228
AW a16,v232
AW a17,v233
AW a18,v234
AW a19,v235
AW a20,v236
AW a21,v237
AW a22,v238
AW a23,v239
AR v232,a24
AR v233,a25
AR v234,a26
AR v235,a27
AR v236,a28
AR v237,a29
AR v238,a30
AR v239,a31
v_mul_f32 v232,v232,v228
v_mul_f32 v233,v233,v228
v_mul_f32 v234,v234,v228
v_mul_f32 v235,v235,v228
v_mul_f32 v236,v236,v228
v_mul_f32 v237,v237,v228
v_mul_f32 v238,v238,v228
v_mul_f32 v239,v239,v228
AW a24,v232
AW a25,v233
AW a26,v234
AW a27,v235
AW a28,v236
AW a29,v237
AW a30,v238
AW a31,v239
v_mul_f32 v188,v188,v228
v_mul_f32 v189,v189,v228
v_mul_f32 v190,v190,v228
v_mul_f32 v191,v191,v228
AR v232,a32
AR v233,a33
AR v234,a34
AR v235,a35
AR v236,a36
AR v237,a37
AR v238,a38
AR v239,a39
v_mul_f32 v232,v232,v229
v_mul_f32 v233,v233,v229
v_mul_f32 v234,v234,v229
v_mul_f32 v235,v235,v229
v_mul_f32 v236,v236,v229
v_mul_f32 v237,v237,v229
v_mul_f32 v238,v238,v229
v_mul_f32 v239,v239,v229
AW a32,v232
AW a33,v233
AW a34,v234
AW a35,v235
AW a36,v236
AW a37,v237
AW a38,v238
AW a39,v239
AR v232,a40
AR v233,a41
AR v234,a42
AR v235,a43
AR v236,a44
AR v237,a45
AR v238,a46
AR v239,a47
v_mul_f32 v232,v232,v229
v_mul_f32 v233,v233,v229
v_mul_f32 v234,v234,v229
v_mul_f32 v235,v235,v229
v_mul_f32 v236,v236,v229
v_mul_f32 v237,v237,v229
v_mul_f32 v238,v238,v229
v_mul_f32 v239,v239,v229
AW a40,v232
AW a41,v233
AW a42,v234
AW a43,v235
AW a44,v236
AW a45,v237
AW a46,v238
AW a47,v239
AR v232,a48
AR v233,a49
AR v234,a50
AR v235,a51
AR v236,a52
AR v237,a53
AR v238,a54
AR v239,a55
v_mul_f32 v232,v232,v229
v_mul_f32 v233,v233,v229
v_mul_f32 v234,v234,v229
v_mul_f32 v235,v235,v229
v_mul_f32 v236,v236,v229
v_mul_f32 v237,v237,v229
v_mul_f32 v238,v238,v229
v_mul_f32 v239,v239,v229
AW a48,v232
AW a49,v233
AW a50,v234
AW a51,v235
AW a52,v236
AW a53,v237
AW a54,v238
AW a55,v239
AR v232,a56
AR v233,a57
AR v234,a58
AR v235,a59
AR v236,a60
AR v237,a61
AR v238,a62
AR v239,a63
v_mul_f32 v232,v232,v229
v_mul_f32 v233,v233,v229
v_mul_f32 v234,v234,v229
v_mul_f32 v235,v235,v229
v_mul_f32 v236,v236,v229
v_mul_f32 v237,v237,v229
v_mul_f32 v238,v238,v229
v_mul_f32 v239,v239,v229
AW a56,v232
AW a57,v233
AW a58,v234
AW a59,v235
AW a60,v236
AW a61,v237
AW a62,v238
AW a63,v239
v_mul_f32 v192,v192,v229
v_mul_f32 v193,v193,v229
v_mul_f32 v194,v194,v229
v_mul_f32 v195,v195,v229
AR v232,a64
AR v233,a65
AR v234,a66
AR v235,a67
AR v236,a68
AR v237,a69
AR v238,a70
AR v239,a71
v_mul_f32 v232,v232,v230
v_mul_f32 v233,v233,v230
v_mul_f32 v234,v234,v230
v_mul_f32 v235,v235,v230
v_mul_f32 v236,v236,v230
v_mul_f32 v237,v237,v230
v_mul_f32 v238,v238,v230
v_mul_f32 v239,v239,v230
AW a64,v232
AW a65,v233
AW a66,v234
AW a67,v235
AW a68,v236
AW a69,v237
AW a70,v238
AW a71,v239
AR v232,a72
AR v233,a73
AR v234,a74
AR v235,a75
AR v236,a76
AR v237,a77
AR v238,a78
AR v239,a79
v_mul_f32 v232,v232,v230
v_mul_f32 v233,v233,v230
v_mul_f32 v234,v234,v230
v_mul_f32 v235,v235,v230
v_mul_f32 v236,v236,v230
v_mul_f32 v237,v237,v230
v_mul_f32 v238,v238,v230
v_mul_f32 v239,v239,v230
AW a72,v232
AW a73,v233
AW a74,v234
AW a75,v235
AW a76,v236
AW a77,v237
AW a78,v238
AW a79,v239
AR v232,a80
AR v233,a81
AR v234,a82
AR v235,a83
AR v236,a84
AR v237,a85
AR v238,a86
AR v239,a87
v_mul_f32 v232,v232,v230
v_mul_f32 v233,v233,v230
v_mul_f32 v234,v234,v230
v_mul_f32 v235,v235,v230
v_mul_f32 v236,v236,v230
v_mul_f32 v237,v237,v230
v_mul_f32 v238,v238,v230
v_mul_f32 v239,v239,v230
AW a80,v232
AW a81,v233
AW a82,v234
AW a83,v235
AW a84,v236
AW a85,v237
AW a86,v238
AW a87,v239
AR v232,a88
AR v233,a89
AR v234,a90
AR v235,a91
AR v236,a92
AR v237,a93
AR v238,a94
AR v239,a95
v_mul_f32 v232,v232,v230
v_mul_f32 v233,v233,v230
v_mul_f32 v234,v234,v230
v_mul_f32 v235,v235,v230
v_mul_f32 v236,v236,v230
v_mul_f32 v237,v237,v230
v_mul_f32 v238,v238,v230
v_mul_f32 v239,v239,v230
AW a88,v232
AW a89,v233
AW a90,v234
AW a91,v235
AW a92,v236
AW a93,v237
AW a94,v238
AW a95,v239
v_mul_f32 v196,v196,v230
v_mul_f32 v197,v197,v230
v_mul_f32 v198,v198,v230
v_mul_f32 v199,v199,v230
AR v232,a96
AR v233,a97
AR v234,a98
AR v235,a99
AR v236,a100
AR v237,a101
AR v238,a102
AR v239,a103
v_mul_f32 v232,v232,v231
v_mul_f32 v233,v233,v231
v_mul_f32 v234,v234,v231
v_mul_f32 v235,v235,v231
v_mul_f32 v236,v236,v231
v_mul_f32 v237,v237,v231
v_mul_f32 v238,v238,v231
v_mul_f32 v239,v239,v231
AW a96,v232
AW a97,v233
AW a98,v234
AW a99,v235
AW a100,v236
AW a101,v237
AW a102,v238
AW a103,v239
AR v232,a104
AR v233,a105
AR v234,a106
AR v235,a107
AR v236,a108
AR v237,a109
AR v238,a110
AR v239,a111
v_mul_f32 v232,v232,v231
v_mul_f32 v233,v233,v231
v_mul_f32 v234,v234,v231
v_mul_f32 v235,v235,v231
v_mul_f32 v236,v236,v231
v_mul_f32 v237,v237,v231
v_mul_f32 v238,v238,v231
v_mul_f32 v239,v239,v231
AW a104,v232
AW a105,v233
AW a106,v234
AW a107,v235
AW a108,v236
AW a109,v237
AW a110,v238
AW a111,v239
AR v232,a112
AR v233,a113
AR v234,a114
AR v235,a115
AR v236,a116
AR v237,a117
AR v238,a118
AR v239,a119
v_mul_f32 v232,v232,v231
v_mul_f32 v233,v233,v231
v_mul_f32 v234,v234,v231
v_mul_f32 v235,v235,v231
v_mul_f32 v236,v236,v231
v_mul_f32 v237,v237,v231
v_mul_f32 v238,v238,v231
v_mul_f32 v239,v239,v231
AW a112,v232
AW a113,v233
AW a114,v234
AW a115,v235
AW a116,v236
AW a117,v237
AW a118,v238
AW a119,v239
AR v232,a120
AR v233,a121
AR v234,a122
AR v235,a123
AR v236,a124
AR v237,a125
AR v238,a126
AR v239,a127
v_mul_f32 v232,v232,v231
v_mul_f32 v233,v233,v231
v_mul_f32 v234,v234,v231
v_mul_f32 v235,v235,v231
v_mul_f32 v236,v236,v231
v_mul_f32 v237,v237,v231
v_mul_f32 v238,v238,v231
v_mul_f32 v239,v239,v231
AW a120,v232
AW a121,v233
AW a122,v234
AW a123,v235
AW a124,v236
AW a125,v237
AW a126,v238
AW a127,v239
v_mul_f32 v200,v200,v231
v_mul_f32 v201,v201,v231
v_mul_f32 v202,v202,v231
v_mul_f32 v203,v203,v231
RD a[128:131],v212
RD a[132:135],v212 offset:1024
RD a[136:139],v212 offset:2048
RD a[140:143],v212 offset:3072
RD a[144:147],v212 offset:8192
RD a[148:151],v212 offset:9216
RD a[152:155],v212 offset:10240
RD a[156:159],v212 offset:11264
RD a[160:163],v212 offset:16384
RD a[164:167],v212 offset:17408
RD a[168:171],v212 offset:18432
RD a[172:175],v212 offset:19456
RD a[176:179],v212 offset:24576
RD a[180:183],v212 offset:25600
RD a[184:187],v212 offset:26624
RD a[188:191],v212 offset:27648
s_mov_b32 s76,2
W lgkmcnt(0)
s_barrier
s_branch fk_EPI
fk_safe2:
AW a0,0
AW a1,0
AW a2,0
AW a3,0
AW a4,0
AW a5,0
AW a6,0
AW a7,0
AW a8,0
AW a9,0
AW a10,0
AW a11,0
AW a12,0
AW a13,0
AW a14,0
AW a15,0
AW a16,0
AW a17,0
AW a18,0
AW a19,0
AW a20,0
AW a21,0
AW a22,0
AW a23,0
AW a24,0
AW a25,0
AW a26,0
AW a27,0
AW a28,0
AW a29,0
AW a30,0
AW a31,0
AW a32,0
AW a33,0
AW a34,0
AW a35,0
AW a36,0
AW a37,0
AW a38,0
AW a39,0
AW a40,0
AW a41,0
AW a42,0
AW a43,0
AW a44,0
AW a45,0
AW a46,0
AW a47,0
AW a48,0
AW a49,0
AW a50,0
AW a51,0
AW a52,0
AW a53,0
AW a54,0
AW a55,0
AW a56,0
AW a57,0
AW a58,0
AW a59,0
AW a60,0
AW a61,0
AW a62,0
AW a63,0
AW a64,0
AW a65,0
AW a66,0
AW a67,0
AW a68,0
AW a69,0
AW a70,0
AW a71,0
AW a72,0
AW a73,0
AW a74,0
AW a75,0
AW a76,0
AW a77,0
AW a78,0
AW a79,0
AW a80,0
AW a81,0
AW a82,0
AW a83,0
AW a84,0
AW a85,0
AW a86,0
AW a87,0
AW a88,0
AW a89,0
AW a90,0
AW a91,0
AW a92,0
AW a93,0
AW a94,0
AW a95,0
AW a96,0
AW a97,0
AW a98,0
AW a99,0
AW a100,0
AW a101,0
AW a102,0
AW a103,0
AW a104,0
AW a105,0
AW a106,0
AW a107,0
AW a108,0
AW a109,0
AW a110,0
AW a111,0
AW a112,0
AW a113,0
AW a114,0
AW a115,0
AW a116,0
AW a117,0
AW a118,0
AW a119,0
AW a120,0
AW a121,0
AW a122,0
AW a123,0
AW a124,0
AW a125,0
AW a126,0
AW a127,0
v_mov_b32 v188,0
v_mov_b32 v189,0
v_mov_b32 v190,0
v_mov_b32 v191,0
v_mov_b32 v192,0
v_mov_b32 v193,0
v_mov_b32 v194,0
v_mov_b32 v195,0
v_mov_b32 v196,0
v_mov_b32 v197,0
v_mov_b32 v198,0
v_mov_b32 v199,0
v_mov_b32 v200,0
v_mov_b32 v201,0
v_mov_b32 v202,0
v_mov_b32 v203,0
v_mov_b32 v218,0xf149f2ca
v_mov_b32 v219,0xf149f2ca
v_mov_b32 v220,0xf149f2ca
v_mov_b32 v221,0xf149f2ca
s_mov_b32 s62,s61
s_lshl_b32 s84,s38,19
SA s63,s84,0
SA s64,s84,0x1000000
s_lshl_b32 s84,s36,13
SA s65,s84,0
SA s66,s84,0x10000
s_lshl_b32 s84,s41,10
v_add_u32 v210,s84,v254
SA s84,s84,0x10000
v_add_u32 v211,s84,v254
fk_sf1_loop:
W vmcnt(0) lgkmcnt(0)
s_barrier
SA m0,s65,0
s_nop 0
BL v208,s[44:47],s63 offen sc1 lds
BL v208,s[44:47],s63 offen offset:1024 sc1 lds
BL v208,s[44:47],s63 offen offset:2048 sc1 lds
BL v208,s[44:47],s63 offen offset:3072 sc1 lds
SA m0,s65,0x1000
s_nop 0
BL v209,s[44:47],s63 offen sc1 lds
BL v209,s[44:47],s63 offen offset:1024 sc1 lds
BL v209,s[44:47],s63 offen offset:2048 sc1 lds
BL v209,s[44:47],s63 offen offset:3072 sc1 lds
SA m0,s66,0
s_nop 0
BL v208,s[44:47],s64 offen sc1 lds
BL v208,s[44:47],s64 offen offset:1024 sc1 lds
BL v208,s[44:47],s64 offen offset:2048 sc1 lds
BL v208,s[44:47],s64 offen offset:3072 sc1 lds
SA m0,s66,0x1000
s_nop 0
BL v209,s[44:47],s64 offen sc1 lds
BL v209,s[44:47],s64 offen offset:1024 sc1 lds
BL v209,s[44:47],s64 offen offset:2048 sc1 lds
BL v209,s[44:47],s64 offen offset:3072 sc1 lds
SA s63,s63,0x8000
SA s64,s64,0x8000
W vmcnt(0)
s_barrier
RD a[192:195],v210
RD a[196:199],v210 offset:4096
RD a[200:203],v210 offset:8192
RD a[204:207],v210 offset:12288
RD a[208:211],v210 offset:2048
RD a[212:215],v210 offset:6144
RD a[216:219],v210 offset:10240
RD a[220:223],v210 offset:14336
RD a[224:227],v210 offset:16384
RD a[228:231],v210 offset:20480
RD a[232:235],v210 offset:24576
RD a[236:239],v210 offset:28672
RD a[240:243],v210 offset:18432
RD a[244:247],v210 offset:22528
RD a[248:251],v210 offset:26624
RD a[252:255],v210 offset:30720
RD v[96:99],v211
RD v[100:103],v211 offset:16384
RD v[104:107],v211 offset:2048
RD v[108:111],v211 offset:18432
RD v[112:115],v211 offset:4096
RD v[116:119],v211 offset:20480
RD v[120:123],v211 offset:6144
RD v[124:127],v211 offset:22528
RD v[128:131],v211 offset:8192
RD v[132:135],v211 offset:24576
RD v[136:139],v211 offset:10240
RD v[140:143],v211 offset:26624
RD v[144:147],v211 offset:12288
RD v[148:151],v211 offset:28672
RD v[152:155],v211 offset:14336
RD v[156:159],v211 offset:30720
W lgkmcnt(0)
s_cmp_eq_u32 s62,1
s_cbranch_scc1 fk_sf1_mask
M v[0:3],a[192:195],a[128:131],0
M v[0:3],a[196:199],a[132:135],v[0:3]
M v[0:3],a[200:203],a[136:139],v[0:3]
M v[0:3],a[204:207],a[140:143],v[0:3]
M v[4:7],a[208:211],a[128:131],0
M v[4:7],a[212:215],a[132:135],v[4:7]
M v[4:7],a[216:219],a[136:139],v[4:7]
M v[4:7],a[220:223],a[140:143],v[4:7]
M v[8:11],a[224:227],a[128:131],0
M v[8:11],a[228:231],a[132:135],v[8:11]
M v[8:11],a[232:235],a[136:139],v[8:11]
M v[8:11],a[236:239],a[140:143],v[8:11]
M v[12:15],a[240:243],a[128:131],0
M v[12:15],a[244:247],a[132:135],v[12:15]
M v[12:15],a[248:251],a[136:139],v[12:15]
M v[12:15],a[252:255],a[140:143],v[12:15]
M v[16:19],a[192:195],a[144:147],0
M v[16:19],a[196:199],a[148:151],v[16:19]
M v[16:19],a[200:203],a[152:155],v[16:19]
M v[16:19],a[204:207],a[156:159],v[16:19]
M v[20:23],a[208:211],a[144:147],0
M v[20:23],a[212:215],a[148:151],v[20:23]
M v[20:23],a[216:219],a[152:155],v[20:23]
M v[20:23],a[220:223],a[156:159],v[20:23]
M v[24:27],a[224:227],a[144:147],0
M v[24:27],a[228:231],a[148:151],v[24:27]
M v[24:27],a[232:235],a[152:155],v[24:27]
M v[24:27],a[236:239],a[156:159],v[24:27]
M v[28:31],a[240:243],a[144:147],0
M v[28:31],a[244:247],a[148:151],v[28:31]
M v[28:31],a[248:251],a[152:155],v[28:31]
M v[28:31],a[252:255],a[156:159],v[28:31]
M v[32:35],a[192:195],a[160:163],0
M v[32:35],a[196:199],a[164:167],v[32:35]
M v[32:35],a[200:203],a[168:171],v[32:35]
M v[32:35],a[204:207],a[172:175],v[32:35]
M v[36:39],a[208:211],a[160:163],0
M v[36:39],a[212:215],a[164:167],v[36:39]
M v[36:39],a[216:219],a[168:171],v[36:39]
M v[36:39],a[220:223],a[172:175],v[36:39]
)~"
R"~(M v[40:43],a[224:227],a[160:163],0
M v[40:43],a[228:231],a[164:167],v[40:43]
M v[40:43],a[232:235],a[168:171],v[40:43]
M v[40:43],a[236:239],a[172:175],v[40:43]
M v[44:47],a[240:243],a[160:163],0
M v[44:47],a[244:247],a[164:167],v[44:47]
M v[44:47],a[248:251],a[168:171],v[44:47]
M v[44:47],a[252:255],a[172:175],v[44:47]
M v[48:51],a[192:195],a[176:179],0
M v[48:51],a[196:199],a[180:183],v[48:51]
M v[48:51],a[200:203],a[184:187],v[48:51]
M v[48:51],a[204:207],a[188:191],v[48:51]
M v[52:55],a[208:211],a[176:179],0
M v[52:55],a[212:215],a[180:183],v[52:55]
M v[52:55],a[216:219],a[184:187],v[52:55]
M v[52:55],a[220:223],a[188:191],v[52:55]
M v[56:59],a[224:227],a[176:179],0
M v[56:59],a[228:231],a[180:183],v[56:59]
M v[56:59],a[232:235],a[184:187],v[56:59]
M v[56:59],a[236:239],a[188:191],v[56:59]
M v[60:63],a[240:243],a[176:179],0
M v[60:63],a[244:247],a[180:183],v[60:63]
M v[60:63],a[248:251],a[184:187],v[60:63]
M v[60:63],a[252:255],a[188:191],v[60:63]
s_branch fk_sf1_qk
fk_sf1_mask:
v_mov_b32 v4,0xff800000
v_mov_b32 v5,0xff800000
v_mov_b32 v6,0xff800000
v_mov_b32 v7,0xff800000
v_mov_b32 v8,0xff800000
v_mov_b32 v9,0xff800000
v_mov_b32 v10,0xff800000
v_mov_b32 v11,0xff800000
v_mov_b32 v12,0xff800000
v_mov_b32 v13,0xff800000
v_mov_b32 v14,0xff800000
v_mov_b32 v15,0xff800000
v_mov_b32 v24,0xff800000
v_mov_b32 v25,0xff800000
v_mov_b32 v26,0xff800000
v_mov_b32 v27,0xff800000
v_mov_b32 v28,0xff800000
v_mov_b32 v29,0xff800000
v_mov_b32 v30,0xff800000
v_mov_b32 v31,0xff800000
v_mov_b32 v44,0xff800000
v_mov_b32 v45,0xff800000
v_mov_b32 v46,0xff800000
v_mov_b32 v47,0xff800000
M v[0:3],a[192:195],a[128:131],v[160:163]
M v[0:3],a[196:199],a[132:135],v[0:3]
M v[0:3],a[200:203],a[136:139],v[0:3]
M v[0:3],a[204:207],a[140:143],v[0:3]
M v[16:19],a[192:195],a[144:147],0
M v[16:19],a[196:199],a[148:151],v[16:19]
M v[16:19],a[200:203],a[152:155],v[16:19]
M v[16:19],a[204:207],a[156:159],v[16:19]
M v[20:23],a[208:211],a[144:147],v[160:163]
M v[20:23],a[212:215],a[148:151],v[20:23]
M v[20:23],a[216:219],a[152:155],v[20:23]
M v[20:23],a[220:223],a[156:159],v[20:23]
M v[32:35],a[192:195],a[160:163],0
M v[32:35],a[196:199],a[164:167],v[32:35]
M v[32:35],a[200:203],a[168:171],v[32:35]
M v[32:35],a[204:207],a[172:175],v[32:35]
M v[36:39],a[208:211],a[160:163],0
M v[36:39],a[212:215],a[164:167],v[36:39]
M v[36:39],a[216:219],a[168:171],v[36:39]
M v[36:39],a[220:223],a[172:175],v[36:39]
M v[40:43],a[224:227],a[160:163],v[160:163]
M v[40:43],a[228:231],a[164:167],v[40:43]
M v[40:43],a[232:235],a[168:171],v[40:43]
M v[40:43],a[236:239],a[172:175],v[40:43]
M v[48:51],a[192:195],a[176:179],0
M v[48:51],a[196:199],a[180:183],v[48:51]
M v[48:51],a[200:203],a[184:187],v[48:51]
M v[48:51],a[204:207],a[188:191],v[48:51]
M v[52:55],a[208:211],a[176:179],0
M v[52:55],a[212:215],a[180:183],v[52:55]
M v[52:55],a[216:219],a[184:187],v[52:55]
M v[52:55],a[220:223],a[188:191],v[52:55]
M v[56:59],a[224:227],a[176:179],0
M v[56:59],a[228:231],a[180:183],v[56:59]
M v[56:59],a[232:235],a[184:187],v[56:59]
M v[56:59],a[236:239],a[188:191],v[56:59]
M v[60:63],a[240:243],a[176:179],v[160:163]
M v[60:63],a[244:247],a[180:183],v[60:63]
M v[60:63],a[248:251],a[184:187],v[60:63]
M v[60:63],a[252:255],a[188:191],v[60:63]
fk_sf1_qk:
s_nop 7
v_max3_f32 v224,v0,v1,v2
v_max3_f32 v224,v224,v3,v4
v_max3_f32 v224,v224,v5,v6
v_max3_f32 v224,v224,v7,v8
v_max3_f32 v224,v224,v9,v10
v_max3_f32 v224,v224,v11,v12
v_max3_f32 v224,v224,v13,v14
v_max_f32 v224,v224,v15
v_max3_f32 v225,v16,v17,v18
v_max3_f32 v225,v225,v19,v20
v_max3_f32 v225,v225,v21,v22
v_max3_f32 v225,v225,v23,v24
v_max3_f32 v225,v225,v25,v26
v_max3_f32 v225,v225,v27,v28
v_max3_f32 v225,v225,v29,v30
v_max_f32 v225,v225,v31
v_max3_f32 v226,v32,v33,v34
v_max3_f32 v226,v226,v35,v36
v_max3_f32 v226,v226,v37,v38
v_max3_f32 v226,v226,v39,v40
v_max3_f32 v226,v226,v41,v42
v_max3_f32 v226,v226,v43,v44
v_max3_f32 v226,v226,v45,v46
v_max_f32 v226,v226,v47
v_max3_f32 v227,v48,v49,v50
v_max3_f32 v227,v227,v51,v52
v_max3_f32 v227,v227,v53,v54
v_max3_f32 v227,v227,v55,v56
v_max3_f32 v227,v227,v57,v58
v_max3_f32 v227,v227,v59,v60
v_max3_f32 v227,v227,v61,v62
v_max_f32 v227,v227,v63
v_mov_b32 v228,v224
v_mov_b32 v229,v225
v_mov_b32 v230,v226
v_mov_b32 v231,v227
s_nop 1
v_permlane16_swap_b32 v224,v228
v_permlane16_swap_b32 v225,v229
v_permlane16_swap_b32 v226,v230
v_permlane16_swap_b32 v227,v231
v_max_f32 v224,v224,v228
v_max_f32 v225,v225,v229
v_max_f32 v226,v226,v230
v_max_f32 v227,v227,v231
v_mov_b32 v228,v224
v_mov_b32 v229,v225
v_mov_b32 v230,v226
v_mov_b32 v231,v227
s_nop 1
v_permlane32_swap_b32 v224,v228
v_permlane32_swap_b32 v225,v229
v_permlane32_swap_b32 v226,v230
v_permlane32_swap_b32 v227,v231
v_max_f32 v224,v224,v228
v_max_f32 v225,v225,v229
v_max_f32 v226,v226,v230
v_max_f32 v227,v227,v231
v_max_f32 v224,v224,v218
v_sub_f32 v228,v218,v224
v_mov_b32 v218,v224
v_max_f32 v225,v225,v219
v_sub_f32 v229,v219,v225
v_mov_b32 v219,v225
v_max_f32 v226,v226,v220
v_sub_f32 v230,v220,v226
v_mov_b32 v220,v226
v_max_f32 v227,v227,v221
v_sub_f32 v231,v221,v227
v_mov_b32 v221,v227
EX v228,v228
EX v229,v229
EX v230,v230
EX v231,v231
v_sub_f32 v0,v0,v218
v_sub_f32 v1,v1,v218
v_sub_f32 v2,v2,v218
v_sub_f32 v3,v3,v218
v_sub_f32 v4,v4,v218
v_sub_f32 v5,v5,v218
v_sub_f32 v6,v6,v218
v_sub_f32 v7,v7,v218
v_sub_f32 v8,v8,v218
v_sub_f32 v9,v9,v218
v_sub_f32 v10,v10,v218
v_sub_f32 v11,v11,v218
v_sub_f32 v12,v12,v218
v_sub_f32 v13,v13,v218
v_sub_f32 v14,v14,v218
v_sub_f32 v15,v15,v218
v_sub_f32 v16,v16,v219
v_sub_f32 v17,v17,v219
v_sub_f32 v18,v18,v219
v_sub_f32 v19,v19,v219
v_sub_f32 v20,v20,v219
v_sub_f32 v21,v21,v219
v_sub_f32 v22,v22,v219
v_sub_f32 v23,v23,v219
v_sub_f32 v24,v24,v219
v_sub_f32 v25,v25,v219
v_sub_f32 v26,v26,v219
v_sub_f32 v27,v27,v219
v_sub_f32 v28,v28,v219
v_sub_f32 v29,v29,v219
v_sub_f32 v30,v30,v219
v_sub_f32 v31,v31,v219
v_sub_f32 v32,v32,v220
v_sub_f32 v33,v33,v220
v_sub_f32 v34,v34,v220
v_sub_f32 v35,v35,v220
v_sub_f32 v36,v36,v220
v_sub_f32 v37,v37,v220
v_sub_f32 v38,v38,v220
v_sub_f32 v39,v39,v220
v_sub_f32 v40,v40,v220
v_sub_f32 v41,v41,v220
v_sub_f32 v42,v42,v220
v_sub_f32 v43,v43,v220
v_sub_f32 v44,v44,v220
v_sub_f32 v45,v45,v220
v_sub_f32 v46,v46,v220
v_sub_f32 v47,v47,v220
v_sub_f32 v48,v48,v221
v_sub_f32 v49,v49,v221
v_sub_f32 v50,v50,v221
v_sub_f32 v51,v51,v221
v_sub_f32 v52,v52,v221
v_sub_f32 v53,v53,v221
v_sub_f32 v54,v54,v221
v_sub_f32 v55,v55,v221
v_sub_f32 v56,v56,v221
v_sub_f32 v57,v57,v221
v_sub_f32 v58,v58,v221
v_sub_f32 v59,v59,v221
v_sub_f32 v60,v60,v221
v_sub_f32 v61,v61,v221
v_sub_f32 v62,v62,v221
v_sub_f32 v63,v63,v221
EX v0,v0
EX v1,v1
EX v2,v2
EX v3,v3
EX v4,v4
EX v5,v5
EX v6,v6
EX v7,v7
EX v8,v8
EX v9,v9
EX v10,v10
EX v11,v11
EX v12,v12
EX v13,v13
EX v14,v14
EX v15,v15
EX v16,v16
EX v17,v17
EX v18,v18
EX v19,v19
EX v20,v20
EX v21,v21
EX v22,v22
EX v23,v23
EX v24,v24
EX v25,v25
EX v26,v26
EX v27,v27
EX v28,v28
EX v29,v29
EX v30,v30
EX v31,v31
EX v32,v32
EX v33,v33
EX v34,v34
EX v35,v35
EX v36,v36
EX v37,v37
EX v38,v38
EX v39,v39
EX v40,v40
EX v41,v41
EX v42,v42
EX v43,v43
EX v44,v44
EX v45,v45
EX v46,v46
EX v47,v47
EX v48,v48
EX v49,v49
EX v50,v50
EX v51,v51
EX v52,v52
EX v53,v53
EX v54,v54
EX v55,v55
EX v56,v56
EX v57,v57
EX v58,v58
EX v59,v59
EX v60,v60
EX v61,v61
EX v62,v62
EX v63,v63
s_nop 0
CV v64,v0,v1
CV v65,v2,v3
CV v66,v4,v5
CV v67,v6,v7
CV v68,v8,v9
CV v69,v10,v11
CV v70,v12,v13
CV v71,v14,v15
CV v72,v16,v17
CV v73,v18,v19
CV v74,v20,v21
CV v75,v22,v23
CV v76,v24,v25
CV v77,v26,v27
CV v78,v28,v29
CV v79,v30,v31
CV v80,v32,v33
CV v81,v34,v35
CV v82,v36,v37
CV v83,v38,v39
CV v84,v40,v41
CV v85,v42,v43
CV v86,v44,v45
CV v87,v46,v47
CV v88,v48,v49
CV v89,v50,v51
CV v90,v52,v53
CV v91,v54,v55
CV v92,v56,v57
CV v93,v58,v59
CV v94,v60,v61
CV v95,v62,v63
AR v232,a0
AR v233,a1
AR v234,a2
AR v235,a3
AR v236,a4
AR v237,a5
AR v238,a6
AR v239,a7
v_mul_f32 v232,v232,v228
v_mul_f32 v233,v233,v228
v_mul_f32 v234,v234,v228
v_mul_f32 v235,v235,v228
v_mul_f32 v236,v236,v228
v_mul_f32 v237,v237,v228
v_mul_f32 v238,v238,v228
v_mul_f32 v239,v239,v228
AW a0,v232
AW a1,v233
AW a2,v234
AW a3,v235
AW a4,v236
AW a5,v237
AW a6,v238
AW a7,v239
AR v232,a8
AR v233,a9
AR v234,a10
AR v235,a11
AR v236,a12
AR v237,a13
AR v238,a14
AR v239,a15
v_mul_f32 v232,v232,v228
v_mul_f32 v233,v233,v228
v_mul_f32 v234,v234,v228
v_mul_f32 v235,v235,v228
v_mul_f32 v236,v236,v228
v_mul_f32 v237,v237,v228
v_mul_f32 v238,v238,v228
v_mul_f32 v239,v239,v228
AW a8,v232
AW a9,v233
AW a10,v234
AW a11,v235
AW a12,v236
AW a13,v237
AW a14,v238
AW a15,v239
AR v232,a16
AR v233,a17
AR v234,a18
AR v235,a19
AR v236,a20
AR v237,a21
AR v238,a22
AR v239,a23
v_mul_f32 v232,v232,v228
v_mul_f32 v233,v233,v228
v_mul_f32 v234,v234,v228
v_mul_f32 v235,v235,v228
v_mul_f32 v236,v236,v228
v_mul_f32 v237,v237,v228
v_mul_f32 v238,v238,v228
v_mul_f32 v239,v239,v228
AW a16,v232
AW a17,v233
AW a18,v234
AW a19,v235
AW a20,v236
AW a21,v237
AW a22,v238
AW a23,v239
AR v232,a24
AR v233,a25
AR v234,a26
AR v235,a27
AR v236,a28
AR v237,a29
AR v238,a30
AR v239,a31
v_mul_f32 v232,v232,v228
v_mul_f32 v233,v233,v228
v_mul_f32 v234,v234,v228
v_mul_f32 v235,v235,v228
v_mul_f32 v236,v236,v228
v_mul_f32 v237,v237,v228
v_mul_f32 v238,v238,v228
v_mul_f32 v239,v239,v228
AW a24,v232
AW a25,v233
AW a26,v234
AW a27,v235
AW a28,v236
AW a29,v237
AW a30,v238
AW a31,v239
v_mul_f32 v188,v188,v228
v_mul_f32 v189,v189,v228
v_mul_f32 v190,v190,v228
v_mul_f32 v191,v191,v228
AR v232,a32
AR v233,a33
AR v234,a34
AR v235,a35
AR v236,a36
AR v237,a37
AR v238,a38
AR v239,a39
v_mul_f32 v232,v232,v229
v_mul_f32 v233,v233,v229
v_mul_f32 v234,v234,v229
v_mul_f32 v235,v235,v229
v_mul_f32 v236,v236,v229
v_mul_f32 v237,v237,v229
v_mul_f32 v238,v238,v229
v_mul_f32 v239,v239,v229
AW a32,v232
AW a33,v233
AW a34,v234
AW a35,v235
AW a36,v236
AW a37,v237
AW a38,v238
AW a39,v239
AR v232,a40
AR v233,a41
AR v234,a42
AR v235,a43
AR v236,a44
AR v237,a45
AR v238,a46
AR v239,a47
v_mul_f32 v232,v232,v229
v_mul_f32 v233,v233,v229
v_mul_f32 v234,v234,v229
v_mul_f32 v235,v235,v229
v_mul_f32 v236,v236,v229
v_mul_f32 v237,v237,v229
v_mul_f32 v238,v238,v229
v_mul_f32 v239,v239,v229
AW a40,v232
AW a41,v233
AW a42,v234
AW a43,v235
AW a44,v236
AW a45,v237
AW a46,v238
AW a47,v239
AR v232,a48
AR v233,a49
AR v234,a50
AR v235,a51
AR v236,a52
AR v237,a53
AR v238,a54
AR v239,a55
v_mul_f32 v232,v232,v229
v_mul_f32 v233,v233,v229
v_mul_f32 v234,v234,v229
v_mul_f32 v235,v235,v229
v_mul_f32 v236,v236,v229
v_mul_f32 v237,v237,v229
v_mul_f32 v238,v238,v229
v_mul_f32 v239,v239,v229
AW a48,v232
AW a49,v233
AW a50,v234
AW a51,v235
AW a52,v236
AW a53,v237
AW a54,v238
AW a55,v239
AR v232,a56
AR v233,a57
AR v234,a58
AR v235,a59
AR v236,a60
AR v237,a61
AR v238,a62
AR v239,a63
v_mul_f32 v232,v232,v229
v_mul_f32 v233,v233,v229
v_mul_f32 v234,v234,v229
v_mul_f32 v235,v235,v229
v_mul_f32 v236,v236,v229
v_mul_f32 v237,v237,v229
v_mul_f32 v238,v238,v229
v_mul_f32 v239,v239,v229
AW a56,v232
AW a57,v233
AW a58,v234
AW a59,v235
AW a60,v236
AW a61,v237
AW a62,v238
AW a63,v239
v_mul_f32 v192,v192,v229
v_mul_f32 v193,v193,v229
v_mul_f32 v194,v194,v229
v_mul_f32 v195,v195,v229
AR v232,a64
AR v233,a65
AR v234,a66
AR v235,a67
AR v236,a68
AR v237,a69
AR v238,a70
AR v239,a71
v_mul_f32 v232,v232,v230
v_mul_f32 v233,v233,v230
v_mul_f32 v234,v234,v230
v_mul_f32 v235,v235,v230
v_mul_f32 v236,v236,v230
v_mul_f32 v237,v237,v230
v_mul_f32 v238,v238,v230
v_mul_f32 v239,v239,v230
AW a64,v232
AW a65,v233
AW a66,v234
AW a67,v235
AW a68,v236
AW a69,v237
AW a70,v238
AW a71,v239
AR v232,a72
AR v233,a73
AR v234,a74
AR v235,a75
AR v236,a76
AR v237,a77
AR v238,a78
AR v239,a79
v_mul_f32 v232,v232,v230
v_mul_f32 v233,v233,v230
v_mul_f32 v234,v234,v230
v_mul_f32 v235,v235,v230
v_mul_f32 v236,v236,v230
v_mul_f32 v237,v237,v230
v_mul_f32 v238,v238,v230
v_mul_f32 v239,v239,v230
AW a72,v232
AW a73,v233
AW a74,v234
AW a75,v235
AW a76,v236
AW a77,v237
AW a78,v238
AW a79,v239
AR v232,a80
AR v233,a81
AR v234,a82
AR v235,a83
AR v236,a84
AR v237,a85
AR v238,a86
AR v239,a87
v_mul_f32 v232,v232,v230
v_mul_f32 v233,v233,v230
v_mul_f32 v234,v234,v230
v_mul_f32 v235,v235,v230
v_mul_f32 v236,v236,v230
v_mul_f32 v237,v237,v230
v_mul_f32 v238,v238,v230
v_mul_f32 v239,v239,v230
AW a80,v232
AW a81,v233
AW a82,v234
AW a83,v235
AW a84,v236
AW a85,v237
AW a86,v238
AW a87,v239
AR v232,a88
AR v233,a89
AR v234,a90
AR v235,a91
AR v236,a92
AR v237,a93
AR v238,a94
AR v239,a95
v_mul_f32 v232,v232,v230
v_mul_f32 v233,v233,v230
v_mul_f32 v234,v234,v230
v_mul_f32 v235,v235,v230
v_mul_f32 v236,v236,v230
v_mul_f32 v237,v237,v230
v_mul_f32 v238,v238,v230
v_mul_f32 v239,v239,v230
AW a88,v232
AW a89,v233
AW a90,v234
AW a91,v235
AW a92,v236
AW a93,v237
AW a94,v238
AW a95,v239
v_mul_f32 v196,v196,v230
v_mul_f32 v197,v197,v230
v_mul_f32 v198,v198,v230
v_mul_f32 v199,v199,v230
AR v232,a96
AR v233,a97
AR v234,a98
AR v235,a99
AR v236,a100
AR v237,a101
AR v238,a102
AR v239,a103
v_mul_f32 v232,v232,v231
v_mul_f32 v233,v233,v231
v_mul_f32 v234,v234,v231
v_mul_f32 v235,v235,v231
v_mul_f32 v236,v236,v231
v_mul_f32 v237,v237,v231
v_mul_f32 v238,v238,v231
v_mul_f32 v239,v239,v231
AW a96,v232
AW a97,v233
AW a98,v234
AW a99,v235
AW a100,v236
AW a101,v237
AW a102,v238
AW a103,v239
AR v232,a104
AR v233,a105
AR v234,a106
AR v235,a107
AR v236,a108
AR v237,a109
AR v238,a110
AR v239,a111
v_mul_f32 v232,v232,v231
v_mul_f32 v233,v233,v231
v_mul_f32 v234,v234,v231
v_mul_f32 v235,v235,v231
v_mul_f32 v236,v236,v231
v_mul_f32 v237,v237,v231
v_mul_f32 v238,v238,v231
v_mul_f32 v239,v239,v231
AW a104,v232
AW a105,v233
AW a106,v234
AW a107,v235
AW a108,v236
AW a109,v237
AW a110,v238
AW a111,v239
AR v232,a112
AR v233,a113
AR v234,a114
AR v235,a115
AR v236,a116
AR v237,a117
AR v238,a118
AR v239,a119
v_mul_f32 v232,v232,v231
v_mul_f32 v233,v233,v231
v_mul_f32 v234,v234,v231
v_mul_f32 v235,v235,v231
v_mul_f32 v236,v236,v231
v_mul_f32 v237,v237,v231
v_mul_f32 v238,v238,v231
v_mul_f32 v239,v239,v231
AW a112,v232
AW a113,v233
AW a114,v234
AW a115,v235
AW a116,v236
AW a117,v237
AW a118,v238
AW a119,v239
AR v232,a120
AR v233,a121
AR v234,a122
AR v235,a123
AR v236,a124
AR v237,a125
AR v238,a126
)~"
R"~(AR v239,a127
v_mul_f32 v232,v232,v231
v_mul_f32 v233,v233,v231
v_mul_f32 v234,v234,v231
v_mul_f32 v235,v235,v231
v_mul_f32 v236,v236,v231
v_mul_f32 v237,v237,v231
v_mul_f32 v238,v238,v231
v_mul_f32 v239,v239,v231
AW a120,v232
AW a121,v233
AW a122,v234
AW a123,v235
AW a124,v236
AW a125,v237
AW a126,v238
AW a127,v239
v_mul_f32 v200,v200,v231
v_mul_f32 v201,v201,v231
v_mul_f32 v202,v202,v231
v_mul_f32 v203,v203,v231
s_nop 1
M a[0:3],v[96:99],v[64:67],a[0:3]
M a[0:3],v[100:103],v[68:71],a[0:3]
M a[4:7],v[104:107],v[64:67],a[4:7]
M a[4:7],v[108:111],v[68:71],a[4:7]
M a[8:11],v[112:115],v[64:67],a[8:11]
M a[8:11],v[116:119],v[68:71],a[8:11]
M a[12:15],v[120:123],v[64:67],a[12:15]
M a[12:15],v[124:127],v[68:71],a[12:15]
M a[16:19],v[128:131],v[64:67],a[16:19]
M a[16:19],v[132:135],v[68:71],a[16:19]
M a[20:23],v[136:139],v[64:67],a[20:23]
M a[20:23],v[140:143],v[68:71],a[20:23]
M a[24:27],v[144:147],v[64:67],a[24:27]
M a[24:27],v[148:151],v[68:71],a[24:27]
M a[28:31],v[152:155],v[64:67],a[28:31]
M a[28:31],v[156:159],v[68:71],a[28:31]
M v[188:191],v[204:207],v[64:67],v[188:191]
M v[188:191],v[204:207],v[68:71],v[188:191]
M a[32:35],v[96:99],v[72:75],a[32:35]
M a[32:35],v[100:103],v[76:79],a[32:35]
M a[36:39],v[104:107],v[72:75],a[36:39]
M a[36:39],v[108:111],v[76:79],a[36:39]
M a[40:43],v[112:115],v[72:75],a[40:43]
M a[40:43],v[116:119],v[76:79],a[40:43]
M a[44:47],v[120:123],v[72:75],a[44:47]
M a[44:47],v[124:127],v[76:79],a[44:47]
M a[48:51],v[128:131],v[72:75],a[48:51]
M a[48:51],v[132:135],v[76:79],a[48:51]
M a[52:55],v[136:139],v[72:75],a[52:55]
M a[52:55],v[140:143],v[76:79],a[52:55]
M a[56:59],v[144:147],v[72:75],a[56:59]
M a[56:59],v[148:151],v[76:79],a[56:59]
M a[60:63],v[152:155],v[72:75],a[60:63]
M a[60:63],v[156:159],v[76:79],a[60:63]
M v[192:195],v[204:207],v[72:75],v[192:195]
M v[192:195],v[204:207],v[76:79],v[192:195]
M a[64:67],v[96:99],v[80:83],a[64:67]
M a[64:67],v[100:103],v[84:87],a[64:67]
M a[68:71],v[104:107],v[80:83],a[68:71]
M a[68:71],v[108:111],v[84:87],a[68:71]
M a[72:75],v[112:115],v[80:83],a[72:75]
M a[72:75],v[116:119],v[84:87],a[72:75]
M a[76:79],v[120:123],v[80:83],a[76:79]
M a[76:79],v[124:127],v[84:87],a[76:79]
M a[80:83],v[128:131],v[80:83],a[80:83]
M a[80:83],v[132:135],v[84:87],a[80:83]
M a[84:87],v[136:139],v[80:83],a[84:87]
M a[84:87],v[140:143],v[84:87],a[84:87]
M a[88:91],v[144:147],v[80:83],a[88:91]
M a[88:91],v[148:151],v[84:87],a[88:91]
M a[92:95],v[152:155],v[80:83],a[92:95]
M a[92:95],v[156:159],v[84:87],a[92:95]
M v[196:199],v[204:207],v[80:83],v[196:199]
M v[196:199],v[204:207],v[84:87],v[196:199]
M a[96:99],v[96:99],v[88:91],a[96:99]
M a[96:99],v[100:103],v[92:95],a[96:99]
M a[100:103],v[104:107],v[88:91],a[100:103]
M a[100:103],v[108:111],v[92:95],a[100:103]
M a[104:107],v[112:115],v[88:91],a[104:107]
M a[104:107],v[116:119],v[92:95],a[104:107]
M a[108:111],v[120:123],v[88:91],a[108:111]
M a[108:111],v[124:127],v[92:95],a[108:111]
M a[112:115],v[128:131],v[88:91],a[112:115]
M a[112:115],v[132:135],v[92:95],a[112:115]
M a[116:119],v[136:139],v[88:91],a[116:119]
M a[116:119],v[140:143],v[92:95],a[116:119]
M a[120:123],v[144:147],v[88:91],a[120:123]
M a[120:123],v[148:151],v[92:95],a[120:123]
M a[124:127],v[152:155],v[88:91],a[124:127]
M a[124:127],v[156:159],v[92:95],a[124:127]
M v[200:203],v[204:207],v[88:91],v[200:203]
M v[200:203],v[204:207],v[92:95],v[200:203]
s_nop 7
s_sub_u32 s62,s62,1
s_cmp_lg_u32 s62,0
s_cbranch_scc1 fk_sf1_loop
s_and_b32 s88,s65,0x8000
s_lshl_b32 s89,s36,10
SA s89,s89,s88
v_add_u32 v226,s89,v254
s_lshl_b32 s89,s40,11
SA s90,s89,s88
v_add_u32 v227,s90,v254
v_xor_b32 v217,0x800,v227
W lgkmcnt(0)
s_barrier
v_mov_b32 v228,v218
v_mov_b32 v229,v219
v_mov_b32 v230,v220
v_mov_b32 v231,v221
WR v226,v[228:231]
v_xor_b32 v227,0x400,v226
W lgkmcnt(0)
s_barrier
RD v[228:231],v227
W lgkmcnt(0)
v_max_f32 v228,v228,v218
v_max_f32 v229,v229,v219
v_max_f32 v230,v230,v220
v_max_f32 v231,v231,v221
v_sub_f32 v228,v218,v228
v_sub_f32 v229,v219,v229
v_sub_f32 v230,v220,v230
v_sub_f32 v231,v221,v231
EX v228,v228
EX v229,v229
EX v230,v230
EX v231,v231
s_nop 0
AR v232,a0
AR v233,a1
AR v234,a2
AR v235,a3
AR v236,a4
AR v237,a5
AR v238,a6
AR v239,a7
v_mul_f32 v232,v232,v228
v_mul_f32 v233,v233,v228
v_mul_f32 v234,v234,v228
v_mul_f32 v235,v235,v228
v_mul_f32 v236,v236,v228
v_mul_f32 v237,v237,v228
v_mul_f32 v238,v238,v228
v_mul_f32 v239,v239,v228
AW a0,v232
AW a1,v233
AW a2,v234
AW a3,v235
AW a4,v236
AW a5,v237
AW a6,v238
AW a7,v239
AR v232,a8
AR v233,a9
AR v234,a10
AR v235,a11
AR v236,a12
AR v237,a13
AR v238,a14
AR v239,a15
v_mul_f32 v232,v232,v228
v_mul_f32 v233,v233,v228
v_mul_f32 v234,v234,v228
v_mul_f32 v235,v235,v228
v_mul_f32 v236,v236,v228
v_mul_f32 v237,v237,v228
v_mul_f32 v238,v238,v228
v_mul_f32 v239,v239,v228
AW a8,v232
AW a9,v233
AW a10,v234
AW a11,v235
AW a12,v236
AW a13,v237
AW a14,v238
AW a15,v239
AR v232,a16
AR v233,a17
AR v234,a18
AR v235,a19
AR v236,a20
AR v237,a21
AR v238,a22
AR v239,a23
v_mul_f32 v232,v232,v228
v_mul_f32 v233,v233,v228
v_mul_f32 v234,v234,v228
v_mul_f32 v235,v235,v228
v_mul_f32 v236,v236,v228
v_mul_f32 v237,v237,v228
v_mul_f32 v238,v238,v228
v_mul_f32 v239,v239,v228
AW a16,v232
AW a17,v233
AW a18,v234
AW a19,v235
AW a20,v236
AW a21,v237
AW a22,v238
AW a23,v239
AR v232,a24
AR v233,a25
AR v234,a26
AR v235,a27
AR v236,a28
AR v237,a29
AR v238,a30
AR v239,a31
v_mul_f32 v232,v232,v228
v_mul_f32 v233,v233,v228
v_mul_f32 v234,v234,v228
v_mul_f32 v235,v235,v228
v_mul_f32 v236,v236,v228
v_mul_f32 v237,v237,v228
v_mul_f32 v238,v238,v228
v_mul_f32 v239,v239,v228
AW a24,v232
AW a25,v233
AW a26,v234
AW a27,v235
AW a28,v236
AW a29,v237
AW a30,v238
AW a31,v239
v_mul_f32 v188,v188,v228
v_mul_f32 v189,v189,v228
v_mul_f32 v190,v190,v228
v_mul_f32 v191,v191,v228
AR v232,a32
AR v233,a33
AR v234,a34
AR v235,a35
AR v236,a36
AR v237,a37
AR v238,a38
AR v239,a39
v_mul_f32 v232,v232,v229
v_mul_f32 v233,v233,v229
v_mul_f32 v234,v234,v229
v_mul_f32 v235,v235,v229
v_mul_f32 v236,v236,v229
v_mul_f32 v237,v237,v229
v_mul_f32 v238,v238,v229
v_mul_f32 v239,v239,v229
AW a32,v232
AW a33,v233
AW a34,v234
AW a35,v235
AW a36,v236
AW a37,v237
AW a38,v238
AW a39,v239
AR v232,a40
AR v233,a41
AR v234,a42
AR v235,a43
AR v236,a44
AR v237,a45
AR v238,a46
AR v239,a47
v_mul_f32 v232,v232,v229
v_mul_f32 v233,v233,v229
v_mul_f32 v234,v234,v229
v_mul_f32 v235,v235,v229
v_mul_f32 v236,v236,v229
v_mul_f32 v237,v237,v229
v_mul_f32 v238,v238,v229
v_mul_f32 v239,v239,v229
AW a40,v232
AW a41,v233
AW a42,v234
AW a43,v235
AW a44,v236
AW a45,v237
AW a46,v238
AW a47,v239
AR v232,a48
AR v233,a49
AR v234,a50
AR v235,a51
AR v236,a52
AR v237,a53
AR v238,a54
AR v239,a55
v_mul_f32 v232,v232,v229
v_mul_f32 v233,v233,v229
v_mul_f32 v234,v234,v229
v_mul_f32 v235,v235,v229
v_mul_f32 v236,v236,v229
v_mul_f32 v237,v237,v229
v_mul_f32 v238,v238,v229
v_mul_f32 v239,v239,v229
AW a48,v232
AW a49,v233
AW a50,v234
AW a51,v235
AW a52,v236
AW a53,v237
AW a54,v238
AW a55,v239
AR v232,a56
AR v233,a57
AR v234,a58
AR v235,a59
AR v236,a60
AR v237,a61
AR v238,a62
AR v239,a63
v_mul_f32 v232,v232,v229
v_mul_f32 v233,v233,v229
v_mul_f32 v234,v234,v229
v_mul_f32 v235,v235,v229
v_mul_f32 v236,v236,v229
v_mul_f32 v237,v237,v229
v_mul_f32 v238,v238,v229
v_mul_f32 v239,v239,v229
AW a56,v232
AW a57,v233
AW a58,v234
AW a59,v235
AW a60,v236
AW a61,v237
AW a62,v238
AW a63,v239
v_mul_f32 v192,v192,v229
v_mul_f32 v193,v193,v229
v_mul_f32 v194,v194,v229
v_mul_f32 v195,v195,v229
AR v232,a64
AR v233,a65
AR v234,a66
AR v235,a67
AR v236,a68
AR v237,a69
AR v238,a70
AR v239,a71
v_mul_f32 v232,v232,v230
v_mul_f32 v233,v233,v230
v_mul_f32 v234,v234,v230
v_mul_f32 v235,v235,v230
v_mul_f32 v236,v236,v230
v_mul_f32 v237,v237,v230
v_mul_f32 v238,v238,v230
v_mul_f32 v239,v239,v230
AW a64,v232
AW a65,v233
AW a66,v234
AW a67,v235
AW a68,v236
AW a69,v237
AW a70,v238
AW a71,v239
AR v232,a72
AR v233,a73
AR v234,a74
AR v235,a75
AR v236,a76
AR v237,a77
AR v238,a78
AR v239,a79
v_mul_f32 v232,v232,v230
v_mul_f32 v233,v233,v230
v_mul_f32 v234,v234,v230
v_mul_f32 v235,v235,v230
v_mul_f32 v236,v236,v230
v_mul_f32 v237,v237,v230
v_mul_f32 v238,v238,v230
v_mul_f32 v239,v239,v230
AW a72,v232
AW a73,v233
AW a74,v234
AW a75,v235
AW a76,v236
AW a77,v237
AW a78,v238
AW a79,v239
AR v232,a80
AR v233,a81
AR v234,a82
AR v235,a83
AR v236,a84
AR v237,a85
AR v238,a86
AR v239,a87
v_mul_f32 v232,v232,v230
v_mul_f32 v233,v233,v230
v_mul_f32 v234,v234,v230
v_mul_f32 v235,v235,v230
v_mul_f32 v236,v236,v230
v_mul_f32 v237,v237,v230
v_mul_f32 v238,v238,v230
v_mul_f32 v239,v239,v230
AW a80,v232
AW a81,v233
AW a82,v234
AW a83,v235
AW a84,v236
AW a85,v237
AW a86,v238
AW a87,v239
AR v232,a88
AR v233,a89
AR v234,a90
AR v235,a91
AR v236,a92
AR v237,a93
AR v238,a94
AR v239,a95
v_mul_f32 v232,v232,v230
v_mul_f32 v233,v233,v230
v_mul_f32 v234,v234,v230
v_mul_f32 v235,v235,v230
v_mul_f32 v236,v236,v230
v_mul_f32 v237,v237,v230
v_mul_f32 v238,v238,v230
v_mul_f32 v239,v239,v230
AW a88,v232
AW a89,v233
AW a90,v234
AW a91,v235
AW a92,v236
AW a93,v237
AW a94,v238
AW a95,v239
v_mul_f32 v196,v196,v230
v_mul_f32 v197,v197,v230
v_mul_f32 v198,v198,v230
v_mul_f32 v199,v199,v230
AR v232,a96
AR v233,a97
AR v234,a98
AR v235,a99
AR v236,a100
AR v237,a101
AR v238,a102
AR v239,a103
v_mul_f32 v232,v232,v231
v_mul_f32 v233,v233,v231
v_mul_f32 v234,v234,v231
v_mul_f32 v235,v235,v231
v_mul_f32 v236,v236,v231
v_mul_f32 v237,v237,v231
v_mul_f32 v238,v238,v231
v_mul_f32 v239,v239,v231
AW a96,v232
AW a97,v233
AW a98,v234
AW a99,v235
AW a100,v236
AW a101,v237
AW a102,v238
AW a103,v239
AR v232,a104
AR v233,a105
AR v234,a106
AR v235,a107
AR v236,a108
AR v237,a109
AR v238,a110
AR v239,a111
v_mul_f32 v232,v232,v231
v_mul_f32 v233,v233,v231
v_mul_f32 v234,v234,v231
v_mul_f32 v235,v235,v231
v_mul_f32 v236,v236,v231
v_mul_f32 v237,v237,v231
v_mul_f32 v238,v238,v231
v_mul_f32 v239,v239,v231
AW a104,v232
AW a105,v233
AW a106,v234
AW a107,v235
AW a108,v236
AW a109,v237
AW a110,v238
AW a111,v239
AR v232,a112
AR v233,a113
AR v234,a114
AR v235,a115
AR v236,a116
AR v237,a117
AR v238,a118
AR v239,a119
v_mul_f32 v232,v232,v231
v_mul_f32 v233,v233,v231
v_mul_f32 v234,v234,v231
v_mul_f32 v235,v235,v231
v_mul_f32 v236,v236,v231
v_mul_f32 v237,v237,v231
v_mul_f32 v238,v238,v231
v_mul_f32 v239,v239,v231
AW a112,v232
AW a113,v233
AW a114,v234
AW a115,v235
AW a116,v236
AW a117,v237
AW a118,v238
AW a119,v239
AR v232,a120
AR v233,a121
AR v234,a122
AR v235,a123
AR v236,a124
AR v237,a125
AR v238,a126
AR v239,a127
v_mul_f32 v232,v232,v231
v_mul_f32 v233,v233,v231
v_mul_f32 v234,v234,v231
v_mul_f32 v235,v235,v231
v_mul_f32 v236,v236,v231
v_mul_f32 v237,v237,v231
v_mul_f32 v238,v238,v231
v_mul_f32 v239,v239,v231
AW a120,v232
AW a121,v233
AW a122,v234
AW a123,v235
AW a124,v236
AW a125,v237
AW a126,v238
AW a127,v239
v_mul_f32 v200,v200,v231
v_mul_f32 v201,v201,v231
v_mul_f32 v202,v202,v231
v_mul_f32 v203,v203,v231
s_mov_b32 s76,3
W lgkmcnt(0)
s_barrier
s_branch fk_EPI
fk_attn_end:
s_cmp_lg_u32 s56,0
s_cbranch_scc1 fk_redo
fk_attn_done:
s_and_b32 s84,s37,7
s_and_b32 s60,s84,3
s_lshr_b32 s84,s84,2
s_lshl_b32 s84,s84,3
s_lshr_b32 s85,s37,3
s_and_b32 s86,s85,7
SA s61,s84,s86
s_lshr_b32 s62,s37,6
s_lshl_b32 s68,s61,2
SA s68,s68,s62
s_mov_b64 s[48:49],s[22:23]
s_and_b32 s49,s49,0xffff
s_mov_b32 s50,0x400000
s_mov_b32 s51,0x20000
s_mov_b64 s[84:85],s[22:23]
s_mov_b64 s[86:87],s[12:13]
s_cmp_eq_u32 s60,3
s_cselect_b32 s84,s86,s84
s_cselect_b32 s85,s87,s85
s_cselect_b32 s88,0x80000,s50
s_mov_b64 s[52:53],s[84:85]
s_and_b32 s53,s53,0xffff
s_mov_b32 s54,s88
s_mov_b32 s55,0x20000
s_mul_i32 s64,s60,0x120000
s_lshl_b32 s84,s62,8
s_lshl_b32 s85,s36,13
SA s84,s84,s85
SA s64,s64,s84
SA s65,s64,0xa0000
s_cmp_eq_u32 s60,3
s_cselect_b32 s65,s84,s65
s_lshl_b32 s67,s36,11
SA s67,s67,0x4000
s_lshl_b32 s85,s41,2
SA s85,s85,s40
s_lshl_b32 s66,s85,11
s_lshl_b32 s84,s61,3
SA s84,s84,s85
s_mul_i32 s63,s84,0x24000
s_mul_i32 s84,s60,0x9000
SA s63,s63,s84
SA s63,s63,0x2000000
v_add_u32 v200,0,v254
v_add_u32 v201,0x48000,v254
v_bfe_u32 v224,v255,2,3
v_lshrrev_b32 v225,2,v224
v_lshlrev_b32 v225,1,v225
v_and_b32 v226,3,v255
v_xor_b32 v225,v225,v226
v_lshlrev_b32 v224,10,v224
v_lshl_add_u32 v224,v225,4,v224
v_lshrrev_b32 v225,5,v255
v_lshl_add_u32 v202,v225,6,v224
v_add_u32 v203,0x80,v202
s_sub_u32 s93,15,s39
s_lshl_b32 s93,s93,5
SA s93,s93,s38
s_lshl_b32 s93,s93,2
SA s93,s93,s36
s_lshl_b32 s93,s93,6
SA s93,s93,0x3520000
s_mov_b32 s94,s39
s_lshl_b32 s94,s94,5
SA s94,s94,s38
s_lshl_b32 s94,s94,2
SA s94,s94,s36
s_lshl_b32 s94,s94,6
SA s94,s94,0x3520000
v_and_b32 v224,15,v255
s_mul_i32 s87,s60,9
s_cmp_eq_u32 s60,3
s_cselect_b32 s88,4,8
v_min_u32 v225,s88,v224
v_add_u32 v225,s87,v225
s_lshl_b32 s86,s61,5
v_add_u32 v225,s86,v225
v_lshlrev_b32 v225,2,v225
v_add_u32 v225,s36,v225
v_lshlrev_b32 v225,6,v225
v_add_u32 v225,0x3520000,v225
s_lshl_b32 s86,s61,4
v_and_b32 v226,15,v255
v_add_u32 v226,s86,v226
v_lshlrev_b32 v226,6,v226
v_add_u32 v226,0x3508000,v226
v_and_b32 v227,16,v255
v_cmp_eq_u32 vcc,0,v227
s_nop 0
s_nop 0
v_cndmask_b32 v216,v226,v225,vcc
s_lshl_b32 s84,s40,13
v_add_u32 v204,s84,v254
v_lshrrev_b32 v224,5,v255
v_lshlrev_b32 v224,11,v224
v_bfe_u32 v225,v255,4,1
v_lshl_add_u32 v224,v225,8,v224
v_bfe_u32 v226,v255,2,2
v_lshl_add_u32 v224,v226,6,v224
v_and_b32 v226,3,v255
v_lshl_add_u32 v224,v226,3,v224
s_lshl_b32 s84,s41,10
SA s84,s84,0x4000
v_add_u32 v224,s84,v224
v_lshl_add_u32 v192,v225,5,v224
v_xor_b32 v225,1,v225
v_lshl_add_u32 v193,v225,5,v224
v_add_u32 v205,0x10000,v204
v_add_u32 v194,0x10000,v192
v_add_u32 v195,0x10000,v193
v_add_u32 v206,0x20000,v204
v_add_u32 v196,0x20000,v192
v_add_u32 v197,0x20000,v193
W vmcnt(0)
v_mov_b32 v218,s42
v_mov_b32 v219,s43
buffer_store_dwordx2 v[218:219],v250,s[44:47],s93 offen sc1
buffer_store_dwordx2 v[218:219],v250,s[44:47],s94 offen sc1
W lgkmcnt(0)
s_barrier
s_nop 0
buffer_load_dwordx2 v[220:221],v216,s[44:47],0 offen sc1
SA s92,s64,0
SA m0,s67,0
s_nop 0
BL v202,s[48:51],s92 offen lds
SA m0,s67,0x400
s_nop 0
BL v203,s[48:51],s92 offen lds
SA s92,s64,0x8000
SA m0,s67,0x2000
s_nop 0
BL v202,s[48:51],s92 offen lds
SA m0,s67,0x2400
s_nop 0
BL v203,s[48:51],s92 offen lds
)~"
R"~(SA s92,s64,0x10000
SA m0,s67,0x8000
s_nop 0
BL v202,s[48:51],s92 offen lds
SA m0,s67,0x8400
s_nop 0
BL v203,s[48:51],s92 offen lds
SA s92,s64,0x18000
SA m0,s67,0xa000
s_nop 0
BL v202,s[48:51],s92 offen lds
SA m0,s67,0xa400
s_nop 0
BL v203,s[48:51],s92 offen lds
SA s92,s64,0x20000
SA m0,s67,0x10000
s_nop 0
BL v202,s[48:51],s92 offen lds
SA m0,s67,0x10400
s_nop 0
BL v203,s[48:51],s92 offen lds
SA s92,s64,0x28000
SA m0,s67,0x12000
s_nop 0
BL v202,s[48:51],s92 offen lds
SA m0,s67,0x12400
s_nop 0
BL v203,s[48:51],s92 offen lds
SA s92,s64,0x30000
SA m0,s67,0x18000
s_nop 0
BL v202,s[48:51],s92 offen lds
SA m0,s67,0x18400
s_nop 0
BL v203,s[48:51],s92 offen lds
SA s92,s64,0x38000
SA m0,s67,0x1a000
s_nop 0
BL v202,s[48:51],s92 offen lds
SA m0,s67,0x1a400
s_nop 0
BL v203,s[48:51],s92 offen lds
W vmcnt(16)
v_cmp_ne_u32 s[96:97],s42,v220
v_cmp_ne_u32 vcc,s43,v221
s_nop 4
s_or_b64 vcc,vcc,s[96:97]
s_cbranch_vccnz fk_spin_ctx
fk_spin_ctx_back:
SA s93,s63,0
SA m0,s66,0
s_nop 0
BL v200,s[44:47],s93 offen sc1 lds
s_nop 0
BL v200,s[44:47],s93 offen offset:1024 sc1 lds
SA m0,s66,0x1000
s_nop 0
BL v201,s[44:47],s93 offen sc1 lds
s_nop 0
BL v201,s[44:47],s93 offen offset:1024 sc1 lds
SA s93,s63,0x800
SA m0,s66,0x8000
s_nop 0
BL v200,s[44:47],s93 offen sc1 lds
s_nop 0
BL v200,s[44:47],s93 offen offset:1024 sc1 lds
SA m0,s66,0x9000
s_nop 0
BL v201,s[44:47],s93 offen sc1 lds
s_nop 0
BL v201,s[44:47],s93 offen offset:1024 sc1 lds
SA s93,s63,0x1000
SA m0,s66,0x10000
s_nop 0
BL v200,s[44:47],s93 offen sc1 lds
s_nop 0
BL v200,s[44:47],s93 offen offset:1024 sc1 lds
SA m0,s66,0x11000
s_nop 0
BL v201,s[44:47],s93 offen sc1 lds
s_nop 0
BL v201,s[44:47],s93 offen offset:1024 sc1 lds
SA s93,s63,0x1800
SA m0,s66,0x18000
s_nop 0
BL v200,s[44:47],s93 offen sc1 lds
s_nop 0
BL v200,s[44:47],s93 offen offset:1024 sc1 lds
SA m0,s66,0x19000
s_nop 0
BL v201,s[44:47],s93 offen sc1 lds
s_nop 0
BL v201,s[44:47],s93 offen offset:1024 sc1 lds
v_and_b32 v224,15,v255
v_lshlrev_b32 v224,2,v224
s_lshl_b32 s84,s61,9
s_lshl_b32 s85,s40,8
SA s84,s84,s85
SA s84,s84,0x3400000
buffer_load_dword v208,v224,s[44:47],s84 offen sc1
buffer_load_dword v209,v224,s[44:47],s84 offen offset:64 sc1
buffer_load_dword v210,v224,s[44:47],s84 offen offset:128 sc1
buffer_load_dword v211,v224,s[44:47],s84 offen offset:192 sc1
W vmcnt(16)
s_barrier
RD a[0:3],v204
RD a[4:7],v204 offset:1024
RD a[8:11],v204 offset:2048
RD a[12:15],v204 offset:3072
RD a[16:19],v204 offset:4096
RD a[20:23],v204 offset:5120
RD a[24:27],v204 offset:6144
RD a[28:31],v204 offset:7168
TR a[64:65],v192
TR a[66:67],v192 offset:4096
TR a[68:69],v192 offset:8192
TR a[70:71],v192 offset:12288
TR a[72:73],v193
TR a[74:75],v193 offset:4096
TR a[76:77],v193 offset:8192
TR a[78:79],v193 offset:12288
TR a[80:81],v192 offset:512
TR a[82:83],v192 offset:4608
TR a[84:85],v192 offset:8704
TR a[86:87],v192 offset:12800
TR a[88:89],v193 offset:512
TR a[90:91],v193 offset:4608
TR a[92:93],v193 offset:8704
TR a[94:95],v193 offset:12800
W vmcnt(12) lgkmcnt(0)
s_barrier
M v[0:3],a[64:67],a[0:3],0
RD a[32:35],v204 offset:32768
RD a[36:39],v204 offset:33792
M v[0:3],a[68:71],a[4:7],v[0:3]
RD a[40:43],v204 offset:34816
RD a[44:47],v204 offset:35840
M v[4:7],a[64:67],a[8:11],0
SA s93,s63,0x2000
SA m0,s66,0x20000
RD a[48:51],v204 offset:36864
RD a[52:55],v204 offset:37888
M v[4:7],a[68:71],a[12:15],v[4:7]
BL v200,s[44:47],s93 offen sc1 lds
M v[8:11],a[64:67],a[16:19],0
RD a[56:59],v204 offset:38912
RD a[60:63],v204 offset:39936
M v[8:11],a[68:71],a[20:23],v[8:11]
TR a[96:97],v192 offset:32768
M v[12:15],a[64:67],a[24:27],0
TR a[98:99],v192 offset:36864
M v[12:15],a[68:71],a[28:31],v[12:15]
BL v200,s[44:47],s93 offen offset:1024 sc1 lds
M v[16:19],a[72:75],a[0:3],0
TR a[100:101],v192 offset:40960
M v[16:19],a[76:79],a[4:7],v[16:19]
TR a[102:103],v192 offset:45056
M v[20:23],a[72:75],a[8:11],0
SA m0,s66,0x21000
TR a[104:105],v193 offset:32768
M v[20:23],a[76:79],a[12:15],v[20:23]
BL v201,s[44:47],s93 offen sc1 lds
M v[24:27],a[72:75],a[16:19],0
TR a[106:107],v193 offset:36864
M v[24:27],a[76:79],a[20:23],v[24:27]
TR a[108:109],v193 offset:40960
M v[28:31],a[72:75],a[24:27],0
TR a[110:111],v193 offset:45056
M v[28:31],a[76:79],a[28:31],v[28:31]
BL v201,s[44:47],s93 offen offset:1024 sc1 lds
M v[32:35],a[80:83],a[0:3],0
TR a[112:113],v192 offset:33280
M v[32:35],a[84:87],a[4:7],v[32:35]
TR a[114:115],v192 offset:37376
M v[36:39],a[80:83],a[8:11],0
SA s92,s64,0x40000
SA m0,s67,0x20000
TR a[116:117],v192 offset:41472
M v[36:39],a[84:87],a[12:15],v[36:39]
BL v202,s[48:51],s92 offen lds
M v[40:43],a[80:83],a[16:19],0
TR a[118:119],v192 offset:45568
M v[40:43],a[84:87],a[20:23],v[40:43]
TR a[120:121],v193 offset:33280
M v[44:47],a[80:83],a[24:27],0
SA m0,s67,0x20400
TR a[122:123],v193 offset:37376
M v[44:47],a[84:87],a[28:31],v[44:47]
BL v203,s[48:51],s92 offen lds
M v[48:51],a[88:91],a[0:3],0
TR a[124:125],v193 offset:41472
M v[48:51],a[92:95],a[4:7],v[48:51]
TR a[126:127],v193 offset:45568
M v[52:55],a[88:91],a[8:11],0
SA s92,s64,0x48000
SA m0,s67,0x22000
M v[52:55],a[92:95],a[12:15],v[52:55]
BL v202,s[48:51],s92 offen lds
M v[56:59],a[88:91],a[16:19],0
M v[56:59],a[92:95],a[20:23],v[56:59]
M v[60:63],a[88:91],a[24:27],0
SA m0,s67,0x22400
M v[60:63],a[92:95],a[28:31],v[60:63]
BL v203,s[48:51],s92 offen lds
W vmcnt(16) lgkmcnt(0)
s_barrier
M v[0:3],a[96:99],a[32:35],v[0:3]
RD a[0:3],v205
RD a[4:7],v205 offset:1024
M v[0:3],a[100:103],a[36:39],v[0:3]
RD a[8:11],v205 offset:2048
RD a[12:15],v205 offset:3072
M v[4:7],a[96:99],a[40:43],v[4:7]
SA s93,s63,0x2800
SA m0,s66,0
RD a[16:19],v205 offset:4096
RD a[20:23],v205 offset:5120
M v[4:7],a[100:103],a[44:47],v[4:7]
BL v200,s[44:47],s93 offen sc1 lds
M v[8:11],a[96:99],a[48:51],v[8:11]
RD a[24:27],v205 offset:6144
RD a[28:31],v205 offset:7168
M v[8:11],a[100:103],a[52:55],v[8:11]
TR a[64:65],v194
M v[12:15],a[96:99],a[56:59],v[12:15]
TR a[66:67],v194 offset:4096
M v[12:15],a[100:103],a[60:63],v[12:15]
BL v200,s[44:47],s93 offen offset:1024 sc1 lds
M v[16:19],a[104:107],a[32:35],v[16:19]
TR a[68:69],v194 offset:8192
M v[16:19],a[108:111],a[36:39],v[16:19]
TR a[70:71],v194 offset:12288
M v[20:23],a[104:107],a[40:43],v[20:23]
SA m0,s66,0x1000
TR a[72:73],v195
M v[20:23],a[108:111],a[44:47],v[20:23]
BL v201,s[44:47],s93 offen sc1 lds
M v[24:27],a[104:107],a[48:51],v[24:27]
TR a[74:75],v195 offset:4096
M v[24:27],a[108:111],a[52:55],v[24:27]
TR a[76:77],v195 offset:8192
M v[28:31],a[104:107],a[56:59],v[28:31]
TR a[78:79],v195 offset:12288
M v[28:31],a[108:111],a[60:63],v[28:31]
BL v201,s[44:47],s93 offen offset:1024 sc1 lds
M v[32:35],a[112:115],a[32:35],v[32:35]
TR a[80:81],v194 offset:512
M v[32:35],a[116:119],a[36:39],v[32:35]
TR a[82:83],v194 offset:4608
M v[36:39],a[112:115],a[40:43],v[36:39]
SA s92,s64,0x50000
SA m0,s67,0
TR a[84:85],v194 offset:8704
M v[36:39],a[116:119],a[44:47],v[36:39]
BL v202,s[48:51],s92 offen lds
M v[40:43],a[112:115],a[48:51],v[40:43]
TR a[86:87],v194 offset:12800
M v[40:43],a[116:119],a[52:55],v[40:43]
TR a[88:89],v195 offset:512
M v[44:47],a[112:115],a[56:59],v[44:47]
SA m0,s67,0x400
TR a[90:91],v195 offset:4608
M v[44:47],a[116:119],a[60:63],v[44:47]
BL v203,s[48:51],s92 offen lds
M v[48:51],a[120:123],a[32:35],v[48:51]
TR a[92:93],v195 offset:8704
M v[48:51],a[124:127],a[36:39],v[48:51]
TR a[94:95],v195 offset:12800
M v[52:55],a[120:123],a[40:43],v[52:55]
SA s92,s64,0x58000
SA m0,s67,0x2000
M v[52:55],a[124:127],a[44:47],v[52:55]
BL v202,s[48:51],s92 offen lds
M v[56:59],a[120:123],a[48:51],v[56:59]
M v[56:59],a[124:127],a[52:55],v[56:59]
M v[60:63],a[120:123],a[56:59],v[60:63]
SA m0,s67,0x2400
M v[60:63],a[124:127],a[60:63],v[60:63]
BL v203,s[48:51],s92 offen lds
W vmcnt(20) lgkmcnt(0)
s_barrier
M v[0:3],a[64:67],a[0:3],v[0:3]
RD a[32:35],v205 offset:32768
RD a[36:39],v205 offset:33792
M v[0:3],a[68:71],a[4:7],v[0:3]
RD a[40:43],v205 offset:34816
RD a[44:47],v205 offset:35840
M v[4:7],a[64:67],a[8:11],v[4:7]
SA s93,s63,0x3000
SA m0,s66,0x8000
RD a[48:51],v205 offset:36864
RD a[52:55],v205 offset:37888
M v[4:7],a[68:71],a[12:15],v[4:7]
BL v200,s[44:47],s93 offen sc1 lds
M v[8:11],a[64:67],a[16:19],v[8:11]
RD a[56:59],v205 offset:38912
RD a[60:63],v205 offset:39936
M v[8:11],a[68:71],a[20:23],v[8:11]
TR a[96:97],v194 offset:32768
M v[12:15],a[64:67],a[24:27],v[12:15]
TR a[98:99],v194 offset:36864
M v[12:15],a[68:71],a[28:31],v[12:15]
BL v200,s[44:47],s93 offen offset:1024 sc1 lds
M v[16:19],a[72:75],a[0:3],v[16:19]
TR a[100:101],v194 offset:40960
M v[16:19],a[76:79],a[4:7],v[16:19]
TR a[102:103],v194 offset:45056
M v[20:23],a[72:75],a[8:11],v[20:23]
SA m0,s66,0x9000
TR a[104:105],v195 offset:32768
M v[20:23],a[76:79],a[12:15],v[20:23]
BL v201,s[44:47],s93 offen sc1 lds
M v[24:27],a[72:75],a[16:19],v[24:27]
TR a[106:107],v195 offset:36864
M v[24:27],a[76:79],a[20:23],v[24:27]
TR a[108:109],v195 offset:40960
M v[28:31],a[72:75],a[24:27],v[28:31]
TR a[110:111],v195 offset:45056
M v[28:31],a[76:79],a[28:31],v[28:31]
BL v201,s[44:47],s93 offen offset:1024 sc1 lds
M v[32:35],a[80:83],a[0:3],v[32:35]
TR a[112:113],v194 offset:33280
M v[32:35],a[84:87],a[4:7],v[32:35]
TR a[114:115],v194 offset:37376
M v[36:39],a[80:83],a[8:11],v[36:39]
SA s92,s64,0x60000
SA m0,s67,0x8000
TR a[116:117],v194 offset:41472
M v[36:39],a[84:87],a[12:15],v[36:39]
BL v202,s[48:51],s92 offen lds
M v[40:43],a[80:83],a[16:19],v[40:43]
TR a[118:119],v194 offset:45568
M v[40:43],a[84:87],a[20:23],v[40:43]
TR a[120:121],v195 offset:33280
M v[44:47],a[80:83],a[24:27],v[44:47]
SA m0,s67,0x8400
TR a[122:123],v195 offset:37376
M v[44:47],a[84:87],a[28:31],v[44:47]
BL v203,s[48:51],s92 offen lds
M v[48:51],a[88:91],a[0:3],v[48:51]
TR a[124:125],v195 offset:41472
M v[48:51],a[92:95],a[4:7],v[48:51]
TR a[126:127],v195 offset:45568
M v[52:55],a[88:91],a[8:11],v[52:55]
SA s92,s64,0x68000
SA m0,s67,0xa000
M v[52:55],a[92:95],a[12:15],v[52:55]
BL v202,s[48:51],s92 offen lds
M v[56:59],a[88:91],a[16:19],v[56:59]
M v[56:59],a[92:95],a[20:23],v[56:59]
M v[60:63],a[88:91],a[24:27],v[60:63]
SA m0,s67,0xa400
M v[60:63],a[92:95],a[28:31],v[60:63]
BL v203,s[48:51],s92 offen lds
W vmcnt(16) lgkmcnt(0)
s_barrier
M v[0:3],a[96:99],a[32:35],v[0:3]
RD a[0:3],v206
RD a[4:7],v206 offset:1024
M v[0:3],a[100:103],a[36:39],v[0:3]
RD a[8:11],v206 offset:2048
RD a[12:15],v206 offset:3072
M v[4:7],a[96:99],a[40:43],v[4:7]
SA s93,s63,0x3800
SA m0,s66,0x10000
RD a[16:19],v206 offset:4096
RD a[20:23],v206 offset:5120
M v[4:7],a[100:103],a[44:47],v[4:7]
BL v200,s[44:47],s93 offen sc1 lds
M v[8:11],a[96:99],a[48:51],v[8:11]
RD a[24:27],v206 offset:6144
RD a[28:31],v206 offset:7168
M v[8:11],a[100:103],a[52:55],v[8:11]
TR a[64:65],v196
M v[12:15],a[96:99],a[56:59],v[12:15]
TR a[66:67],v196 offset:4096
M v[12:15],a[100:103],a[60:63],v[12:15]
BL v200,s[44:47],s93 offen offset:1024 sc1 lds
M v[16:19],a[104:107],a[32:35],v[16:19]
TR a[68:69],v196 offset:8192
M v[16:19],a[108:111],a[36:39],v[16:19]
TR a[70:71],v196 offset:12288
M v[20:23],a[104:107],a[40:43],v[20:23]
SA m0,s66,0x11000
TR a[72:73],v197
M v[20:23],a[108:111],a[44:47],v[20:23]
BL v201,s[44:47],s93 offen sc1 lds
M v[24:27],a[104:107],a[48:51],v[24:27]
TR a[74:75],v197 offset:4096
M v[24:27],a[108:111],a[52:55],v[24:27]
TR a[76:77],v197 offset:8192
M v[28:31],a[104:107],a[56:59],v[28:31]
TR a[78:79],v197 offset:12288
M v[28:31],a[108:111],a[60:63],v[28:31]
BL v201,s[44:47],s93 offen offset:1024 sc1 lds
M v[32:35],a[112:115],a[32:35],v[32:35]
TR a[80:81],v196 offset:512
M v[32:35],a[116:119],a[36:39],v[32:35]
TR a[82:83],v196 offset:4608
M v[36:39],a[112:115],a[40:43],v[36:39]
SA s92,s64,0x70000
SA m0,s67,0x10000
TR a[84:85],v196 offset:8704
M v[36:39],a[116:119],a[44:47],v[36:39]
BL v202,s[48:51],s92 offen lds
M v[40:43],a[112:115],a[48:51],v[40:43]
TR a[86:87],v196 offset:12800
M v[40:43],a[116:119],a[52:55],v[40:43]
TR a[88:89],v197 offset:512
M v[44:47],a[112:115],a[56:59],v[44:47]
SA m0,s67,0x10400
TR a[90:91],v197 offset:4608
M v[44:47],a[116:119],a[60:63],v[44:47]
BL v203,s[48:51],s92 offen lds
M v[48:51],a[120:123],a[32:35],v[48:51]
TR a[92:93],v197 offset:8704
M v[48:51],a[124:127],a[36:39],v[48:51]
TR a[94:95],v197 offset:12800
M v[52:55],a[120:123],a[40:43],v[52:55]
SA s92,s64,0x78000
SA m0,s67,0x12000
M v[52:55],a[124:127],a[44:47],v[52:55]
BL v202,s[48:51],s92 offen lds
M v[56:59],a[120:123],a[48:51],v[56:59]
M v[56:59],a[124:127],a[52:55],v[56:59]
M v[60:63],a[120:123],a[56:59],v[60:63]
SA m0,s67,0x12400
M v[60:63],a[124:127],a[60:63],v[60:63]
BL v203,s[48:51],s92 offen lds
W vmcnt(16) lgkmcnt(0)
s_barrier
M v[0:3],a[64:67],a[0:3],v[0:3]
RD a[32:35],v204
RD a[36:39],v204 offset:1024
M v[0:3],a[68:71],a[4:7],v[0:3]
RD a[40:43],v204 offset:2048
RD a[44:47],v204 offset:3072
M v[4:7],a[64:67],a[8:11],v[4:7]
SA s93,s63,0x4000
SA m0,s66,0x18000
RD a[48:51],v204 offset:4096
RD a[52:55],v204 offset:5120
M v[4:7],a[68:71],a[12:15],v[4:7]
BL v200,s[44:47],s93 offen sc1 lds
M v[8:11],a[64:67],a[16:19],v[8:11]
RD a[56:59],v204 offset:6144
RD a[60:63],v204 offset:7168
M v[8:11],a[68:71],a[20:23],v[8:11]
TR a[96:97],v192
M v[12:15],a[64:67],a[24:27],v[12:15]
TR a[98:99],v192 offset:4096
M v[12:15],a[68:71],a[28:31],v[12:15]
BL v200,s[44:47],s93 offen offset:1024 sc1 lds
M v[16:19],a[72:75],a[0:3],v[16:19]
TR a[100:101],v192 offset:8192
M v[16:19],a[76:79],a[4:7],v[16:19]
TR a[102:103],v192 offset:12288
M v[20:23],a[72:75],a[8:11],v[20:23]
SA m0,s66,0x19000
TR a[104:105],v193
M v[20:23],a[76:79],a[12:15],v[20:23]
BL v201,s[44:47],s93 offen sc1 lds
M v[24:27],a[72:75],a[16:19],v[24:27]
TR a[106:107],v193 offset:4096
M v[24:27],a[76:79],a[20:23],v[24:27]
TR a[108:109],v193 offset:8192
M v[28:31],a[72:75],a[24:27],v[28:31]
TR a[110:111],v193 offset:12288
M v[28:31],a[76:79],a[28:31],v[28:31]
BL v201,s[44:47],s93 offen offset:1024 sc1 lds
M v[32:35],a[80:83],a[0:3],v[32:35]
TR a[112:113],v192 offset:512
M v[32:35],a[84:87],a[4:7],v[32:35]
TR a[114:115],v192 offset:4608
M v[36:39],a[80:83],a[8:11],v[36:39]
SA s92,s64,0x80000
SA m0,s67,0x18000
TR a[116:117],v192 offset:8704
M v[36:39],a[84:87],a[12:15],v[36:39]
BL v202,s[48:51],s92 offen lds
)~"
R"~(M v[40:43],a[80:83],a[16:19],v[40:43]
TR a[118:119],v192 offset:12800
M v[40:43],a[84:87],a[20:23],v[40:43]
TR a[120:121],v193 offset:512
M v[44:47],a[80:83],a[24:27],v[44:47]
SA m0,s67,0x18400
TR a[122:123],v193 offset:4608
M v[44:47],a[84:87],a[28:31],v[44:47]
BL v203,s[48:51],s92 offen lds
M v[48:51],a[88:91],a[0:3],v[48:51]
TR a[124:125],v193 offset:8704
M v[48:51],a[92:95],a[4:7],v[48:51]
TR a[126:127],v193 offset:12800
M v[52:55],a[88:91],a[8:11],v[52:55]
SA s92,s64,0x88000
SA m0,s67,0x1a000
M v[52:55],a[92:95],a[12:15],v[52:55]
BL v202,s[48:51],s92 offen lds
M v[56:59],a[88:91],a[16:19],v[56:59]
M v[56:59],a[92:95],a[20:23],v[56:59]
M v[60:63],a[88:91],a[24:27],v[60:63]
SA m0,s67,0x1a400
M v[60:63],a[92:95],a[28:31],v[60:63]
BL v203,s[48:51],s92 offen lds
W vmcnt(16) lgkmcnt(0)
s_barrier
M v[0:3],a[96:99],a[32:35],v[0:3]
RD a[0:3],v204 offset:32768
RD a[4:7],v204 offset:33792
M v[0:3],a[100:103],a[36:39],v[0:3]
RD a[8:11],v204 offset:34816
RD a[12:15],v204 offset:35840
M v[4:7],a[96:99],a[40:43],v[4:7]
SA s93,s63,0x4800
SA m0,s66,0x20000
RD a[16:19],v204 offset:36864
RD a[20:23],v204 offset:37888
M v[4:7],a[100:103],a[44:47],v[4:7]
BL v200,s[44:47],s93 offen sc1 lds
M v[8:11],a[96:99],a[48:51],v[8:11]
RD a[24:27],v204 offset:38912
RD a[28:31],v204 offset:39936
M v[8:11],a[100:103],a[52:55],v[8:11]
TR a[64:65],v192 offset:32768
M v[12:15],a[96:99],a[56:59],v[12:15]
TR a[66:67],v192 offset:36864
M v[12:15],a[100:103],a[60:63],v[12:15]
BL v200,s[44:47],s93 offen offset:1024 sc1 lds
M v[16:19],a[104:107],a[32:35],v[16:19]
TR a[68:69],v192 offset:40960
M v[16:19],a[108:111],a[36:39],v[16:19]
TR a[70:71],v192 offset:45056
M v[20:23],a[104:107],a[40:43],v[20:23]
SA m0,s66,0x21000
TR a[72:73],v193 offset:32768
M v[20:23],a[108:111],a[44:47],v[20:23]
BL v201,s[44:47],s93 offen sc1 lds
M v[24:27],a[104:107],a[48:51],v[24:27]
TR a[74:75],v193 offset:36864
M v[24:27],a[108:111],a[52:55],v[24:27]
TR a[76:77],v193 offset:40960
M v[28:31],a[104:107],a[56:59],v[28:31]
TR a[78:79],v193 offset:45056
M v[28:31],a[108:111],a[60:63],v[28:31]
BL v201,s[44:47],s93 offen offset:1024 sc1 lds
M v[32:35],a[112:115],a[32:35],v[32:35]
TR a[80:81],v192 offset:33280
M v[32:35],a[116:119],a[36:39],v[32:35]
TR a[82:83],v192 offset:37376
M v[36:39],a[112:115],a[40:43],v[36:39]
SA s92,s64,0x90000
SA m0,s67,0x20000
TR a[84:85],v192 offset:41472
M v[36:39],a[116:119],a[44:47],v[36:39]
BL v202,s[48:51],s92 offen lds
M v[40:43],a[112:115],a[48:51],v[40:43]
TR a[86:87],v192 offset:45568
M v[40:43],a[116:119],a[52:55],v[40:43]
TR a[88:89],v193 offset:33280
M v[44:47],a[112:115],a[56:59],v[44:47]
SA m0,s67,0x20400
TR a[90:91],v193 offset:37376
M v[44:47],a[116:119],a[60:63],v[44:47]
BL v203,s[48:51],s92 offen lds
M v[48:51],a[120:123],a[32:35],v[48:51]
TR a[92:93],v193 offset:41472
M v[48:51],a[124:127],a[36:39],v[48:51]
TR a[94:95],v193 offset:45568
M v[52:55],a[120:123],a[40:43],v[52:55]
SA s92,s64,0x98000
SA m0,s67,0x22000
M v[52:55],a[124:127],a[44:47],v[52:55]
BL v202,s[48:51],s92 offen lds
M v[56:59],a[120:123],a[48:51],v[56:59]
M v[56:59],a[124:127],a[52:55],v[56:59]
M v[60:63],a[120:123],a[56:59],v[60:63]
SA m0,s67,0x22400
M v[60:63],a[124:127],a[60:63],v[60:63]
BL v203,s[48:51],s92 offen lds
W vmcnt(16) lgkmcnt(0)
s_barrier
M v[0:3],a[64:67],a[0:3],v[0:3]
RD a[32:35],v205
RD a[36:39],v205 offset:1024
M v[0:3],a[68:71],a[4:7],v[0:3]
RD a[40:43],v205 offset:2048
RD a[44:47],v205 offset:3072
M v[4:7],a[64:67],a[8:11],v[4:7]
SA s93,s63,0x5000
SA m0,s66,0
RD a[48:51],v205 offset:4096
RD a[52:55],v205 offset:5120
M v[4:7],a[68:71],a[12:15],v[4:7]
BL v200,s[44:47],s93 offen sc1 lds
M v[8:11],a[64:67],a[16:19],v[8:11]
RD a[56:59],v205 offset:6144
RD a[60:63],v205 offset:7168
M v[8:11],a[68:71],a[20:23],v[8:11]
TR a[96:97],v194
M v[12:15],a[64:67],a[24:27],v[12:15]
TR a[98:99],v194 offset:4096
M v[12:15],a[68:71],a[28:31],v[12:15]
BL v200,s[44:47],s93 offen offset:1024 sc1 lds
M v[16:19],a[72:75],a[0:3],v[16:19]
TR a[100:101],v194 offset:8192
M v[16:19],a[76:79],a[4:7],v[16:19]
TR a[102:103],v194 offset:12288
M v[20:23],a[72:75],a[8:11],v[20:23]
SA m0,s66,0x1000
TR a[104:105],v195
M v[20:23],a[76:79],a[12:15],v[20:23]
BL v201,s[44:47],s93 offen sc1 lds
M v[24:27],a[72:75],a[16:19],v[24:27]
TR a[106:107],v195 offset:4096
M v[24:27],a[76:79],a[20:23],v[24:27]
TR a[108:109],v195 offset:8192
M v[28:31],a[72:75],a[24:27],v[28:31]
TR a[110:111],v195 offset:12288
M v[28:31],a[76:79],a[28:31],v[28:31]
BL v201,s[44:47],s93 offen offset:1024 sc1 lds
M v[32:35],a[80:83],a[0:3],v[32:35]
TR a[112:113],v194 offset:512
M v[32:35],a[84:87],a[4:7],v[32:35]
TR a[114:115],v194 offset:4608
M v[36:39],a[80:83],a[8:11],v[36:39]
SA s92,s65,0
SA m0,s67,0
TR a[116:117],v194 offset:8704
M v[36:39],a[84:87],a[12:15],v[36:39]
BL v202,s[52:55],s92 offen lds
M v[40:43],a[80:83],a[16:19],v[40:43]
TR a[118:119],v194 offset:12800
M v[40:43],a[84:87],a[20:23],v[40:43]
TR a[120:121],v195 offset:512
M v[44:47],a[80:83],a[24:27],v[44:47]
SA m0,s67,0x400
TR a[122:123],v195 offset:4608
M v[44:47],a[84:87],a[28:31],v[44:47]
BL v203,s[52:55],s92 offen lds
M v[48:51],a[88:91],a[0:3],v[48:51]
TR a[124:125],v195 offset:8704
M v[48:51],a[92:95],a[4:7],v[48:51]
TR a[126:127],v195 offset:12800
M v[52:55],a[88:91],a[8:11],v[52:55]
SA s92,s65,0x8000
SA m0,s67,0x2000
M v[52:55],a[92:95],a[12:15],v[52:55]
BL v202,s[52:55],s92 offen lds
M v[56:59],a[88:91],a[16:19],v[56:59]
M v[56:59],a[92:95],a[20:23],v[56:59]
M v[60:63],a[88:91],a[24:27],v[60:63]
SA m0,s67,0x2400
M v[60:63],a[92:95],a[28:31],v[60:63]
BL v203,s[52:55],s92 offen lds
W vmcnt(16) lgkmcnt(0)
s_barrier
M v[0:3],a[96:99],a[32:35],v[0:3]
RD a[0:3],v205 offset:32768
RD a[4:7],v205 offset:33792
M v[0:3],a[100:103],a[36:39],v[0:3]
RD a[8:11],v205 offset:34816
RD a[12:15],v205 offset:35840
M v[4:7],a[96:99],a[40:43],v[4:7]
SA s93,s63,0x5800
SA m0,s66,0x8000
RD a[16:19],v205 offset:36864
RD a[20:23],v205 offset:37888
M v[4:7],a[100:103],a[44:47],v[4:7]
BL v200,s[44:47],s93 offen sc1 lds
M v[8:11],a[96:99],a[48:51],v[8:11]
RD a[24:27],v205 offset:38912
RD a[28:31],v205 offset:39936
M v[8:11],a[100:103],a[52:55],v[8:11]
TR a[64:65],v194 offset:32768
M v[12:15],a[96:99],a[56:59],v[12:15]
TR a[66:67],v194 offset:36864
M v[12:15],a[100:103],a[60:63],v[12:15]
BL v200,s[44:47],s93 offen offset:1024 sc1 lds
M v[16:19],a[104:107],a[32:35],v[16:19]
TR a[68:69],v194 offset:40960
M v[16:19],a[108:111],a[36:39],v[16:19]
TR a[70:71],v194 offset:45056
M v[20:23],a[104:107],a[40:43],v[20:23]
SA m0,s66,0x9000
TR a[72:73],v195 offset:32768
M v[20:23],a[108:111],a[44:47],v[20:23]
BL v201,s[44:47],s93 offen sc1 lds
M v[24:27],a[104:107],a[48:51],v[24:27]
TR a[74:75],v195 offset:36864
M v[24:27],a[108:111],a[52:55],v[24:27]
TR a[76:77],v195 offset:40960
M v[28:31],a[104:107],a[56:59],v[28:31]
TR a[78:79],v195 offset:45056
M v[28:31],a[108:111],a[60:63],v[28:31]
BL v201,s[44:47],s93 offen offset:1024 sc1 lds
M v[32:35],a[112:115],a[32:35],v[32:35]
TR a[80:81],v194 offset:33280
M v[32:35],a[116:119],a[36:39],v[32:35]
TR a[82:83],v194 offset:37376
M v[36:39],a[112:115],a[40:43],v[36:39]
SA s92,s65,0x10000
SA m0,s67,0x8000
TR a[84:85],v194 offset:41472
M v[36:39],a[116:119],a[44:47],v[36:39]
BL v202,s[52:55],s92 offen lds
M v[40:43],a[112:115],a[48:51],v[40:43]
TR a[86:87],v194 offset:45568
M v[40:43],a[116:119],a[52:55],v[40:43]
TR a[88:89],v195 offset:33280
M v[44:47],a[112:115],a[56:59],v[44:47]
SA m0,s67,0x8400
TR a[90:91],v195 offset:37376
M v[44:47],a[116:119],a[60:63],v[44:47]
BL v203,s[52:55],s92 offen lds
M v[48:51],a[120:123],a[32:35],v[48:51]
TR a[92:93],v195 offset:41472
M v[48:51],a[124:127],a[36:39],v[48:51]
TR a[94:95],v195 offset:45568
M v[52:55],a[120:123],a[40:43],v[52:55]
SA s92,s65,0x18000
SA m0,s67,0xa000
M v[52:55],a[124:127],a[44:47],v[52:55]
BL v202,s[52:55],s92 offen lds
M v[56:59],a[120:123],a[48:51],v[56:59]
M v[56:59],a[124:127],a[52:55],v[56:59]
M v[60:63],a[120:123],a[56:59],v[60:63]
SA m0,s67,0xa400
M v[60:63],a[124:127],a[60:63],v[60:63]
BL v203,s[52:55],s92 offen lds
W vmcnt(16) lgkmcnt(0)
s_barrier
M v[0:3],a[64:67],a[0:3],v[0:3]
RD a[32:35],v206
RD a[36:39],v206 offset:1024
M v[0:3],a[68:71],a[4:7],v[0:3]
RD a[40:43],v206 offset:2048
RD a[44:47],v206 offset:3072
M v[4:7],a[64:67],a[8:11],v[4:7]
SA s93,s63,0x6000
SA m0,s66,0x10000
RD a[48:51],v206 offset:4096
RD a[52:55],v206 offset:5120
M v[4:7],a[68:71],a[12:15],v[4:7]
BL v200,s[44:47],s93 offen sc1 lds
M v[8:11],a[64:67],a[16:19],v[8:11]
RD a[56:59],v206 offset:6144
RD a[60:63],v206 offset:7168
M v[8:11],a[68:71],a[20:23],v[8:11]
TR a[96:97],v196
M v[12:15],a[64:67],a[24:27],v[12:15]
TR a[98:99],v196 offset:4096
M v[12:15],a[68:71],a[28:31],v[12:15]
BL v200,s[44:47],s93 offen offset:1024 sc1 lds
M v[16:19],a[72:75],a[0:3],v[16:19]
TR a[100:101],v196 offset:8192
M v[16:19],a[76:79],a[4:7],v[16:19]
TR a[102:103],v196 offset:12288
M v[20:23],a[72:75],a[8:11],v[20:23]
SA m0,s66,0x11000
TR a[104:105],v197
M v[20:23],a[76:79],a[12:15],v[20:23]
BL v201,s[44:47],s93 offen sc1 lds
M v[24:27],a[72:75],a[16:19],v[24:27]
TR a[106:107],v197 offset:4096
M v[24:27],a[76:79],a[20:23],v[24:27]
TR a[108:109],v197 offset:8192
M v[28:31],a[72:75],a[24:27],v[28:31]
TR a[110:111],v197 offset:12288
M v[28:31],a[76:79],a[28:31],v[28:31]
BL v201,s[44:47],s93 offen offset:1024 sc1 lds
M v[32:35],a[80:83],a[0:3],v[32:35]
TR a[112:113],v196 offset:512
M v[32:35],a[84:87],a[4:7],v[32:35]
TR a[114:115],v196 offset:4608
M v[36:39],a[80:83],a[8:11],v[36:39]
SA s92,s65,0x20000
SA m0,s67,0x10000
TR a[116:117],v196 offset:8704
M v[36:39],a[84:87],a[12:15],v[36:39]
BL v202,s[52:55],s92 offen lds
M v[40:43],a[80:83],a[16:19],v[40:43]
TR a[118:119],v196 offset:12800
M v[40:43],a[84:87],a[20:23],v[40:43]
TR a[120:121],v197 offset:512
M v[44:47],a[80:83],a[24:27],v[44:47]
SA m0,s67,0x10400
TR a[122:123],v197 offset:4608
M v[44:47],a[84:87],a[28:31],v[44:47]
BL v203,s[52:55],s92 offen lds
M v[48:51],a[88:91],a[0:3],v[48:51]
TR a[124:125],v197 offset:8704
M v[48:51],a[92:95],a[4:7],v[48:51]
TR a[126:127],v197 offset:12800
M v[52:55],a[88:91],a[8:11],v[52:55]
SA s92,s65,0x28000
SA m0,s67,0x12000
M v[52:55],a[92:95],a[12:15],v[52:55]
BL v202,s[52:55],s92 offen lds
M v[56:59],a[88:91],a[16:19],v[56:59]
M v[56:59],a[92:95],a[20:23],v[56:59]
M v[60:63],a[88:91],a[24:27],v[60:63]
SA m0,s67,0x12400
M v[60:63],a[92:95],a[28:31],v[60:63]
BL v203,s[52:55],s92 offen lds
W vmcnt(16) lgkmcnt(0)
s_barrier
M v[0:3],a[96:99],a[32:35],v[0:3]
RD a[0:3],v204
RD a[4:7],v204 offset:1024
M v[0:3],a[100:103],a[36:39],v[0:3]
RD a[8:11],v204 offset:2048
RD a[12:15],v204 offset:3072
M v[4:7],a[96:99],a[40:43],v[4:7]
SA s93,s63,0x6800
SA m0,s66,0x18000
RD a[16:19],v204 offset:4096
RD a[20:23],v204 offset:5120
M v[4:7],a[100:103],a[44:47],v[4:7]
BL v200,s[44:47],s93 offen sc1 lds
M v[8:11],a[96:99],a[48:51],v[8:11]
RD a[24:27],v204 offset:6144
RD a[28:31],v204 offset:7168
M v[8:11],a[100:103],a[52:55],v[8:11]
TR a[64:65],v192
M v[12:15],a[96:99],a[56:59],v[12:15]
TR a[66:67],v192 offset:4096
M v[12:15],a[100:103],a[60:63],v[12:15]
BL v200,s[44:47],s93 offen offset:1024 sc1 lds
M v[16:19],a[104:107],a[32:35],v[16:19]
TR a[68:69],v192 offset:8192
M v[16:19],a[108:111],a[36:39],v[16:19]
TR a[70:71],v192 offset:12288
M v[20:23],a[104:107],a[40:43],v[20:23]
SA m0,s66,0x19000
TR a[72:73],v193
M v[20:23],a[108:111],a[44:47],v[20:23]
BL v201,s[44:47],s93 offen sc1 lds
M v[24:27],a[104:107],a[48:51],v[24:27]
TR a[74:75],v193 offset:4096
M v[24:27],a[108:111],a[52:55],v[24:27]
TR a[76:77],v193 offset:8192
M v[28:31],a[104:107],a[56:59],v[28:31]
TR a[78:79],v193 offset:12288
M v[28:31],a[108:111],a[60:63],v[28:31]
BL v201,s[44:47],s93 offen offset:1024 sc1 lds
M v[32:35],a[112:115],a[32:35],v[32:35]
TR a[80:81],v192 offset:512
M v[32:35],a[116:119],a[36:39],v[32:35]
TR a[82:83],v192 offset:4608
M v[36:39],a[112:115],a[40:43],v[36:39]
SA s92,s65,0x30000
SA m0,s67,0x18000
TR a[84:85],v192 offset:8704
M v[36:39],a[116:119],a[44:47],v[36:39]
BL v202,s[52:55],s92 offen lds
M v[40:43],a[112:115],a[48:51],v[40:43]
TR a[86:87],v192 offset:12800
M v[40:43],a[116:119],a[52:55],v[40:43]
TR a[88:89],v193 offset:512
M v[44:47],a[112:115],a[56:59],v[44:47]
SA m0,s67,0x18400
TR a[90:91],v193 offset:4608
M v[44:47],a[116:119],a[60:63],v[44:47]
BL v203,s[52:55],s92 offen lds
M v[48:51],a[120:123],a[32:35],v[48:51]
TR a[92:93],v193 offset:8704
M v[48:51],a[124:127],a[36:39],v[48:51]
TR a[94:95],v193 offset:12800
M v[52:55],a[120:123],a[40:43],v[52:55]
SA s92,s65,0x38000
SA m0,s67,0x1a000
M v[52:55],a[124:127],a[44:47],v[52:55]
BL v202,s[52:55],s92 offen lds
M v[56:59],a[120:123],a[48:51],v[56:59]
M v[56:59],a[124:127],a[52:55],v[56:59]
M v[60:63],a[120:123],a[56:59],v[60:63]
SA m0,s67,0x1a400
M v[60:63],a[124:127],a[60:63],v[60:63]
BL v203,s[52:55],s92 offen lds
W vmcnt(16) lgkmcnt(0)
s_barrier
M v[64:67],a[64:67],a[0:3],0
RD a[32:35],v204 offset:32768
RD a[36:39],v204 offset:33792
M v[64:67],a[68:71],a[4:7],v[64:67]
RD a[40:43],v204 offset:34816
RD a[44:47],v204 offset:35840
M v[68:71],a[64:67],a[8:11],0
SA s93,s63,0x7000
SA m0,s66,0x20000
RD a[48:51],v204 offset:36864
RD a[52:55],v204 offset:37888
M v[68:71],a[68:71],a[12:15],v[68:71]
BL v200,s[44:47],s93 offen sc1 lds
M v[72:75],a[64:67],a[16:19],0
RD a[56:59],v204 offset:38912
RD a[60:63],v204 offset:39936
M v[72:75],a[68:71],a[20:23],v[72:75]
TR a[96:97],v192 offset:32768
M v[76:79],a[64:67],a[24:27],0
TR a[98:99],v192 offset:36864
M v[76:79],a[68:71],a[28:31],v[76:79]
BL v200,s[44:47],s93 offen offset:1024 sc1 lds
M v[80:83],a[72:75],a[0:3],0
TR a[100:101],v192 offset:40960
M v[80:83],a[76:79],a[4:7],v[80:83]
TR a[102:103],v192 offset:45056
M v[84:87],a[72:75],a[8:11],0
SA m0,s66,0x21000
TR a[104:105],v193 offset:32768
M v[84:87],a[76:79],a[12:15],v[84:87]
BL v201,s[44:47],s93 offen sc1 lds
M v[88:91],a[72:75],a[16:19],0
TR a[106:107],v193 offset:36864
M v[88:91],a[76:79],a[20:23],v[88:91]
TR a[108:109],v193 offset:40960
M v[92:95],a[72:75],a[24:27],0
TR a[110:111],v193 offset:45056
M v[92:95],a[76:79],a[28:31],v[92:95]
BL v201,s[44:47],s93 offen offset:1024 sc1 lds
)~"
R"~(M v[96:99],a[80:83],a[0:3],0
TR a[112:113],v192 offset:33280
M v[96:99],a[84:87],a[4:7],v[96:99]
TR a[114:115],v192 offset:37376
M v[100:103],a[80:83],a[8:11],0
SA s92,s65,0x40000
SA m0,s67,0x20000
TR a[116:117],v192 offset:41472
M v[100:103],a[84:87],a[12:15],v[100:103]
BL v202,s[52:55],s92 offen lds
M v[104:107],a[80:83],a[16:19],0
TR a[118:119],v192 offset:45568
M v[104:107],a[84:87],a[20:23],v[104:107]
TR a[120:121],v193 offset:33280
M v[108:111],a[80:83],a[24:27],0
SA m0,s67,0x20400
TR a[122:123],v193 offset:37376
M v[108:111],a[84:87],a[28:31],v[108:111]
BL v203,s[52:55],s92 offen lds
M v[112:115],a[88:91],a[0:3],0
TR a[124:125],v193 offset:41472
M v[112:115],a[92:95],a[4:7],v[112:115]
TR a[126:127],v193 offset:45568
M v[116:119],a[88:91],a[8:11],0
SA s92,s65,0x48000
SA m0,s67,0x22000
M v[116:119],a[92:95],a[12:15],v[116:119]
BL v202,s[52:55],s92 offen lds
M v[120:123],a[88:91],a[16:19],0
M v[120:123],a[92:95],a[20:23],v[120:123]
M v[124:127],a[88:91],a[24:27],0
SA m0,s67,0x22400
M v[124:127],a[92:95],a[28:31],v[124:127]
BL v203,s[52:55],s92 offen lds
W vmcnt(16) lgkmcnt(0)
s_barrier
M v[64:67],a[96:99],a[32:35],v[64:67]
RD a[0:3],v205
RD a[4:7],v205 offset:1024
M v[64:67],a[100:103],a[36:39],v[64:67]
RD a[8:11],v205 offset:2048
RD a[12:15],v205 offset:3072
M v[68:71],a[96:99],a[40:43],v[68:71]
SA s93,s63,0x7800
SA m0,s66,0
RD a[16:19],v205 offset:4096
RD a[20:23],v205 offset:5120
M v[68:71],a[100:103],a[44:47],v[68:71]
BL v200,s[44:47],s93 offen sc1 lds
M v[72:75],a[96:99],a[48:51],v[72:75]
RD a[24:27],v205 offset:6144
RD a[28:31],v205 offset:7168
M v[72:75],a[100:103],a[52:55],v[72:75]
TR a[64:65],v194
M v[76:79],a[96:99],a[56:59],v[76:79]
TR a[66:67],v194 offset:4096
M v[76:79],a[100:103],a[60:63],v[76:79]
BL v200,s[44:47],s93 offen offset:1024 sc1 lds
M v[80:83],a[104:107],a[32:35],v[80:83]
TR a[68:69],v194 offset:8192
M v[80:83],a[108:111],a[36:39],v[80:83]
TR a[70:71],v194 offset:12288
M v[84:87],a[104:107],a[40:43],v[84:87]
SA m0,s66,0x1000
TR a[72:73],v195
M v[84:87],a[108:111],a[44:47],v[84:87]
BL v201,s[44:47],s93 offen sc1 lds
M v[88:91],a[104:107],a[48:51],v[88:91]
TR a[74:75],v195 offset:4096
M v[88:91],a[108:111],a[52:55],v[88:91]
TR a[76:77],v195 offset:8192
M v[92:95],a[104:107],a[56:59],v[92:95]
TR a[78:79],v195 offset:12288
M v[92:95],a[108:111],a[60:63],v[92:95]
BL v201,s[44:47],s93 offen offset:1024 sc1 lds
M v[96:99],a[112:115],a[32:35],v[96:99]
TR a[80:81],v194 offset:512
M v[96:99],a[116:119],a[36:39],v[96:99]
TR a[82:83],v194 offset:4608
M v[100:103],a[112:115],a[40:43],v[100:103]
SA s92,s65,0x50000
SA m0,s67,0
TR a[84:85],v194 offset:8704
M v[100:103],a[116:119],a[44:47],v[100:103]
BL v202,s[52:55],s92 offen lds
M v[104:107],a[112:115],a[48:51],v[104:107]
TR a[86:87],v194 offset:12800
M v[104:107],a[116:119],a[52:55],v[104:107]
TR a[88:89],v195 offset:512
M v[108:111],a[112:115],a[56:59],v[108:111]
SA m0,s67,0x400
TR a[90:91],v195 offset:4608
M v[108:111],a[116:119],a[60:63],v[108:111]
BL v203,s[52:55],s92 offen lds
M v[112:115],a[120:123],a[32:35],v[112:115]
TR a[92:93],v195 offset:8704
M v[112:115],a[124:127],a[36:39],v[112:115]
TR a[94:95],v195 offset:12800
M v[116:119],a[120:123],a[40:43],v[116:119]
SA s92,s65,0x58000
SA m0,s67,0x2000
M v[116:119],a[124:127],a[44:47],v[116:119]
BL v202,s[52:55],s92 offen lds
M v[120:123],a[120:123],a[48:51],v[120:123]
M v[120:123],a[124:127],a[52:55],v[120:123]
M v[124:127],a[120:123],a[56:59],v[124:127]
SA m0,s67,0x2400
M v[124:127],a[124:127],a[60:63],v[124:127]
BL v203,s[52:55],s92 offen lds
W vmcnt(16) lgkmcnt(0)
s_barrier
M v[64:67],a[64:67],a[0:3],v[64:67]
RD a[32:35],v205 offset:32768
RD a[36:39],v205 offset:33792
M v[64:67],a[68:71],a[4:7],v[64:67]
RD a[40:43],v205 offset:34816
RD a[44:47],v205 offset:35840
M v[68:71],a[64:67],a[8:11],v[68:71]
SA s93,s63,0x8000
SA m0,s66,0x8000
RD a[48:51],v205 offset:36864
RD a[52:55],v205 offset:37888
M v[68:71],a[68:71],a[12:15],v[68:71]
BL v200,s[44:47],s93 offen sc1 lds
M v[72:75],a[64:67],a[16:19],v[72:75]
RD a[56:59],v205 offset:38912
RD a[60:63],v205 offset:39936
M v[72:75],a[68:71],a[20:23],v[72:75]
TR a[96:97],v194 offset:32768
M v[76:79],a[64:67],a[24:27],v[76:79]
TR a[98:99],v194 offset:36864
M v[76:79],a[68:71],a[28:31],v[76:79]
BL v200,s[44:47],s93 offen offset:1024 sc1 lds
M v[80:83],a[72:75],a[0:3],v[80:83]
TR a[100:101],v194 offset:40960
M v[80:83],a[76:79],a[4:7],v[80:83]
TR a[102:103],v194 offset:45056
M v[84:87],a[72:75],a[8:11],v[84:87]
SA m0,s66,0x9000
TR a[104:105],v195 offset:32768
M v[84:87],a[76:79],a[12:15],v[84:87]
BL v201,s[44:47],s93 offen sc1 lds
M v[88:91],a[72:75],a[16:19],v[88:91]
TR a[106:107],v195 offset:36864
M v[88:91],a[76:79],a[20:23],v[88:91]
TR a[108:109],v195 offset:40960
M v[92:95],a[72:75],a[24:27],v[92:95]
TR a[110:111],v195 offset:45056
M v[92:95],a[76:79],a[28:31],v[92:95]
BL v201,s[44:47],s93 offen offset:1024 sc1 lds
M v[96:99],a[80:83],a[0:3],v[96:99]
TR a[112:113],v194 offset:33280
M v[96:99],a[84:87],a[4:7],v[96:99]
TR a[114:115],v194 offset:37376
M v[100:103],a[80:83],a[8:11],v[100:103]
SA s92,s65,0x60000
SA m0,s67,0x8000
TR a[116:117],v194 offset:41472
M v[100:103],a[84:87],a[12:15],v[100:103]
BL v202,s[52:55],s92 offen lds
M v[104:107],a[80:83],a[16:19],v[104:107]
TR a[118:119],v194 offset:45568
M v[104:107],a[84:87],a[20:23],v[104:107]
TR a[120:121],v195 offset:33280
M v[108:111],a[80:83],a[24:27],v[108:111]
SA m0,s67,0x8400
TR a[122:123],v195 offset:37376
M v[108:111],a[84:87],a[28:31],v[108:111]
BL v203,s[52:55],s92 offen lds
M v[112:115],a[88:91],a[0:3],v[112:115]
TR a[124:125],v195 offset:41472
M v[112:115],a[92:95],a[4:7],v[112:115]
TR a[126:127],v195 offset:45568
M v[116:119],a[88:91],a[8:11],v[116:119]
SA s92,s65,0x68000
SA m0,s67,0xa000
M v[116:119],a[92:95],a[12:15],v[116:119]
BL v202,s[52:55],s92 offen lds
M v[120:123],a[88:91],a[16:19],v[120:123]
M v[120:123],a[92:95],a[20:23],v[120:123]
M v[124:127],a[88:91],a[24:27],v[124:127]
SA m0,s67,0xa400
M v[124:127],a[92:95],a[28:31],v[124:127]
BL v203,s[52:55],s92 offen lds
W vmcnt(16) lgkmcnt(0)
s_barrier
M v[64:67],a[96:99],a[32:35],v[64:67]
RD a[0:3],v206
RD a[4:7],v206 offset:1024
M v[64:67],a[100:103],a[36:39],v[64:67]
RD a[8:11],v206 offset:2048
RD a[12:15],v206 offset:3072
M v[68:71],a[96:99],a[40:43],v[68:71]
SA s93,s63,0x8800
SA m0,s66,0x10000
RD a[16:19],v206 offset:4096
RD a[20:23],v206 offset:5120
M v[68:71],a[100:103],a[44:47],v[68:71]
BL v200,s[44:47],s93 offen sc1 lds
M v[72:75],a[96:99],a[48:51],v[72:75]
RD a[24:27],v206 offset:6144
RD a[28:31],v206 offset:7168
M v[72:75],a[100:103],a[52:55],v[72:75]
TR a[64:65],v196
M v[76:79],a[96:99],a[56:59],v[76:79]
TR a[66:67],v196 offset:4096
M v[76:79],a[100:103],a[60:63],v[76:79]
BL v200,s[44:47],s93 offen offset:1024 sc1 lds
M v[80:83],a[104:107],a[32:35],v[80:83]
TR a[68:69],v196 offset:8192
M v[80:83],a[108:111],a[36:39],v[80:83]
TR a[70:71],v196 offset:12288
M v[84:87],a[104:107],a[40:43],v[84:87]
SA m0,s66,0x11000
TR a[72:73],v197
M v[84:87],a[108:111],a[44:47],v[84:87]
BL v201,s[44:47],s93 offen sc1 lds
M v[88:91],a[104:107],a[48:51],v[88:91]
TR a[74:75],v197 offset:4096
M v[88:91],a[108:111],a[52:55],v[88:91]
TR a[76:77],v197 offset:8192
M v[92:95],a[104:107],a[56:59],v[92:95]
TR a[78:79],v197 offset:12288
M v[92:95],a[108:111],a[60:63],v[92:95]
BL v201,s[44:47],s93 offen offset:1024 sc1 lds
M v[96:99],a[112:115],a[32:35],v[96:99]
TR a[80:81],v196 offset:512
M v[96:99],a[116:119],a[36:39],v[96:99]
TR a[82:83],v196 offset:4608
M v[100:103],a[112:115],a[40:43],v[100:103]
SA s92,s65,0x70000
SA m0,s67,0x10000
TR a[84:85],v196 offset:8704
M v[100:103],a[116:119],a[44:47],v[100:103]
BL v202,s[52:55],s92 offen lds
M v[104:107],a[112:115],a[48:51],v[104:107]
TR a[86:87],v196 offset:12800
M v[104:107],a[116:119],a[52:55],v[104:107]
TR a[88:89],v197 offset:512
M v[108:111],a[112:115],a[56:59],v[108:111]
SA m0,s67,0x10400
TR a[90:91],v197 offset:4608
M v[108:111],a[116:119],a[60:63],v[108:111]
BL v203,s[52:55],s92 offen lds
M v[112:115],a[120:123],a[32:35],v[112:115]
TR a[92:93],v197 offset:8704
M v[112:115],a[124:127],a[36:39],v[112:115]
TR a[94:95],v197 offset:12800
M v[116:119],a[120:123],a[40:43],v[116:119]
SA s92,s65,0x78000
SA m0,s67,0x12000
M v[116:119],a[124:127],a[44:47],v[116:119]
BL v202,s[52:55],s92 offen lds
M v[120:123],a[120:123],a[48:51],v[120:123]
M v[120:123],a[124:127],a[52:55],v[120:123]
M v[124:127],a[120:123],a[56:59],v[124:127]
SA m0,s67,0x12400
M v[124:127],a[124:127],a[60:63],v[124:127]
BL v203,s[52:55],s92 offen lds
W vmcnt(16) lgkmcnt(0)
s_barrier
M v[64:67],a[64:67],a[0:3],v[64:67]
RD a[32:35],v204
RD a[36:39],v204 offset:1024
M v[64:67],a[68:71],a[4:7],v[64:67]
RD a[40:43],v204 offset:2048
RD a[44:47],v204 offset:3072
M v[68:71],a[64:67],a[8:11],v[68:71]
RD a[48:51],v204 offset:4096
RD a[52:55],v204 offset:5120
M v[68:71],a[68:71],a[12:15],v[68:71]
RD a[56:59],v204 offset:6144
RD a[60:63],v204 offset:7168
M v[72:75],a[64:67],a[16:19],v[72:75]
TR a[96:97],v192
M v[72:75],a[68:71],a[20:23],v[72:75]
TR a[98:99],v192 offset:4096
M v[76:79],a[64:67],a[24:27],v[76:79]
TR a[100:101],v192 offset:8192
M v[76:79],a[68:71],a[28:31],v[76:79]
TR a[102:103],v192 offset:12288
M v[80:83],a[72:75],a[0:3],v[80:83]
TR a[104:105],v193
M v[80:83],a[76:79],a[4:7],v[80:83]
TR a[106:107],v193 offset:4096
M v[84:87],a[72:75],a[8:11],v[84:87]
TR a[108:109],v193 offset:8192
M v[84:87],a[76:79],a[12:15],v[84:87]
TR a[110:111],v193 offset:12288
M v[88:91],a[72:75],a[16:19],v[88:91]
TR a[112:113],v192 offset:512
M v[88:91],a[76:79],a[20:23],v[88:91]
TR a[114:115],v192 offset:4608
M v[92:95],a[72:75],a[24:27],v[92:95]
TR a[116:117],v192 offset:8704
M v[92:95],a[76:79],a[28:31],v[92:95]
TR a[118:119],v192 offset:12800
M v[96:99],a[80:83],a[0:3],v[96:99]
TR a[120:121],v193 offset:512
M v[96:99],a[84:87],a[4:7],v[96:99]
TR a[122:123],v193 offset:4608
M v[100:103],a[80:83],a[8:11],v[100:103]
TR a[124:125],v193 offset:8704
M v[100:103],a[84:87],a[12:15],v[100:103]
TR a[126:127],v193 offset:12800
M v[104:107],a[80:83],a[16:19],v[104:107]
M v[104:107],a[84:87],a[20:23],v[104:107]
M v[108:111],a[80:83],a[24:27],v[108:111]
M v[108:111],a[84:87],a[28:31],v[108:111]
M v[112:115],a[88:91],a[0:3],v[112:115]
M v[112:115],a[92:95],a[4:7],v[112:115]
M v[116:119],a[88:91],a[8:11],v[116:119]
M v[116:119],a[92:95],a[12:15],v[116:119]
M v[120:123],a[88:91],a[16:19],v[120:123]
M v[120:123],a[92:95],a[20:23],v[120:123]
M v[124:127],a[88:91],a[24:27],v[124:127]
M v[124:127],a[92:95],a[28:31],v[124:127]
W vmcnt(8) lgkmcnt(0)
s_barrier
M v[64:67],a[96:99],a[32:35],v[64:67]
RD a[0:3],v204 offset:32768
RD a[4:7],v204 offset:33792
M v[64:67],a[100:103],a[36:39],v[64:67]
RD a[8:11],v204 offset:34816
RD a[12:15],v204 offset:35840
M v[68:71],a[96:99],a[40:43],v[68:71]
RD a[16:19],v204 offset:36864
RD a[20:23],v204 offset:37888
M v[68:71],a[100:103],a[44:47],v[68:71]
RD a[24:27],v204 offset:38912
RD a[28:31],v204 offset:39936
M v[72:75],a[96:99],a[48:51],v[72:75]
TR a[64:65],v192 offset:32768
M v[72:75],a[100:103],a[52:55],v[72:75]
TR a[66:67],v192 offset:36864
M v[76:79],a[96:99],a[56:59],v[76:79]
TR a[68:69],v192 offset:40960
M v[76:79],a[100:103],a[60:63],v[76:79]
TR a[70:71],v192 offset:45056
M v[80:83],a[104:107],a[32:35],v[80:83]
TR a[72:73],v193 offset:32768
M v[80:83],a[108:111],a[36:39],v[80:83]
TR a[74:75],v193 offset:36864
M v[84:87],a[104:107],a[40:43],v[84:87]
TR a[76:77],v193 offset:40960
M v[84:87],a[108:111],a[44:47],v[84:87]
TR a[78:79],v193 offset:45056
M v[88:91],a[104:107],a[48:51],v[88:91]
TR a[80:81],v192 offset:33280
M v[88:91],a[108:111],a[52:55],v[88:91]
TR a[82:83],v192 offset:37376
M v[92:95],a[104:107],a[56:59],v[92:95]
TR a[84:85],v192 offset:41472
M v[92:95],a[108:111],a[60:63],v[92:95]
TR a[86:87],v192 offset:45568
M v[96:99],a[112:115],a[32:35],v[96:99]
TR a[88:89],v193 offset:33280
M v[96:99],a[116:119],a[36:39],v[96:99]
TR a[90:91],v193 offset:37376
M v[100:103],a[112:115],a[40:43],v[100:103]
TR a[92:93],v193 offset:41472
M v[100:103],a[116:119],a[44:47],v[100:103]
TR a[94:95],v193 offset:45568
M v[104:107],a[112:115],a[48:51],v[104:107]
M v[104:107],a[116:119],a[52:55],v[104:107]
M v[108:111],a[112:115],a[56:59],v[108:111]
M v[108:111],a[116:119],a[60:63],v[108:111]
M v[112:115],a[120:123],a[32:35],v[112:115]
M v[112:115],a[124:127],a[36:39],v[112:115]
M v[116:119],a[120:123],a[40:43],v[116:119]
M v[116:119],a[124:127],a[44:47],v[116:119]
M v[120:123],a[120:123],a[48:51],v[120:123]
M v[120:123],a[124:127],a[52:55],v[120:123]
M v[124:127],a[120:123],a[56:59],v[124:127]
M v[124:127],a[124:127],a[60:63],v[124:127]
W vmcnt(0) lgkmcnt(0)
s_barrier
M v[64:67],a[64:67],a[0:3],v[64:67]
RD a[32:35],v205
RD a[36:39],v205 offset:1024
M v[64:67],a[68:71],a[4:7],v[64:67]
RD a[40:43],v205 offset:2048
RD a[44:47],v205 offset:3072
M v[68:71],a[64:67],a[8:11],v[68:71]
RD a[48:51],v205 offset:4096
RD a[52:55],v205 offset:5120
M v[68:71],a[68:71],a[12:15],v[68:71]
RD a[56:59],v205 offset:6144
RD a[60:63],v205 offset:7168
M v[72:75],a[64:67],a[16:19],v[72:75]
TR a[96:97],v194
M v[72:75],a[68:71],a[20:23],v[72:75]
TR a[98:99],v194 offset:4096
M v[76:79],a[64:67],a[24:27],v[76:79]
TR a[100:101],v194 offset:8192
M v[76:79],a[68:71],a[28:31],v[76:79]
TR a[102:103],v194 offset:12288
M v[80:83],a[72:75],a[0:3],v[80:83]
TR a[104:105],v195
M v[80:83],a[76:79],a[4:7],v[80:83]
TR a[106:107],v195 offset:4096
M v[84:87],a[72:75],a[8:11],v[84:87]
TR a[108:109],v195 offset:8192
M v[84:87],a[76:79],a[12:15],v[84:87]
TR a[110:111],v195 offset:12288
M v[88:91],a[72:75],a[16:19],v[88:91]
TR a[112:113],v194 offset:512
M v[88:91],a[76:79],a[20:23],v[88:91]
TR a[114:115],v194 offset:4608
M v[92:95],a[72:75],a[24:27],v[92:95]
TR a[116:117],v194 offset:8704
M v[92:95],a[76:79],a[28:31],v[92:95]
TR a[118:119],v194 offset:12800
M v[96:99],a[80:83],a[0:3],v[96:99]
TR a[120:121],v195 offset:512
M v[96:99],a[84:87],a[4:7],v[96:99]
TR a[122:123],v195 offset:4608
M v[100:103],a[80:83],a[8:11],v[100:103]
TR a[124:125],v195 offset:8704
M v[100:103],a[84:87],a[12:15],v[100:103]
TR a[126:127],v195 offset:12800
)~"
R"~(M v[104:107],a[80:83],a[16:19],v[104:107]
M v[104:107],a[84:87],a[20:23],v[104:107]
M v[108:111],a[80:83],a[24:27],v[108:111]
M v[108:111],a[84:87],a[28:31],v[108:111]
M v[112:115],a[88:91],a[0:3],v[112:115]
M v[112:115],a[92:95],a[4:7],v[112:115]
M v[116:119],a[88:91],a[8:11],v[116:119]
M v[116:119],a[92:95],a[12:15],v[116:119]
M v[120:123],a[88:91],a[16:19],v[120:123]
M v[120:123],a[92:95],a[20:23],v[120:123]
M v[124:127],a[88:91],a[24:27],v[124:127]
M v[124:127],a[92:95],a[28:31],v[124:127]
W lgkmcnt(14)
M v[64:67],a[96:99],a[32:35],v[64:67]
W lgkmcnt(12)
M v[64:67],a[100:103],a[36:39],v[64:67]
M v[68:71],a[96:99],a[40:43],v[68:71]
M v[68:71],a[100:103],a[44:47],v[68:71]
M v[72:75],a[96:99],a[48:51],v[72:75]
M v[72:75],a[100:103],a[52:55],v[72:75]
M v[76:79],a[96:99],a[56:59],v[76:79]
M v[76:79],a[100:103],a[60:63],v[76:79]
W lgkmcnt(10)
M v[80:83],a[104:107],a[32:35],v[80:83]
W lgkmcnt(8)
M v[80:83],a[108:111],a[36:39],v[80:83]
M v[84:87],a[104:107],a[40:43],v[84:87]
M v[84:87],a[108:111],a[44:47],v[84:87]
M v[88:91],a[104:107],a[48:51],v[88:91]
M v[88:91],a[108:111],a[52:55],v[88:91]
M v[92:95],a[104:107],a[56:59],v[92:95]
M v[92:95],a[108:111],a[60:63],v[92:95]
W lgkmcnt(6)
M v[96:99],a[112:115],a[32:35],v[96:99]
W lgkmcnt(4)
M v[96:99],a[116:119],a[36:39],v[96:99]
M v[100:103],a[112:115],a[40:43],v[100:103]
M v[100:103],a[116:119],a[44:47],v[100:103]
M v[104:107],a[112:115],a[48:51],v[104:107]
M v[104:107],a[116:119],a[52:55],v[104:107]
M v[108:111],a[112:115],a[56:59],v[108:111]
M v[108:111],a[116:119],a[60:63],v[108:111]
W lgkmcnt(2)
M v[112:115],a[120:123],a[32:35],v[112:115]
W lgkmcnt(0)
M v[112:115],a[124:127],a[36:39],v[112:115]
M v[116:119],a[120:123],a[40:43],v[116:119]
M v[116:119],a[124:127],a[44:47],v[116:119]
M v[120:123],a[120:123],a[48:51],v[120:123]
M v[120:123],a[124:127],a[52:55],v[120:123]
M v[124:127],a[120:123],a[56:59],v[124:127]
M v[124:127],a[124:127],a[60:63],v[124:127]
s_cmp_eq_u32 s60,3
s_cselect_b32 s84,1.0,0
s_cselect_b32 s85,0,1.0
v_sub_f32 v224,1.0,v208
v_mul_f32 v208,s84,v208
v_fma_f32 v208,s85,v224,v208
v_sub_f32 v226,1.0,v209
v_mul_f32 v209,s84,v209
v_fma_f32 v209,s85,v226,v209
v_sub_f32 v228,1.0,v210
v_mul_f32 v210,s84,v210
v_fma_f32 v210,s85,v228,v210
v_sub_f32 v230,1.0,v211
v_mul_f32 v211,s84,v211
v_fma_f32 v211,s85,v230,v211
v_mov_b32 v225,v224
v_mov_b32 v144,v208
v_mov_b32 v145,v208
v_mov_b32 v227,v226
v_mov_b32 v146,v209
v_mov_b32 v147,v209
v_mov_b32 v229,v228
v_mov_b32 v148,v210
v_mov_b32 v149,v210
v_mov_b32 v231,v230
v_mov_b32 v150,v211
v_mov_b32 v151,v211
s_lshl_b32 s86,s68,17
s_lshl_b32 s87,s36,11
SA s86,s86,s87
SA s86,s86,0x4000000
s_lshl_b32 s87,s60,13
SA s87,s87,s86
SA s88,s87,0
s_cmp_eq_u32 s60,0
s_cselect_b32 s88,0x40000000,s88
v_pk_mul_f32 v[0:1],v[0:1],v[224:225]
v_pk_mul_f32 v[2:3],v[2:3],v[224:225]
v_pk_fma_f32 v[0:1],v[64:65],v[144:145],v[0:1]
v_pk_fma_f32 v[2:3],v[66:67],v[144:145],v[2:3]
v_pk_mul_f32 v[4:5],v[4:5],v[226:227]
v_pk_mul_f32 v[6:7],v[6:7],v[226:227]
v_pk_fma_f32 v[4:5],v[68:69],v[146:147],v[4:5]
v_pk_fma_f32 v[6:7],v[70:71],v[146:147],v[6:7]
v_pk_mul_f32 v[8:9],v[8:9],v[228:229]
v_pk_mul_f32 v[10:11],v[10:11],v[228:229]
v_pk_fma_f32 v[8:9],v[72:73],v[148:149],v[8:9]
v_pk_fma_f32 v[10:11],v[74:75],v[148:149],v[10:11]
v_pk_mul_f32 v[12:13],v[12:13],v[230:231]
v_pk_mul_f32 v[14:15],v[14:15],v[230:231]
v_pk_fma_f32 v[12:13],v[76:77],v[150:151],v[12:13]
v_pk_fma_f32 v[14:15],v[78:79],v[150:151],v[14:15]
CV v64,v0,v1
CV v65,v2,v3
CV v66,v4,v5
CV v67,v6,v7
BS v[64:67],v254,s[44:47],s88 offen sc1
CV v72,v8,v9
CV v73,v10,v11
CV v74,v12,v13
CV v75,v14,v15
BS v[72:75],v254,s[44:47],s88 offen offset:1024 sc1
SA s88,s87,0x8000
s_cmp_eq_u32 s60,1
s_cselect_b32 s88,0x40000000,s88
v_pk_mul_f32 v[16:17],v[16:17],v[224:225]
v_pk_mul_f32 v[18:19],v[18:19],v[224:225]
v_pk_fma_f32 v[16:17],v[80:81],v[144:145],v[16:17]
v_pk_fma_f32 v[18:19],v[82:83],v[144:145],v[18:19]
v_pk_mul_f32 v[20:21],v[20:21],v[226:227]
v_pk_mul_f32 v[22:23],v[22:23],v[226:227]
v_pk_fma_f32 v[20:21],v[84:85],v[146:147],v[20:21]
v_pk_fma_f32 v[22:23],v[86:87],v[146:147],v[22:23]
v_pk_mul_f32 v[24:25],v[24:25],v[228:229]
v_pk_mul_f32 v[26:27],v[26:27],v[228:229]
v_pk_fma_f32 v[24:25],v[88:89],v[148:149],v[24:25]
v_pk_fma_f32 v[26:27],v[90:91],v[148:149],v[26:27]
v_pk_mul_f32 v[28:29],v[28:29],v[230:231]
v_pk_mul_f32 v[30:31],v[30:31],v[230:231]
v_pk_fma_f32 v[28:29],v[92:93],v[150:151],v[28:29]
v_pk_fma_f32 v[30:31],v[94:95],v[150:151],v[30:31]
CV v80,v16,v17
CV v81,v18,v19
CV v82,v20,v21
CV v83,v22,v23
BS v[80:83],v254,s[44:47],s88 offen sc1
CV v88,v24,v25
CV v89,v26,v27
CV v90,v28,v29
CV v91,v30,v31
BS v[88:91],v254,s[44:47],s88 offen offset:1024 sc1
SA s88,s87,0x10000
s_cmp_eq_u32 s60,2
s_cselect_b32 s88,0x40000000,s88
v_pk_mul_f32 v[32:33],v[32:33],v[224:225]
v_pk_mul_f32 v[34:35],v[34:35],v[224:225]
v_pk_fma_f32 v[32:33],v[96:97],v[144:145],v[32:33]
v_pk_fma_f32 v[34:35],v[98:99],v[144:145],v[34:35]
v_pk_mul_f32 v[36:37],v[36:37],v[226:227]
v_pk_mul_f32 v[38:39],v[38:39],v[226:227]
v_pk_fma_f32 v[36:37],v[100:101],v[146:147],v[36:37]
v_pk_fma_f32 v[38:39],v[102:103],v[146:147],v[38:39]
v_pk_mul_f32 v[40:41],v[40:41],v[228:229]
v_pk_mul_f32 v[42:43],v[42:43],v[228:229]
v_pk_fma_f32 v[40:41],v[104:105],v[148:149],v[40:41]
v_pk_fma_f32 v[42:43],v[106:107],v[148:149],v[42:43]
v_pk_mul_f32 v[44:45],v[44:45],v[230:231]
v_pk_mul_f32 v[46:47],v[46:47],v[230:231]
v_pk_fma_f32 v[44:45],v[108:109],v[150:151],v[44:45]
v_pk_fma_f32 v[46:47],v[110:111],v[150:151],v[46:47]
CV v96,v32,v33
CV v97,v34,v35
CV v98,v36,v37
CV v99,v38,v39
BS v[96:99],v254,s[44:47],s88 offen sc1
CV v104,v40,v41
CV v105,v42,v43
CV v106,v44,v45
CV v107,v46,v47
BS v[104:107],v254,s[44:47],s88 offen offset:1024 sc1
SA s88,s87,0x18000
s_cmp_eq_u32 s60,3
s_cselect_b32 s88,0x40000000,s88
v_pk_mul_f32 v[48:49],v[48:49],v[224:225]
v_pk_mul_f32 v[50:51],v[50:51],v[224:225]
v_pk_fma_f32 v[48:49],v[112:113],v[144:145],v[48:49]
v_pk_fma_f32 v[50:51],v[114:115],v[144:145],v[50:51]
v_pk_mul_f32 v[52:53],v[52:53],v[226:227]
v_pk_mul_f32 v[54:55],v[54:55],v[226:227]
v_pk_fma_f32 v[52:53],v[116:117],v[146:147],v[52:53]
v_pk_fma_f32 v[54:55],v[118:119],v[146:147],v[54:55]
v_pk_mul_f32 v[56:57],v[56:57],v[228:229]
v_pk_mul_f32 v[58:59],v[58:59],v[228:229]
v_pk_fma_f32 v[56:57],v[120:121],v[148:149],v[56:57]
v_pk_fma_f32 v[58:59],v[122:123],v[148:149],v[58:59]
v_pk_mul_f32 v[60:61],v[60:61],v[230:231]
v_pk_mul_f32 v[62:63],v[62:63],v[230:231]
v_pk_fma_f32 v[60:61],v[124:125],v[150:151],v[60:61]
v_pk_fma_f32 v[62:63],v[126:127],v[150:151],v[62:63]
CV v112,v48,v49
CV v113,v50,v51
CV v114,v52,v53
CV v115,v54,v55
BS v[112:115],v254,s[44:47],s88 offen sc1
CV v120,v56,v57
CV v121,v58,v59
CV v122,v60,v61
CV v123,v62,v63
BS v[120:123],v254,s[44:47],s88 offen offset:1024 sc1
s_cmp_eq_u32 s60,0
s_cbranch_scc0 fk_own0
v_mov_b32 v128,v0
v_mov_b32 v129,v1
v_mov_b32 v130,v2
v_mov_b32 v131,v3
v_mov_b32 v132,v4
v_mov_b32 v133,v5
v_mov_b32 v134,v6
v_mov_b32 v135,v7
v_mov_b32 v136,v8
v_mov_b32 v137,v9
v_mov_b32 v138,v10
v_mov_b32 v139,v11
v_mov_b32 v140,v12
v_mov_b32 v141,v13
v_mov_b32 v142,v14
v_mov_b32 v143,v15
fk_own0:
s_cmp_eq_u32 s60,1
s_cbranch_scc0 fk_own1
v_mov_b32 v128,v16
v_mov_b32 v129,v17
v_mov_b32 v130,v18
v_mov_b32 v131,v19
v_mov_b32 v132,v20
v_mov_b32 v133,v21
v_mov_b32 v134,v22
v_mov_b32 v135,v23
v_mov_b32 v136,v24
v_mov_b32 v137,v25
v_mov_b32 v138,v26
v_mov_b32 v139,v27
v_mov_b32 v140,v28
v_mov_b32 v141,v29
v_mov_b32 v142,v30
v_mov_b32 v143,v31
fk_own1:
s_cmp_eq_u32 s60,2
s_cbranch_scc0 fk_own2
v_mov_b32 v128,v32
v_mov_b32 v129,v33
v_mov_b32 v130,v34
v_mov_b32 v131,v35
v_mov_b32 v132,v36
v_mov_b32 v133,v37
v_mov_b32 v134,v38
v_mov_b32 v135,v39
v_mov_b32 v136,v40
v_mov_b32 v137,v41
v_mov_b32 v138,v42
v_mov_b32 v139,v43
v_mov_b32 v140,v44
v_mov_b32 v141,v45
v_mov_b32 v142,v46
v_mov_b32 v143,v47
fk_own2:
s_cmp_eq_u32 s60,3
s_cbranch_scc0 fk_own3
v_mov_b32 v128,v48
v_mov_b32 v129,v49
v_mov_b32 v130,v50
v_mov_b32 v131,v51
v_mov_b32 v132,v52
v_mov_b32 v133,v53
v_mov_b32 v134,v54
v_mov_b32 v135,v55
v_mov_b32 v136,v56
v_mov_b32 v137,v57
v_mov_b32 v138,v58
v_mov_b32 v139,v59
v_mov_b32 v140,v60
v_mov_b32 v141,v61
v_mov_b32 v142,v62
v_mov_b32 v143,v63
fk_own3:
s_lshl_b32 s89,s68,10
s_lshl_b32 s90,s36,6
SA s89,s89,s90
SA s89,s89,0x3510000
s_lshl_b32 s90,s60,8
SA s90,s90,s89
v_and_b32 v216,3,v255
v_lshlrev_b32 v216,8,v216
v_add_u32 v216,s89,v216
s_mov_b64 s[48:49],s[26:27]
s_and_b32 s49,s49,0xffff
s_mov_b32 s50,0x200000
s_mov_b32 s51,0x20000
v_and_b32 v232,15,v255
v_lshlrev_b32 v232,10,v232
v_lshrrev_b32 v233,4,v255
v_lshl_add_u32 v232,v233,3,v232
s_lshl_b32 s84,s61,17
s_lshl_b32 s85,s40,16
SA s84,s84,s85
s_lshl_b32 s85,s62,8
SA s84,s84,s85
s_lshl_b32 s85,s41,7
SA s84,s84,s85
s_lshl_b32 s85,s60,5
SA s84,s84,s85
s_lshl_b32 s87,s60,15
SA s87,s87,s86
W vmcnt(0)
v_mov_b32 v218,s42
v_mov_b32 v219,s43
buffer_store_dwordx2 v[218:219],v250,s[44:47],s90 offen sc1
buffer_load_dwordx2 v[220:221],v216,s[44:47],0 offen sc1
W vmcnt(0)
v_cmp_ne_u32 s[96:97],s42,v220
v_cmp_ne_u32 vcc,s43,v221
s_nop 4
s_or_b64 vcc,vcc,s[96:97]
s_cbranch_vccnz fk_spin_p
fk_spin_p_back:
SA s88,s60,1
s_and_b32 s88,s88,3
s_lshl_b32 s88,s88,13
SA s88,s88,s87
BL v[0:3],v254,s[44:47],s88 offen sc1
BL v[4:7],v254,s[44:47],s88 offen offset:1024 sc1
SA s88,s60,2
s_and_b32 s88,s88,3
s_lshl_b32 s88,s88,13
SA s88,s88,s87
BL v[8:11],v254,s[44:47],s88 offen sc1
BL v[12:15],v254,s[44:47],s88 offen offset:1024 sc1
SA s88,s60,3
s_and_b32 s88,s88,3
s_lshl_b32 s88,s88,13
SA s88,s88,s87
BL v[16:19],v254,s[44:47],s88 offen sc1
BL v[20:23],v254,s[44:47],s88 offen offset:1024 sc1
W vmcnt(4)
v_lshlrev_b32 v32,16,v0
v_and_b32 v33,0xffff0000,v0
v_pk_add_f32 v[128:129],v[128:129],v[32:33]
v_lshlrev_b32 v32,16,v1
v_and_b32 v33,0xffff0000,v1
v_pk_add_f32 v[130:131],v[130:131],v[32:33]
v_lshlrev_b32 v32,16,v2
v_and_b32 v33,0xffff0000,v2
v_pk_add_f32 v[132:133],v[132:133],v[32:33]
v_lshlrev_b32 v32,16,v3
v_and_b32 v33,0xffff0000,v3
v_pk_add_f32 v[134:135],v[134:135],v[32:33]
v_lshlrev_b32 v32,16,v4
v_and_b32 v33,0xffff0000,v4
v_pk_add_f32 v[136:137],v[136:137],v[32:33]
v_lshlrev_b32 v32,16,v5
v_and_b32 v33,0xffff0000,v5
v_pk_add_f32 v[138:139],v[138:139],v[32:33]
v_lshlrev_b32 v32,16,v6
v_and_b32 v33,0xffff0000,v6
v_pk_add_f32 v[140:141],v[140:141],v[32:33]
v_lshlrev_b32 v32,16,v7
v_and_b32 v33,0xffff0000,v7
v_pk_add_f32 v[142:143],v[142:143],v[32:33]
W vmcnt(2)
v_lshlrev_b32 v32,16,v8
v_and_b32 v33,0xffff0000,v8
v_pk_add_f32 v[128:129],v[128:129],v[32:33]
v_lshlrev_b32 v32,16,v9
v_and_b32 v33,0xffff0000,v9
v_pk_add_f32 v[130:131],v[130:131],v[32:33]
v_lshlrev_b32 v32,16,v10
v_and_b32 v33,0xffff0000,v10
v_pk_add_f32 v[132:133],v[132:133],v[32:33]
v_lshlrev_b32 v32,16,v11
v_and_b32 v33,0xffff0000,v11
v_pk_add_f32 v[134:135],v[134:135],v[32:33]
v_lshlrev_b32 v32,16,v12
v_and_b32 v33,0xffff0000,v12
v_pk_add_f32 v[136:137],v[136:137],v[32:33]
v_lshlrev_b32 v32,16,v13
v_and_b32 v33,0xffff0000,v13
v_pk_add_f32 v[138:139],v[138:139],v[32:33]
v_lshlrev_b32 v32,16,v14
v_and_b32 v33,0xffff0000,v14
v_pk_add_f32 v[140:141],v[140:141],v[32:33]
v_lshlrev_b32 v32,16,v15
v_and_b32 v33,0xffff0000,v15
v_pk_add_f32 v[142:143],v[142:143],v[32:33]
W vmcnt(0)
v_lshlrev_b32 v32,16,v16
v_and_b32 v33,0xffff0000,v16
v_pk_add_f32 v[128:129],v[128:129],v[32:33]
v_lshlrev_b32 v32,16,v17
v_and_b32 v33,0xffff0000,v17
v_pk_add_f32 v[130:131],v[130:131],v[32:33]
v_lshlrev_b32 v32,16,v18
v_and_b32 v33,0xffff0000,v18
v_pk_add_f32 v[132:133],v[132:133],v[32:33]
v_lshlrev_b32 v32,16,v19
v_and_b32 v33,0xffff0000,v19
v_pk_add_f32 v[134:135],v[134:135],v[32:33]
v_lshlrev_b32 v32,16,v20
v_and_b32 v33,0xffff0000,v20
v_pk_add_f32 v[136:137],v[136:137],v[32:33]
v_lshlrev_b32 v32,16,v21
v_and_b32 v33,0xffff0000,v21
v_pk_add_f32 v[138:139],v[138:139],v[32:33]
v_lshlrev_b32 v32,16,v22
v_and_b32 v33,0xffff0000,v22
v_pk_add_f32 v[140:141],v[140:141],v[32:33]
v_lshlrev_b32 v32,16,v23
v_and_b32 v33,0xffff0000,v23
v_pk_add_f32 v[142:143],v[142:143],v[32:33]
CV v128,v128,v129
CV v129,v130,v131
buffer_store_dwordx2 v[128:129],v232,s[48:51],s84 offen sc1
SA s84,s84,0x4000
CV v132,v132,v133
CV v133,v134,v135
buffer_store_dwordx2 v[132:133],v232,s[48:51],s84 offen sc1
SA s84,s84,0x4000
CV v136,v136,v137
CV v137,v138,v139
buffer_store_dwordx2 v[136:137],v232,s[48:51],s84 offen sc1
SA s84,s84,0x4000
CV v140,v140,v141
CV v141,v142,v143
buffer_store_dwordx2 v[140:141],v232,s[48:51],s84 offen sc1
s_cmp_eq_u32 s37,0
s_cbranch_scc0 fk_no_epoch
s_cmp_eq_u32 s36,0
s_cbranch_scc0 fk_no_epoch2
v_add_u32 v224,1,v251
s_mov_b32 s84,0x3500000
buffer_store_dword v224,v250,s[44:47],s84 offen sc1
fk_no_epoch2:
fk_no_epoch:
W vmcnt(0) lgkmcnt(0)
s_branch fk_exit
fk_spin_kv:
s_mov_b32 s95,0x40000
fk_spin_kv_l:
s_sleep 2
buffer_load_dwordx2 v[234:235],v232,s[44:47],0 offen sc1
W vmcnt(0)
v_cmp_ne_u32 s[96:97],s42,v234
v_cmp_ne_u32 vcc,s43,v235
s_nop 4
s_or_b64 vcc,vcc,s[96:97]
s_cbranch_vccz fk_spin_kv_back
s_sub_u32 s95,s95,1
s_cmp_lg_u32 s95,0
s_cbranch_scc1 fk_spin_kv_l
s_branch fk_spin_kv_back
fk_spin_ctx:
s_mov_b32 s95,0x40000
fk_spin_ctx_l:
s_sleep 2
buffer_load_dwordx2 v[220:221],v216,s[44:47],0 offen sc1
W vmcnt(0)
v_cmp_ne_u32 s[96:97],s42,v220
v_cmp_ne_u32 vcc,s43,v221
s_nop 4
s_or_b64 vcc,vcc,s[96:97]
s_cbranch_vccz fk_spin_ctx_back
s_sub_u32 s95,s95,1
s_cmp_lg_u32 s95,0
s_cbranch_scc1 fk_spin_ctx_l
s_branch fk_spin_ctx_back
fk_spin_p:
s_mov_b32 s95,0x40000
fk_spin_p_l:
s_sleep 2
buffer_load_dwordx2 v[220:221],v216,s[44:47],0 offen sc1
W vmcnt(0)
v_cmp_ne_u32 s[96:97],s42,v220
v_cmp_ne_u32 vcc,s43,v221
s_nop 4
s_or_b64 vcc,vcc,s[96:97]
s_cbranch_vccz fk_spin_p_back
s_sub_u32 s95,s95,1
s_cmp_lg_u32 s95,0
s_cbranch_scc1 fk_spin_p_l
s_branch fk_spin_p_back
fk_exit:
.purgem M
.purgem BL
.purgem BS
.purgem TR
.purgem RD
.purgem WR
.purgem CV
.purgem EX
.purgem AR
.purgem AW
.purgem SA
.purgem W
)~"
    :: [ka] "s"(ka), [wid] "s"(wid), [bid] "s"(bid), [ldsb] "s"(ldsb)
    : "memory", "vcc", "scc", "s8", "s9", "s10", "s11", "s12", "s13", "s14", "s15", "s16", "s17", "s18", "s19", "s20", "s21", "s22", "s23", "s24", "s25", "s26", "s27", "s28", "s29", "s36", "s37", "s38", "s39", "s40", "s41", "s42", "s43", "s44", "s45", "s46", "s47", "s48", "s49", "s50", "s51", "s52", "s53", "s54", "s55", "s56", "s57", "s58", "s59", "s60", "s61", "s62", "s63", "s64", "s65", "s66", "s67", "s68", "s69", "s70", "s71", "s72", "s73", "s74", "s75", "s76", "s77", "s78", "s79", "s80", "s81", "s82", "s83", "s84", "s85", "s86", "s87", "s88", "s89", "s90", "s91", "s92", "s93", "s94", "s95", "s96", "s97", "s98", "s99", "v0", "v1", "v2", "v3", "v4", "v5", "v6", "v7", "v8", "v9", "v10", "v11", "v12", "v13", "v14", "v15", "v16", "v17", "v18", "v19", "v20", "v21", "v22", "v23", "v24", "v25", "v26", "v27", "v28", "v29", "v30", "v31", "v32", "v33", "v34", "v35", "v36", "v37", "v38", "v39", "v40", "v41", "v42", "v43", "v44", "v45", "v46", "v47", "v48", "v49", "v50", "v51", "v52", "v53", "v54", "v55", "v56", "v57", "v58", "v59", "v60", "v61", "v62", "v63", "v64", "v65", "v66", "v67", "v68", "v69", "v70", "v71", "v72", "v73", "v74", "v75", "v76", "v77", "v78", "v79", "v80", "v81", "v82", "v83", "v84", "v85", "v86", "v87", "v88", "v89", "v90", "v91", "v92", "v93", "v94", "v95", "v96", "v97", "v98", "v99", "v100", "v101", "v102", "v103", "v104", "v105", "v106", "v107", "v108", "v109", "v110", "v111", "v112", "v113", "v114", "v115", "v116", "v117", "v118", "v119", "v120", "v121", "v122", "v123", "v124", "v125", "v126", "v127", "v128", "v129", "v130", "v131", "v132", "v133", "v134", "v135", "v136", "v137", "v138", "v139", "v140", "v141", "v142", "v143", "v144", "v145", "v146", "v147", "v148", "v149", "v150", "v151", "v152", "v153", "v154", "v155", "v156", "v157", "v158", "v159", "v160", "v161", "v162", "v163", "v164", "v165", "v166", "v167", "v168", "v169", "v170", "v171", "v172", "v173", "v174", "v175", "v176", "v177", "v178", "v179", "v180", "v181", "v182", "v183", "v184", "v185", "v186", "v187", "v188", "v189", "v190", "v191", "v192", "v193", "v194", "v195", "v196", "v197", "v198", "v199", "v200", "v201", "v202", "v203", "v204", "v205", "v206", "v207", "v208", "v209", "v210", "v211", "v212", "v213", "v214", "v215", "v216", "v217", "v218", "v219", "v220", "v221", "v222", "v223", "v224", "v225", "v226", "v227", "v228", "v229", "v230", "v231", "v232", "v233", "v234", "v235", "v236", "v237", "v238", "v239", "v240", "v241", "v242", "v243", "v244", "v245", "v246", "v247", "v248", "v249", "v250", "v251", "v252", "v253", "v254", "v255", "a0", "a1", "a2", "a3", "a4", "a5", "a6", "a7", "a8", "a9", "a10", "a11", "a12", "a13", "a14", "a15", "a16", "a17", "a18", "a19", "a20", "a21", "a22", "a23", "a24", "a25", "a26", "a27", "a28", "a29", "a30", "a31", "a32", "a33", "a34", "a35", "a36", "a37", "a38", "a39", "a40", "a41", "a42", "a43", "a44", "a45", "a46", "a47", "a48", "a49", "a50", "a51", "a52", "a53", "a54", "a55", "a56", "a57", "a58", "a59", "a60", "a61", "a62", "a63", "a64", "a65", "a66", "a67", "a68", "a69", "a70", "a71", "a72", "a73", "a74", "a75", "a76", "a77", "a78", "a79", "a80", "a81", "a82", "a83", "a84", "a85", "a86", "a87", "a88", "a89", "a90", "a91", "a92", "a93", "a94", "a95", "a96", "a97", "a98", "a99", "a100", "a101", "a102", "a103", "a104", "a105", "a106", "a107", "a108", "a109", "a110", "a111", "a112", "a113", "a114", "a115", "a116", "a117", "a118", "a119", "a120", "a121", "a122", "a123", "a124", "a125", "a126", "a127", "a128", "a129", "a130", "a131", "a132", "a133", "a134", "a135", "a136", "a137", "a138", "a139", "a140", "a141", "a142", "a143", "a144", "a145", "a146", "a147", "a148", "a149", "a150", "a151", "a152", "a153", "a154", "a155", "a156", "a157", "a158", "a159", "a160", "a161", "a162", "a163", "a164", "a165", "a166", "a167", "a168", "a169", "a170", "a171", "a172", "a173", "a174", "a175", "a176", "a177", "a178", "a179", "a180", "a181", "a182", "a183", "a184", "a185", "a186", "a187", "a188", "a189", "a190", "a191", "a192", "a193", "a194", "a195", "a196", "a197", "a198", "a199", "a200", "a201", "a202", "a203", "a204", "a205", "a206", "a207", "a208", "a209", "a210", "a211", "a212", "a213", "a214", "a215", "a216", "a217", "a218", "a219", "a220", "a221", "a222", "a223", "a224", "a225", "a226", "a227", "a228", "a229", "a230", "a231", "a232", "a233", "a234", "a235", "a236", "a237", "a238", "a239", "a240", "a241", "a242", "a243", "a244", "a245", "a246", "a247", "a248", "a249", "a250", "a251", "a252", "a253", "a254", "a255");
}
extern "C" void kernel_launch(void* const* d_in, const int* in_sizes, int n_in, void* d_out, int out_size, void* d_ws, size_t ws_size,
                              hipStream_t stream) {
  (void)in_sizes; (void)n_in; (void)out_size; (void)ws_size;
  gated_mix__sliding_local_dilated__compressed_kv_cla__prompt_kernel<<<256, 256, 0, stream>>>(d_in[0], d_in[1], d_in[2], d_in[4], d_in[5], d_in[6], d_in[7], d_in[8], d_in[9], d_out, d_ws);
}
```
